```python
import torch

def _softmax(x, axis=-1):
    return torch.softmax(x, dim=axis)
def _gelu(x):
    return torch.nn.functional.gelu(x, approximate="tanh")
def _silu(x):
    return torch.nn.functional.silu(x)
def _sigmoid(x):
    return torch.sigmoid(x)
def _layernorm(x, axis=-1, eps=1e-5):
    m = x.mean(dim=axis, keepdim=True)
    v = x.var(dim=axis, keepdim=True, unbiased=False)
    return (x - m) / torch.sqrt(v + eps)
def _scatter_add(tgt, idx, src, axis=0):
    return tgt.clone().index_add_(axis, idx, src)
def _segment_sum(src, seg, n):
    out = torch.zeros((n,) + src.shape[1:], dtype=src.dtype)
    return out.index_add_(0, seg, src)
def _pad(x, pw):
    flat = tuple(v for pair in reversed(pw) for v in pair)
    return torch.nn.functional.pad(x, flat)

kernel_name = 'sequential_preLN__gqa_block_cached__glu_gated_bilinear__decode'

def setup_inputs():

    _w = (lambda a: (lambda t: t.to(torch.bfloat16) if t.is_floating_point() else t)(torch.as_tensor(a)))
    def _A_setup():
        B, Sq, D = 1, 1, 512
        Hq, Hkv, Sk, Dh = 32, 8, 65536, 128
        assert Hq % Hkv == 0, (Hq, Hkv)
        return {
            "x": _w((lambda *s: torch.randn(*s, dtype=torch.bfloat16) * 0.05)(B, Sq, D)),
            "k_cache": _w((lambda *s: torch.randn(*s, dtype=torch.bfloat16) * 3.2)(B, Hkv, Sk, Dh)),
            "v_cache": _w((lambda *s: torch.randn(*s, dtype=torch.bfloat16) * 1.6)(B, Hkv, Sk, Dh)),
            "wq": _w((lambda *s: torch.randn(*s, dtype=torch.bfloat16) * 3.2)(D, Hq * Dh) * (D ** -0.5)),
            "wo": _w((lambda *s: torch.randn(*s, dtype=torch.bfloat16) * 1.6)(Hq * Dh, D) * ((Hq * Dh) ** -0.5)),
        }
    _A_in = _A_setup()

    def _B_setup():
        B, S, D, F = 1, 1, 512, 1792
        return {
            "x": _w((lambda *s: torch.randn(*s, dtype=torch.bfloat16) * 0.05)(B, S, D)),
            "w_gate": _w((lambda *s: torch.randn(*s, dtype=torch.bfloat16) * 1.6)(D, F) * (D ** -0.5)),
            "w_up": _w((lambda *s: torch.randn(*s, dtype=torch.bfloat16) * 1.6)(D, F) * (D ** -0.5)),
            "w_down": _w((lambda *s: torch.randn(*s, dtype=torch.bfloat16) * 1.6)(F, D) * (F ** -0.5)),
        }
    _B_in = _B_setup()

    return {
        "A_x": _A_in["x"],
        "A_k_cache": _A_in["k_cache"],
        "A_v_cache": _A_in["v_cache"],
        "A_wq": _A_in["wq"],
        "A_wo": _A_in["wo"],
        "B_x": _B_in["x"],
        "B_w_gate": _B_in["w_gate"],
        "B_w_up": _B_in["w_up"],
        "B_w_down": _B_in["w_down"],
    }

def reference(A_x, A_k_cache, A_v_cache, A_wq, A_wo, B_x, B_w_gate, B_w_up, B_w_down):
    def _A_ref(x, k_cache, v_cache, wq, wo):
        B, Sq, D = x.shape
        Hq, Dh = 32, 128
        Hkv = k_cache.shape[1]
        rep = Hq // Hkv
        q = (lambda x, *d: x.permute(*d))((x @ wq).reshape(B, Sq, Hq, Dh), 0, 2, 1, 3)
        k = (lambda x, n, axis: torch.repeat_interleave(x, n, dim=axis))(k_cache, rep, axis=1)
        v = (lambda x, n, axis: torch.repeat_interleave(x, n, dim=axis))(v_cache, rep, axis=1)
        scores = torch.einsum("bhqd,bhkd->bhqk", q, k) * (Dh ** -0.5)
        p = _softmax(scores, axis=-1)
        o = torch.einsum("bhqk,bhkd->bhqd", p, v)
        return (lambda x, *d: x.permute(*d))(o, 0, 2, 1, 3).reshape(B, Sq, Hq * Dh) @ wo

    def _B_ref(x, w_gate, w_up, w_down):
        g = torch.einsum("bsd,df->bsf", x, w_gate)
        u = torch.einsum("bsd,df->bsf", x, w_up)
        h = g * u
        return torch.einsum("bsf,fd->bsd", h, w_down)

    x = A_x
    h = x + _A_ref(_layernorm(x, axis=-1), A_k_cache, A_v_cache, A_wq, A_wo)
    h2 = h + _B_ref(_layernorm(h, axis=-1), B_w_gate, B_w_up, B_w_down)
    return h2
```

```cpp
#include <hip/hip_runtime.h>
#include <hip/hip_bf16.h>
#include <stdint.h>
#define MIMIC 0
constexpr int D=512, HQ=32, HKV=8, SK=65536, DH=128, F=1792, NSPLIT=32;
typedef unsigned short u16;
__device__ __forceinline__ float bf2f(u16 b){ return __uint_as_float(((unsigned)b)<<16); }
__device__ __forceinline__ u16 f2bf(float f){ unsigned u=__float_as_uint(f); u += 0x7FFFu + ((u>>16)&1u); return (u16)(u>>16); }
__device__ __forceinline__ float rb(float f){ return bf2f(f2bf(f)); }
__device__ __forceinline__ float mr(float f){ return MIMIC ? rb(f) : f; }

__device__ float block_sum(float v, float* sh){
  for(int o=32;o>0;o>>=1) v += __shfl_xor(v,o);
  int w=threadIdx.x>>6, nw=blockDim.x>>6;
  __syncthreads();
  if((threadIdx.x&63)==0) sh[w]=v;
  __syncthreads();
  float s=0; for(int i=0;i<nw;i++) s+=sh[i];
  return s;
}
__device__ float ln512(float v, float* sh){
  float m = block_sum(v, sh)/D;
  float dv = v-m;
  float var = block_sum(dv*dv, sh)/D;
  if(MIMIC){ float mb=rb(m); float vb=rb(var); float den=rb(sqrtf(rb(vb+1e-5f))); return rb(rb(v-mb)/den);}
  return (v-m)/sqrtf(var+1e-5f);
}
__global__ void k_q(const u16* x, const u16* wq, float* q){
  __shared__ float sh[16]; __shared__ float xn[D];
  float v=bf2f(x[threadIdx.x]);
  xn[threadIdx.x]=ln512(v,sh);
  __syncthreads();
  int col=blockIdx.x*512+threadIdx.x;
  float acc=0; for(int i=0;i<D;i++) acc += xn[i]*bf2f(wq[(size_t)i*4096+col]);
  q[col]=rb(acc);
}
__global__ void k_attn(const u16* k, const u16* v, const float* q, float* pm, float* pl, float* po){
  int s=blockIdx.x, kh=blockIdx.y; int tid=threadIdx.x, lane=tid&63, w=tid>>6;
  __shared__ float sm[4][4], sl[4][4], so[4][4][128];
  float qr[4][2]; for(int h=0;h<4;h++){ qr[h][0]=q[(kh*4+h)*128+2*lane]; qr[h][1]=q[(kh*4+h)*128+2*lane+1]; }
  float m[4],l[4],o[4][2]; for(int h=0;h<4;h++){m[h]=-INFINITY;l[h]=0;o[h][0]=o[h][1]=0;}
  const int per=SK/NSPLIT;
  for(int i=w;i<per;i+=4){
    size_t key=(size_t)kh*SK+(size_t)s*per+i;
    unsigned kk=*(const unsigned*)(k+key*128+2*lane), vv=*(const unsigned*)(v+key*128+2*lane);
    float k0=__uint_as_float(kk<<16),k1=__uint_as_float(kk&0xffff0000u),v0=__uint_as_float(vv<<16),v1=__uint_as_float(vv&0xffff0000u);
    for(int h=0;h<4;h++){
      float d=qr[h][0]*k0+qr[h][1]*k1; for(int o2=32;o2>0;o2>>=1) d+=__shfl_xor(d,o2);
      d = mr(mr(d)*0.08838834764831845f);
      float mn=fmaxf(m[h],d); float a=__expf(m[h]-mn), p=__expf(d-mn);
      l[h]=l[h]*a+p; float pb=rb(p); o[h][0]=o[h][0]*a+pb*v0; o[h][1]=o[h][1]*a+pb*v1; m[h]=mn;
    }
  }
  for(int h=0;h<4;h++){ if(lane==0){sm[w][h]=m[h]; sl[w][h]=l[h];} so[w][h][2*lane]=o[h][0]; so[w][h][2*lane+1]=o[h][1]; }
  __syncthreads();
  int h=w; float M=-INFINITY; for(int j=0;j<4;j++) M=fmaxf(M,sm[j][h]);
  float L=0,a0=0,a1=0; for(int j=0;j<4;j++){ float e=__expf(sm[j][h]-M); L+=e*sl[j][h]; a0+=e*so[j][h][2*lane]; a1+=e*so[j][h][2*lane+1]; }
  size_t slot=(size_t)(kh*4+h)*NSPLIT+s;
  if(lane==0){pm[slot]=M; pl[slot]=L;} po[slot*128+2*lane]=a0; po[slot*128+2*lane+1]=a1;
}
__global__ void k_comb(const float* pm,const float* pl,const float* po,float* o){
  int h=blockIdx.x, d=threadIdx.x; float M=-INFINITY; for(int s=0;s<NSPLIT;s++) M=fmaxf(M,pm[h*NSPLIT+s]);
  float L=0,a=0; for(int s=0;s<NSPLIT;s++){ float e=__expf(pm[h*NSPLIT+s]-M); L+=e*pl[h*NSPLIT+s]; a+=e*po[((size_t)h*NSPLIT+s)*128+d]; }
  o[h*128+d]=mr(a/L);
}
__global__ void k_wo(const u16* x,const float* o,const u16* wo,float* h){
  int col=blockIdx.x*64+threadIdx.x; float acc=0; for(int i=0;i<4096;i++) acc+=o[i]*bf2f(wo[(size_t)i*512+col]);
  h[col]=mr(bf2f(x[col])+mr(acc));
}
__global__ void k_gu(const float* h,const u16* wg,const u16* wu,float* hid){
  __shared__ float sh[16]; __shared__ float xn[D];
  xn[threadIdx.x]=ln512(h[threadIdx.x],sh); __syncthreads();
  if(threadIdx.x<448){ int col=blockIdx.x*448+threadIdx.x; float g=0,u=0; for(int i=0;i<D;i++){ g+=xn[i]*bf2f(wg[(size_t)i*F+col]); u+=xn[i]*bf2f(wu[(size_t)i*F+col]); }
    hid[col]=mr(mr(g)*mr(u)); }
}
__global__ void k_down(const float* h,const float* hid,const u16* wd,u16* out){
  int col=blockIdx.x*64+threadIdx.x; float acc=0; for(int i=0;i<F;i++) acc+=hid[i]*bf2f(wd[(size_t)i*512+col]);
  out[col]=f2bf(h[col]+mr(acc));
}
extern "C" void kernel_launch(void* const* d_in, const int* in_sizes, int n_in, void* d_out, int out_size, void* d_ws, size_t ws_size, hipStream_t stream){
  const u16* x=(const u16*)d_in[0]; const u16* k=(const u16*)d_in[1]; const u16* v=(const u16*)d_in[2];
  const u16* wq=(const u16*)d_in[3]; const u16* wo=(const u16*)d_in[4];
  const u16* wg=(const u16*)d_in[6]; const u16* wu=(const u16*)d_in[7]; const u16* wd=(const u16*)d_in[8];
  float* ws=(float*)d_ws; float* q=ws; float* pm=q+4096; float* pl=pm+32*NSPLIT; float* po=pl+32*NSPLIT; float* o=po+32*NSPLIT*128; float* h=o+4096; float* hid=h+512;
  k_q<<<8,512,0,stream>>>(x,wq,q);
  k_attn<<<dim3(NSPLIT,HKV),256,0,stream>>>(k,v,q,pm,pl,po);
  k_comb<<<32,128,0,stream>>>(pm,pl,po,o);
  k_wo<<<8,64,0,stream>>>(x,o,wo,h);
  k_gu<<<4,512,0,stream>>>(h,wg,wu,hid);
  k_down<<<8,64,0,stream>>>(h,hid,wd,(u16*)d_out);
}
```

```cpp
#include <hip/hip_runtime.h>
#include <stdint.h>

#define STAMP 0
#define NB1 64
#define ND 32
#define REP 8

namespace {
typedef unsigned v4u __attribute__((ext_vector_type(4)));
typedef unsigned v2u __attribute__((ext_vector_type(2)));
typedef float f32x4 __attribute__((ext_vector_type(4)));
typedef float f32x2 __attribute__((ext_vector_type(2)));
typedef short s16x4 __attribute__((ext_vector_type(4)));
typedef __bf16 bf16x8 __attribute__((ext_vector_type(8)));
typedef __bf16 bf16x2 __attribute__((ext_vector_type(2)));
typedef __amdgpu_buffer_rsrc_t rsrc_t;
#define LAS __attribute__((address_space(3)))

constexpr int DM = 512, SK = 65536, DH = 128, FF = 1792;
constexpr int NWG = 256, NTHR = 256;
constexpr int NPF = 224;
constexpr int AUX_NT = 2, AUX_SC1 = 16;
constexpr int RS3 = 0x00020000;
constexpr int WS_EPOCH = 0;
constexpr int WS_QG = 4096;
constexpr int WS_ML = WS_QG + 16384;
constexpr int WS_HG = WS_ML + 16384;
constexpr int WS_OP = 1 << 20;
constexpr int WS_PB = 2 << 20;
constexpr int WS_PD = 3 << 20;
constexpr int WS_USED = 4 << 20;
constexpr int WS_STAMP = 8 << 20;
constexpr unsigned SPIN_CAP = 1u << 14;
constexpr float LOG2E = 1.4426950408889634f;
constexpr float SCALE = 0.08838834764831845f;

struct Params {
  const void *x, *k, *v, *wq, *wo, *wg, *wu, *wd;
  void *out, *ws;
};

__device__ __forceinline__ float asf(unsigned u) { return __builtin_bit_cast(float, u); }
__device__ __forceinline__ unsigned asu(float f) { return __builtin_bit_cast(unsigned, f); }
template <int C> __device__ __forceinline__ float dpp(float x) {
  return __builtin_bit_cast(float, __builtin_amdgcn_mov_dpp(__builtin_bit_cast(int, x), C, 0xf, 0xf, true));
}
constexpr int XOR1 = 0xB1, XOR2 = 0x4E, HMIR = 0x141, MIR = 0x140, ROR4 = 0x124, ROR8 = 0x128;
__device__ __forceinline__ unsigned cvtpk(float lo, float hi) {
  f32x2 v = {lo, hi};
  bf16x2 r = __builtin_convertvector(v, bf16x2);
  return __builtin_bit_cast(unsigned, r);
}
__device__ __forceinline__ float lo16(unsigned u) { return asf(u << 16); }
__device__ __forceinline__ float hi16(unsigned u) { return asf(u & 0xffff0000u); }
__device__ __forceinline__ float rbf(float x) { return lo16(cvtpk(x, x)); }
__device__ __forceinline__ float xr16_max(float x) {
  auto s = __builtin_amdgcn_permlane16_swap(asu(x), asu(x), false, false);
  x = fmaxf(asf(s[0]), asf(s[1]));
  auto t = __builtin_amdgcn_permlane32_swap(asu(x), asu(x), false, false);
  return fmaxf(asf(t[0]), asf(t[1]));
}
__device__ __forceinline__ float xr16_sum(float x) {
  auto s = __builtin_amdgcn_permlane16_swap(asu(x), asu(x), false, false);
  x = asf(s[0]) + asf(s[1]);
  auto t = __builtin_amdgcn_permlane32_swap(asu(x), asu(x), false, false);
  return asf(t[0]) + asf(t[1]);
}
__device__ __forceinline__ float row_sum(float x) {
  x += dpp<XOR1>(x); x += dpp<XOR2>(x); x += dpp<HMIR>(x); x += dpp<MIR>(x); return x;
}
__device__ __forceinline__ float row_max(float x) {
  x = fmaxf(x, dpp<XOR1>(x)); x = fmaxf(x, dpp<XOR2>(x)); x = fmaxf(x, dpp<HMIR>(x)); x = fmaxf(x, dpp<MIR>(x)); return x;
}
__device__ __forceinline__ float wave_sum(float x) { return xr16_sum(row_sum(x)); }
__device__ __forceinline__ f32x4 mfma32(v4u a, v4u b, f32x4 c) {
  return __builtin_amdgcn_mfma_f32_16x16x32_bf16(__builtin_bit_cast(bf16x8, a), __builtin_bit_cast(bf16x8, b), c, 0, 0, 0);
}
__device__ __forceinline__ f32x4 mfma16(v2u a, unsigned b0, unsigned b1, f32x4 c) {
  v2u b = {b0, b1};
  return __builtin_amdgcn_mfma_f32_16x16x16bf16_1k(__builtin_bit_cast(s16x4, a), __builtin_bit_cast(s16x4, b), c, 0, 0, 0);
}
template <int AUX> __device__ __forceinline__ v4u ld16(rsrc_t r, int voff, int soff) {
  return __builtin_amdgcn_raw_buffer_load_b128(r, voff, soff, AUX);
}
__device__ __forceinline__ void st16(rsrc_t r, int voff, v4u v) { __builtin_amdgcn_raw_buffer_store_b128(v, r, voff, 0, AUX_SC1); }
__device__ __forceinline__ void st8(rsrc_t r, int voff, v2u v) { __builtin_amdgcn_raw_buffer_store_b64(v, r, voff, 0, AUX_SC1); }
#define WG_BARRIER() asm volatile("s_waitcnt lgkmcnt(0)\n\ts_barrier" ::: "memory")
#define LDS_WAIT() asm volatile("s_waitcnt lgkmcnt(0)" ::: "memory")
#define SB() __builtin_amdgcn_sched_barrier(0)

__device__ __forceinline__ v4u layernorm8(v4u p) {
  float x[8];
#pragma unroll
  for (int i = 0; i < 4; ++i) { x[2 * i] = lo16(p[i]); x[2 * i + 1] = hi16(p[i]); }
  float s = ((x[0] + x[1]) + (x[2] + x[3])) + ((x[4] + x[5]) + (x[6] + x[7]));
  const float mean = wave_sum(s) * (1.f / DM);
  float q = 0.f;
#pragma unroll
  for (int i = 0; i < 8; ++i) { const float d = x[i] - mean; q = fmaf(d, d, q); }
  const float var = wave_sum(q) * (1.f / DM);
  const float mb = rbf(mean);
  const float den = rbf(sqrtf(rbf(rbf(var) + 1e-5f)));
  v4u r;
#pragma unroll
  for (int i = 0; i < 4; ++i) r[i] = cvtpk(rbf(x[2 * i] - mb) / den, rbf(x[2 * i + 1] - mb) / den);
  return r;
}
__device__ __forceinline__ unsigned pick4(v4u v, int w) {
  return w == 0 ? v[0] : w == 1 ? v[1] : w == 2 ? v[2] : v[3];
}
__device__ __forceinline__ float gemv16(unsigned xp, v4u a0, v4u a1, v4u b0, v4u b1, int lane) {
  float v[16];
  const bf16x2 xb = __builtin_bit_cast(bf16x2, xp);
#pragma unroll
  for (int i = 0; i < 4; ++i) {
    const unsigned alo = __builtin_amdgcn_perm(a1[i], a0[i], 0x05040100u), ahi = __builtin_amdgcn_perm(a1[i], a0[i], 0x07060302u);
    const unsigned blo = __builtin_amdgcn_perm(b1[i], b0[i], 0x05040100u), bhi = __builtin_amdgcn_perm(b1[i], b0[i], 0x07060302u);
    v[2 * i] = __builtin_amdgcn_fdot2_f32_bf16(__builtin_bit_cast(bf16x2, alo), xb, 0.f, false);
    v[2 * i + 1] = __builtin_amdgcn_fdot2_f32_bf16(__builtin_bit_cast(bf16x2, ahi), xb, 0.f, false);
    v[8 + 2 * i] = __builtin_amdgcn_fdot2_f32_bf16(__builtin_bit_cast(bf16x2, blo), xb, 0.f, false);
    v[8 + 2 * i + 1] = __builtin_amdgcn_fdot2_f32_bf16(__builtin_bit_cast(bf16x2, bhi), xb, 0.f, false);
  }
  float u[8];
#pragma unroll
  for (int k = 0; k < 8; ++k) { auto s = __builtin_amdgcn_permlane32_swap(asu(v[k]), asu(v[k + 8]), false, false); u[k] = asf(s[0]) + asf(s[1]); }
  float t[4];
#pragma unroll
  for (int k = 0; k < 4; ++k) { auto s = __builtin_amdgcn_permlane16_swap(asu(u[k]), asu(u[k + 4]), false, false); t[k] = asf(s[0]) + asf(s[1]); }
  float y[2];
#pragma unroll
  for (int k = 0; k < 2; ++k) { const float p = t[k] + dpp<ROR8>(t[k]), q = t[k + 2] + dpp<ROR8>(t[k + 2]); y[k] = (lane & 8) ? q : p; }
#pragma unroll
  for (int k = 0; k < 2; ++k) { y[k] += dpp<XOR1>(y[k]); y[k] += dpp<XOR2>(y[k]); y[k] += dpp<HMIR>(y[k]); }
  return (lane & 4) ? y[1] : y[0];
}

struct Tile { v4u k[8], v[8]; };

template <bool REFILL>
__device__ __forceinline__ void tile_step(Tile &t, v4u q, rsrc_t rk, rsrc_t rv, int koff, int voff, int snext, unsigned mk0, unsigned mk1,
                                          float &m2, float &lsum, f32x4 &acc0, f32x4 &acc1) {
  float s[8];
  const f32x4 z = {0.f, 0.f, 0.f, 0.f};
  f32x4 d[8];
#pragma unroll
  for (int g = 0; g < 8; ++g) {
    d[g] = mfma32(t.k[g], q, z);
    if (REFILL) t.k[g] = ld16<AUX_NT>(rk, koff, snext + g * 1024);
    SB();
  }
#pragma unroll
  for (int g = 0; g < 8; ++g) {
    s[g] = ((dpp<0x00>(d[g][0]) + dpp<0x55>(d[g][1])) + dpp<0xAA>(d[g][2])) + dpp<0xFF>(d[g][3]);
    asm("" : "+v"(s[g]));
  }
#pragma unroll
  for (int g = 0; g < 8; g += 2) {
    const unsigned a = cvtpk(s[g], s[g + 1]);
    const unsigned b = cvtpk(lo16(a) * SCALE, hi16(a) * SCALE);
    s[g] = lo16(b); s[g + 1] = hi16(b);
  }
  float mx = fmaxf(fmaxf(fmaxf(s[0], s[1]), fmaxf(s[2], s[3])), fmaxf(fmaxf(s[4], s[5]), fmaxf(s[6], s[7])));
  mx = xr16_max(mx);
  const float mn = fmaxf(m2, mx * LOG2E);
  const float alpha = __builtin_amdgcn_exp2f(m2 - mn);
  m2 = mn;
  lsum *= alpha;
  int ai = __builtin_bit_cast(int, alpha), ar;
  ar = __builtin_amdgcn_update_dpp(ai, ai, 0x150, 0x1, 0xf, false);
  ar = __builtin_amdgcn_update_dpp(ar, ai, 0x154, 0x2, 0xf, false);
  ar = __builtin_amdgcn_update_dpp(ar, ai, 0x158, 0x4, 0xf, false);
  ar = __builtin_amdgcn_update_dpp(ar, ai, 0x15c, 0x8, 0xf, false);
  const float al = __builtin_bit_cast(float, ar);
  acc0 *= al; acc1 *= al;
#pragma unroll
  for (int g = 0; g < 8; ++g) {
    const float p = __builtin_amdgcn_exp2f(fmaf(s[g], LOG2E, -mn));
    lsum += p;
    const unsigned pp = cvtpk(p, p);
    const v2u a = {pp & mk0, pp & mk1};
    acc0 = mfma16(a, t.v[g][0], t.v[g][1], acc0);
    acc1 = mfma16(a, t.v[g][2], t.v[g][3], acc1);
    if (REFILL) t.v[g] = ld16<AUX_NT>(rv, voff, snext + g * 1024);
    SB();
  }
}

template <int N>
__device__ __forceinline__ void gather(rsrc_t rs, const int (&off)[N], unsigned tag, v4u (&x)[N]) {
  for (unsigned spins = 0;; ++spins) {
    bool ok = true;
#pragma unroll
    for (int i = 0; i < N; ++i) x[i] = ld16<AUX_SC1>(rs, off[i], 0);
#pragma unroll
    for (int i = 0; i < N; ++i) ok = ok && x[i][1] == tag && x[i][3] == tag;
    if (__all(ok) || spins > SPIN_CAP) break;
    __builtin_amdgcn_s_sleep(1);
  }
}
template <int S> __device__ __forceinline__ float strided_sum(float x) {
  if (S <= 1) x += dpp<XOR1>(x);
  if (S <= 2) x += dpp<XOR2>(x);
  if (S <= 8) x += dpp<ROR8>(x);
  if (S <= 4) x += dpp<ROR4>(x);
  return xr16_sum(x);
}
template <int C>
__device__ __forceinline__ void publish512(rsrc_t rs, int base, int nprod, int p, f32x4 a0, f32x4 a1, LAS float *tr, int w, int lane, unsigned tag) {
  if (lane < 16) {
    *(LAS f32x4 *)(tr + w * 128 + 8 * lane) = a0;
    *(LAS f32x4 *)(tr + w * 128 + 8 * lane + 4) = a1;
  }
  LDS_WAIT();
  const f32x2 y = *(LAS f32x2 *)(tr + w * 128 + 2 * lane);
  const int col = 128 * w + 2 * lane, j = col / C, within = col % C;
  const v4u g = {asu(y[0]), tag, asu(y[1]), tag};
  st16(rs, base + ((j * nprod + p) * C + within) * 8, g);
}
template <int C, int NP>
__device__ __forceinline__ f32x2 reduce_cols(rsrc_t rs, int base, int j, unsigned tag, LAS float *red, int tid, int w, int lane) {
  constexpr int S = C / 2, NPIECE = NP * S, N = (NPIECE + NTHR - 1) / NTHR;
  int off[N]; v4u x[N];
#pragma unroll
  for (int i = 0; i < N; ++i) { const int pc = i * NTHR + tid; off[i] = base + j * (NP * C * 8) + (pc < NPIECE ? pc : tid) * 16; }
  gather<N>(rs, off, tag, x);
  float s0 = 0.f, s1 = 0.f;
#pragma unroll
  for (int i = 0; i < N; ++i) { const bool live = i * NTHR + tid < NPIECE; s0 += live ? asf(x[i][0]) : 0.f; s1 += live ? asf(x[i][2]) : 0.f; }
  s0 = strided_sum<S>(s0); s1 = strided_sum<S>(s1);
  if (lane < S) { f32x2 y = {s0, s1}; *(LAS f32x2 *)(red + w * 64 + 2 * lane) = y; }
  WG_BARRIER();
  f32x2 r = {0.f, 0.f};
#pragma unroll
  for (int ww = 0; ww < 4; ++ww) r += *(LAS f32x2 *)(red + ww * 64 + 2 * (lane % S));
  return r;
}

#define STAMP_AT(i) do { } while (0)

constexpr int L_MRG = 0;
constexpr int L_ML = L_MRG + 2048;
constexpr int L_RED = L_ML + 32;
constexpr int L_RED2 = L_RED + 256;
constexpr int L_TR = L_RED2 + 256;
constexpr int L_END = L_TR + 512;
constexpr int LDS_BYTES = 84 * 1024;

__global__ void __launch_bounds__(NTHR, 2) fused_decode_kernel(Params p) {
  extern __shared__ __attribute__((aligned(16))) float smem[];
  LAS float *L = (LAS float *)smem;
  const int tid = threadIdx.x, lane = tid & 63, w = __builtin_amdgcn_readfirstlane(tid >> 6), b = blockIdx.x;
  STAMP_AT(0);
  const int kh = b >> 5, sp = b & 31;
  const rsrc_t rws = __builtin_amdgcn_make_buffer_rsrc(p.ws, 0, WS_USED, RS3);
  const rsrc_t rk = __builtin_amdgcn_make_buffer_rsrc((char *)p.k + ((size_t)kh * SK + sp * 2048) * 256, 0, 2048 * 256, RS3);
  const rsrc_t rv = __builtin_amdgcn_make_buffer_rsrc((char *)p.v + ((size_t)kh * SK + sp * 2048) * 256, 0, 2048 * 256, RS3);
  const rsrc_t rx = __builtin_amdgcn_make_buffer_rsrc((void *)p.x, 0, DM * 2, RS3);
  const rsrc_t rwq = __builtin_amdgcn_make_buffer_rsrc((void *)p.wq, 0, DM * 4096 * 2, RS3);
  const unsigned epoch = __builtin_amdgcn_raw_buffer_load_b32(rws, WS_EPOCH, 0, AUX_SC1);
  const v4u xr = ld16<0>(rx, lane * 16, 0);
  const int qf = (b & 7) * 32 + (b >> 3);
  const int wqoff = (8 * lane + 2 * w) * 8192 + qf * 32;
  const v4u wa0 = ld16<AUX_NT>(rwq, wqoff, 0), wa1 = ld16<AUX_NT>(rwq, wqoff, 8192);
  const v4u wb0 = ld16<AUX_NT>(rwq, wqoff, 16), wb1 = ld16<AUX_NT>(rwq, wqoff, 8192 + 16);
  SB();
  const int g4 = lane >> 4, a4 = (lane >> 2) & 3, u4 = lane & 3;
  const int koff = a4 * 256 + (4 * g4 + u4) * 16, voff = lane * 16;
  Tile ta, tb;
#pragma unroll
  for (int g = 0; g < 8; ++g) ta.k[g] = ld16<AUX_NT>(rk, koff, w * 8192 + g * 1024);
#pragma unroll
  for (int g = 0; g < 8; ++g) ta.v[g] = ld16<AUX_NT>(rv, voff, w * 8192 + g * 1024);
  SB();
  const unsigned tag = epoch + 1u;
  {
    const v4u xn = layernorm8(xr);
    const float r = gemv16(pick4(xn, w), wa0, wa1, wb0, wb1, lane);
    if ((lane & 3) == 0) L[L_RED + w * 64 + (lane >> 2)] = r;
    WG_BARRIER();
    if (w == 0) {
      const int i = lane & 7;
      float s0 = 0.f, s1 = 0.f;
#pragma unroll
      for (int ww = 0; ww < 4; ++ww) { const f32x2 y = *(LAS f32x2 *)(L + L_RED + ww * 64 + 2 * i); s0 += y[0]; s1 += y[1]; }
      const v2u g = {cvtpk(s0, s1), tag};
      if (lane < 8) st8(rws, WS_QG + ((b & 7) * 256 + (b >> 3) * 8 + i) * 8, g);
    }
  }
  STAMP_AT(1);
  asm volatile("s_waitcnt vmcnt(9)" ::: "memory");
  v4u q;
  {
    const int qoff = WS_QG + (kh * 256 + a4 * 64 + (4 * g4 + u4) * 4) * 8;
    v4u x0 = ld16<AUX_SC1>(rws, qoff, 0), x1 = ld16<AUX_SC1>(rws, qoff, 16);
    SB();
#pragma unroll
    for (int g = 0; g < 8; ++g) tb.k[g] = ld16<AUX_NT>(rk, koff, (4 + w) * 8192 + g * 1024);
#pragma unroll
    for (int g = 0; g < 8; ++g) tb.v[g] = ld16<AUX_NT>(rv, voff, (4 + w) * 8192 + g * 1024);
    SB();
    bool ok = x0[1] == tag && x0[3] == tag && x1[1] == tag && x1[3] == tag;
    for (unsigned spins = 0; !__all(ok) && spins < SPIN_CAP; ++spins) {
      x0 = ld16<AUX_SC1>(rws, qoff, 0); x1 = ld16<AUX_SC1>(rws, qoff, 16);
      ok = x0[1] == tag && x0[3] == tag && x1[1] == tag && x1[3] == tag;
    }
    q = v4u{x0[0], x0[2], x1[0], x1[2]};
  }
  STAMP_AT(2);
  const unsigned mk0 = u4 == 0 ? 0xFFFFu : u4 == 1 ? 0xFFFF0000u : 0u;
  const unsigned mk1 = u4 == 2 ? 0xFFFFu : u4 == 3 ? 0xFFFF0000u : 0u;
  float m2 = -3.0e38f, lsum = 0.f;
  f32x4 acc0 = {0.f, 0.f, 0.f, 0.f}, acc1 = acc0;
  for (int it = 0; it < 7; ++it) {
    const int s0 = ((2 * it + 2) * 4 + w) * 8192;
    tile_step<true>(ta, q, rk, rv, koff, voff, s0, mk0, mk1, m2, lsum, acc0, acc1);
    tile_step<true>(tb, q, rk, rv, koff, voff, s0 + 4 * 8192, mk0, mk1, m2, lsum, acc0, acc1);
  }
  const int pf = (b & 7) * 28 + (b >> 3);
  const bool hasf = (b >> 3) < 28;
  const rsrc_t rwo = __builtin_amdgcn_make_buffer_rsrc((char *)p.wo + (size_t)b * 16384, 0, 16384, RS3);
  const rsrc_t rwg = __builtin_amdgcn_make_buffer_rsrc((void *)p.wg, 0, hasf ? DM * FF * 2 : 0, RS3);
  const rsrc_t rwu = __builtin_amdgcn_make_buffer_rsrc((void *)p.wu, 0, hasf ? DM * FF * 2 : 0, RS3);
  const rsrc_t rwd = __builtin_amdgcn_make_buffer_rsrc((char *)p.wd + (size_t)(hasf ? pf : 0) * 8192, 0, hasf ? 8192 : 0, RS3);
  v4u wo[4], wg0, wg1, wu0, wu1, wd[2];
  {
    const int o = g4 * 1024 + w * 256 + (lane & 15) * 16;
#pragma unroll
    for (int i = 0; i < 4; ++i) wo[i] = ld16<AUX_NT>(rwo, o, i * 4096);
    const int go = (8 * lane + 2 * w) * (FF * 2) + pf * 16;
    wg0 = ld16<AUX_NT>(rwg, go, 0); wg1 = ld16<AUX_NT>(rwg, go, FF * 2);
    wu0 = ld16<AUX_NT>(rwu, go, 0); wu1 = ld16<AUX_NT>(rwu, go, FF * 2);
#pragma unroll
    for (int i = 0; i < 2; ++i) wd[i] = ld16<AUX_NT>(rwd, o, i * 4096);
    SB();
  }
  tile_step<false>(ta, q, rk, rv, koff, voff, 0, mk0, mk1, m2, lsum, acc0, acc1);
  tile_step<false>(tb, q, rk, rv, koff, voff, 0, mk0, mk1, m2, lsum, acc0, acc1);
  STAMP_AT(3);
  {
    const float lw = xr16_sum(lsum);
    *(LAS f32x4 *)(L + L_MRG + w * 512 + g4 * 128 + 8 * (lane & 15)) = acc0;
    *(LAS f32x4 *)(L + L_MRG + w * 512 + g4 * 128 + 8 * (lane & 15) + 4) = acc1;
    if (lane < 16 && u4 == 0) { f32x2 y = {m2, lw}; *(LAS f32x2 *)(L + L_ML + (w * 4 + a4) * 2) = y; }
    WG_BARRIER();
    f32x2 ml[4];
#pragma unroll
    for (int ww = 0; ww < 4; ++ww) ml[ww] = *(LAS f32x2 *)(L + L_ML + (ww * 4 + w) * 2);
    const float M = fmaxf(fmaxf(ml[0][0], ml[1][0]), fmaxf(ml[2][0], ml[3][0]));
    float Ls = 0.f; f32x2 o = {0.f, 0.f};
#pragma unroll
    for (int ww = 0; ww < 4; ++ww) {
      const float e = __builtin_amdgcn_exp2f(ml[ww][0] - M);
      Ls = fmaf(e, ml[ww][1], Ls);
      o += e * *(LAS f32x2 *)(L + L_MRG + ww * 512 + w * 128 + 2 * lane);
    }
    const int head = 4 * kh + w;
    st16(rws, WS_OP + (((head * 8 + (lane >> 3)) * 32 + sp) * 16 + 2 * (lane & 7)) * 8, v4u{asu(o[0]), tag, asu(o[1]), tag});
    if (lane == 0) st16(rws, WS_ML + (head * 32 + sp) * 16, v4u{asu(M), tag, asu(Ls), tag});
  }
  STAMP_AT(4);
  {
    const int off[2] = {WS_OP + b * 4096 + tid * 16, WS_ML + ((b >> 3) * 32 + (lane & 31)) * 16};
    v4u x[2];
    gather<2>(rws, off, tag, x);
    STAMP_AT(5);
    const float ms = asf(x[1][0]), ls = asf(x[1][2]);
    const float M = xr16_max(row_max(ms));
    const float e = __builtin_amdgcn_exp2f(ms - M);
    const float Lt = 0.5f * wave_sum(e * ls);
    const float cs = __shfl(e / Lt, 8 * w + (lane >> 3));
    float v0 = cs * asf(x[0][0]), v1 = cs * asf(x[0][2]);
    v0 = strided_sum<8>(v0); v1 = strided_sum<8>(v1);
    if (lane < 8) {
      const int d = 2 * lane;
      L[L_RED2 + w * 64 + (d & 3) * 4 + (d >> 2)] = v0;
      L[L_RED2 + w * 64 + ((d + 1) & 3) * 4 + (d >> 2)] = v1;
    }
    WG_BARRIER();
    f32x4 o = {0.f, 0.f, 0.f, 0.f};
#pragma unroll
    for (int ww = 0; ww < 4; ++ww) o += *(LAS f32x4 *)(L + L_RED2 + ww * 64 + g4 * 4);
    f32x4 c0 = {0.f, 0.f, 0.f, 0.f}, c1 = c0;
#pragma unroll
    for (int i = 0; i < 4; ++i) {
      const unsigned pp = cvtpk(o[i], o[i]);
      const v2u a = {pp & mk0, pp & mk1};
      c0 = mfma16(a, wo[i][0], wo[i][1], c0);
      c1 = mfma16(a, wo[i][2], wo[i][3], c1);
    }
    publish512<512 / NB1>(rws, WS_PB, 256, b, c0, c1, L + L_TR, w, lane, tag);
  }
  STAMP_AT(6);
  if (b < NB1) {
    constexpr int C = 512 / NB1, S = C / 2;
    const unsigned xp = __builtin_amdgcn_raw_buffer_load_b32(rx, (b * C + 2 * (lane % S)) * 2, 0, 0);
    const f32x2 r = reduce_cols<C, 256>(rws, WS_PB, b, tag, L + L_RED, tid, w, lane);
    STAMP_AT(7);
    const unsigned hp = cvtpk(lo16(xp) + rbf(r[0]), hi16(xp) + rbf(r[1]));
    if (w == 0 && lane < S * REP) st8(rws, WS_HG + (lane / S) * 2048 + (b * S + lane % S) * 8, v2u{hp, tag});
    if (S * REP > 64 && w == 1 && 64 + lane < S * REP) st8(rws, WS_HG + ((64 + lane) / S) * 2048 + (b * S + lane % S) * 8, v2u{hp, tag});
  }
  STAMP_AT(8);
  v4u hb;
  {
    const int base = WS_HG + (b % REP) * 2048 + lane * 32;
    const int off[2] = {base, base + 16};
    v4u x[2];
    gather<2>(rws, off, tag, x);
    hb = v4u{x[0][0], x[0][2], x[1][0], x[1][2]};
  }
  STAMP_AT(9);
  if (hasf) {
    const v4u hn = layernorm8(hb);
    const float r = gemv16(pick4(hn, w), wg0, wg1, wu0, wu1, lane);
    if ((lane & 3) == 0) { const int vi = lane >> 2, col = vi & 7; L[L_RED2 + w * 64 + 16 + (col & 3) * 4 + (vi >> 3) * 2 + (col >> 2)] = r; }
    WG_BARRIER();
    f32x4 gu = {0.f, 0.f, 0.f, 0.f};
#pragma unroll
    for (int ww = 0; ww < 4; ++ww) gu += *(LAS f32x4 *)(L + L_RED2 + ww * 64 + 16 + g4 * 4);
    f32x4 c0 = {0.f, 0.f, 0.f, 0.f}, c1 = c0;
#pragma unroll
    for (int i = 0; i < 2; ++i) {
      const float hv = rbf(gu[i]) * rbf(gu[2 + i]);
      const unsigned pp = cvtpk(hv, hv);
      const v2u a = {pp & mk0, pp & mk1};
      c0 = mfma16(a, wd[i][0], wd[i][1], c0);
      c1 = mfma16(a, wd[i][2], wd[i][3], c1);
    }
    publish512<512 / ND>(rws, WS_PD, NPF, pf, c0, c1, L + L_TR, w, lane, tag);
  }
  STAMP_AT(10);
  if (b >= NWG - ND) {
    constexpr int C = 512 / ND, S = C / 2;
    const int j = b - (NWG - ND);
    const int col = j * C + 2 * (lane % S);
    const unsigned h0 = __shfl(hb[0], col >> 3), h1 = __shfl(hb[1], col >> 3), h2 = __shfl(hb[2], col >> 3), h3 = __shfl(hb[3], col >> 3);
    const int pi = (col >> 1) & 3;
    const unsigned hp = pi == 0 ? h0 : pi == 1 ? h1 : pi == 2 ? h2 : h3;
    WG_BARRIER();
    const f32x2 r = reduce_cols<C, NPF>(rws, WS_PD, j, tag, L + L_RED, tid, w, lane);
    const unsigned op = cvtpk(lo16(hp) + rbf(r[0]), hi16(hp) + rbf(r[1]));
    if (w == 0 && lane < S) ((unsigned *)p.out)[j * S + lane] = op;
    if (j == 0 && tid == 0) __builtin_amdgcn_raw_buffer_store_b32(tag, rws, WS_EPOCH, 0, AUX_SC1);
  }
  STAMP_AT(11);
}
}

extern "C" void kernel_launch(void *const *d_in, const int *in_sizes, int n_in, void *d_out, int out_size, void *d_ws, size_t ws_size,
                              hipStream_t stream) {
  Params p;
  p.x = d_in[0]; p.k = d_in[1]; p.v = d_in[2]; p.wq = d_in[3]; p.wo = d_in[4];
  p.wg = d_in[6]; p.wu = d_in[7]; p.wd = d_in[8];
  p.out = d_out; p.ws = d_ws;
  (void)hipFuncSetAttribute((const void *)fused_decode_kernel, hipFuncAttributeMaxDynamicSharedMemorySize, LDS_BYTES);
  fused_decode_kernel<<<NWG, NTHR, LDS_BYTES, stream>>>(p);
}
```

```cpp
#include <hip/hip_runtime.h>
#include <stdint.h>

#define STAMP 0
#define NB1 64
#define ND 32
#define REP 32
#define QREP 4

namespace {
typedef unsigned v4u __attribute__((ext_vector_type(4)));
typedef unsigned v2u __attribute__((ext_vector_type(2)));
typedef float f32x4 __attribute__((ext_vector_type(4)));
typedef float f32x2 __attribute__((ext_vector_type(2)));
typedef short s16x4 __attribute__((ext_vector_type(4)));
typedef __bf16 bf16x8 __attribute__((ext_vector_type(8)));
typedef __bf16 bf16x2 __attribute__((ext_vector_type(2)));
typedef __amdgpu_buffer_rsrc_t rsrc_t;
#define LAS __attribute__((address_space(3)))

constexpr int DM = 512, SK = 65536, DH = 128, FF = 1792;
constexpr int NWG = 256, NTHR = 256;
constexpr int NPF = 224;
constexpr int AUX_NT = 2, AUX_SC1 = 16;
constexpr int RS3 = 0x00020000;
constexpr int WS_EPOCH = 0;
constexpr int WS_QG = 4096;
constexpr int WS_ML = WS_QG + 8 * 16384;
constexpr int WS_HG = WS_ML + 131072;
constexpr int WS_OP = 1 << 20;
constexpr int WS_PB = 2 << 20;
constexpr int WS_PD = 3 << 20;
constexpr int WS_USED = 4 << 20;
constexpr int WS_STAMP = 8 << 20;
constexpr unsigned SPIN_CAP = 1u << 14;
constexpr float LOG2E = 1.4426950408889634f;
constexpr float SCALE = 0.08838834764831845f;

struct Params {
  const void *x, *k, *v, *wq, *wo, *wg, *wu, *wd;
  void *out, *ws;
};

__device__ __forceinline__ float asf(unsigned u) { return __builtin_bit_cast(float, u); }
__device__ __forceinline__ unsigned asu(float f) { return __builtin_bit_cast(unsigned, f); }
template <int C> __device__ __forceinline__ float dpp(float x) {
  return __builtin_bit_cast(float, __builtin_amdgcn_mov_dpp(__builtin_bit_cast(int, x), C, 0xf, 0xf, true));
}
constexpr int XOR1 = 0xB1, XOR2 = 0x4E, HMIR = 0x141, MIR = 0x140, ROR4 = 0x124, ROR8 = 0x128;
__device__ __forceinline__ unsigned cvtpk(float lo, float hi) {
  f32x2 v = {lo, hi};
  bf16x2 r = __builtin_convertvector(v, bf16x2);
  return __builtin_bit_cast(unsigned, r);
}
__device__ __forceinline__ float lo16(unsigned u) { return asf(u << 16); }
__device__ __forceinline__ float hi16(unsigned u) { return asf(u & 0xffff0000u); }
__device__ __forceinline__ float rbf(float x) { return lo16(cvtpk(x, x)); }
__device__ __forceinline__ float xr16_max(float x) {
  auto s = __builtin_amdgcn_permlane16_swap(asu(x), asu(x), false, false);
  x = fmaxf(asf(s[0]), asf(s[1]));
  auto t = __builtin_amdgcn_permlane32_swap(asu(x), asu(x), false, false);
  return fmaxf(asf(t[0]), asf(t[1]));
}
__device__ __forceinline__ float xr16_sum(float x) {
  auto s = __builtin_amdgcn_permlane16_swap(asu(x), asu(x), false, false);
  x = asf(s[0]) + asf(s[1]);
  auto t = __builtin_amdgcn_permlane32_swap(asu(x), asu(x), false, false);
  return asf(t[0]) + asf(t[1]);
}
__device__ __forceinline__ float row_sum(float x) {
  x += dpp<XOR1>(x); x += dpp<XOR2>(x); x += dpp<HMIR>(x); x += dpp<MIR>(x); return x;
}
__device__ __forceinline__ float row_max(float x) {
  x = fmaxf(x, dpp<XOR1>(x)); x = fmaxf(x, dpp<XOR2>(x)); x = fmaxf(x, dpp<HMIR>(x)); x = fmaxf(x, dpp<MIR>(x)); return x;
}
__device__ __forceinline__ float wave_sum(float x) { return xr16_sum(row_sum(x)); }
__device__ __forceinline__ f32x4 mfma32(v4u a, v4u b, f32x4 c) {
  return __builtin_amdgcn_mfma_f32_16x16x32_bf16(__builtin_bit_cast(bf16x8, a), __builtin_bit_cast(bf16x8, b), c, 0, 0, 0);
}
__device__ __forceinline__ f32x4 mfma16(v2u a, unsigned b0, unsigned b1, f32x4 c) {
  v2u b = {b0, b1};
  return __builtin_amdgcn_mfma_f32_16x16x16bf16_1k(__builtin_bit_cast(s16x4, a), __builtin_bit_cast(s16x4, b), c, 0, 0, 0);
}
template <int AUX> __device__ __forceinline__ v4u ld16(rsrc_t r, int voff, int soff) {
  return __builtin_amdgcn_raw_buffer_load_b128(r, voff, soff, AUX);
}
__device__ __forceinline__ void st16(rsrc_t r, int voff, v4u v) { __builtin_amdgcn_raw_buffer_store_b128(v, r, voff, 0, AUX_SC1); }
__device__ __forceinline__ void st8(rsrc_t r, int voff, v2u v) { __builtin_amdgcn_raw_buffer_store_b64(v, r, voff, 0, AUX_SC1); }
#define WG_BARRIER() asm volatile("s_waitcnt lgkmcnt(0)\n\ts_barrier" ::: "memory")
#define LDS_WAIT() asm volatile("s_waitcnt lgkmcnt(0)" ::: "memory")
#define SB() __builtin_amdgcn_sched_barrier(0)

__device__ __forceinline__ v4u layernorm8(v4u p) {
  float x[8];
#pragma unroll
  for (int i = 0; i < 4; ++i) { x[2 * i] = lo16(p[i]); x[2 * i + 1] = hi16(p[i]); }
  float s = ((x[0] + x[1]) + (x[2] + x[3])) + ((x[4] + x[5]) + (x[6] + x[7]));
  const float mean = wave_sum(s) * (1.f / DM);
  float q = 0.f;
#pragma unroll
  for (int i = 0; i < 8; ++i) { const float d = x[i] - mean; q = fmaf(d, d, q); }
  const float var = wave_sum(q) * (1.f / DM);
  const float mb = rbf(mean);
  const float den = rbf(sqrtf(rbf(rbf(var) + 1e-5f)));
  v4u r;
#pragma unroll
  for (int i = 0; i < 4; ++i) r[i] = cvtpk(rbf(x[2 * i] - mb) / den, rbf(x[2 * i + 1] - mb) / den);
  return r;
}
struct LnStat { float mb, rden; float den; };
__device__ __forceinline__ LnStat ln_stats(v4u p) {
  float x[8];
#pragma unroll
  for (int i = 0; i < 4; ++i) { x[2 * i] = lo16(p[i]); x[2 * i + 1] = hi16(p[i]); }
  float s = ((x[0] + x[1]) + (x[2] + x[3])) + ((x[4] + x[5]) + (x[6] + x[7]));
  const float mean = wave_sum(s) * (1.f / DM);
  float q = 0.f;
#pragma unroll
  for (int i = 0; i < 8; ++i) { const float d = x[i] - mean; q = fmaf(d, d, q); }
  const float var = wave_sum(q) * (1.f / DM);
  LnStat r; r.mb = rbf(mean); r.den = rbf(sqrtf(rbf(rbf(var) + 1e-5f))); r.rden = 0.f;
  return r;
}
__device__ __forceinline__ unsigned ln_pair(unsigned p, LnStat st) { return cvtpk(rbf(lo16(p) - st.mb) / st.den, rbf(hi16(p) - st.mb) / st.den); }
__device__ __forceinline__ unsigned pick4(v4u v, int w) {
  const unsigned a = (w & 1) ? v[1] : v[0], b = (w & 1) ? v[3] : v[2];
  return (w & 2) ? b : a;
}
__device__ __forceinline__ float gemv16(unsigned xp, v4u a0, v4u a1, v4u b0, v4u b1, int lane) {
  float v[16];
  const bf16x2 xb = __builtin_bit_cast(bf16x2, xp);
#pragma unroll
  for (int i = 0; i < 4; ++i) {
    const unsigned alo = __builtin_amdgcn_perm(a1[i], a0[i], 0x05040100u), ahi = __builtin_amdgcn_perm(a1[i], a0[i], 0x07060302u);
    const unsigned blo = __builtin_amdgcn_perm(b1[i], b0[i], 0x05040100u), bhi = __builtin_amdgcn_perm(b1[i], b0[i], 0x07060302u);
    v[2 * i] = __builtin_amdgcn_fdot2_f32_bf16(__builtin_bit_cast(bf16x2, alo), xb, 0.f, false);
    v[2 * i + 1] = __builtin_amdgcn_fdot2_f32_bf16(__builtin_bit_cast(bf16x2, ahi), xb, 0.f, false);
    v[8 + 2 * i] = __builtin_amdgcn_fdot2_f32_bf16(__builtin_bit_cast(bf16x2, blo), xb, 0.f, false);
    v[8 + 2 * i + 1] = __builtin_amdgcn_fdot2_f32_bf16(__builtin_bit_cast(bf16x2, bhi), xb, 0.f, false);
  }
  float u[8];
#pragma unroll
  for (int k = 0; k < 8; ++k) { auto s = __builtin_amdgcn_permlane32_swap(asu(v[k]), asu(v[k + 8]), false, false); u[k] = asf(s[0]) + asf(s[1]); }
  float t[4];
#pragma unroll
  for (int k = 0; k < 4; ++k) { auto s = __builtin_amdgcn_permlane16_swap(asu(u[k]), asu(u[k + 4]), false, false); t[k] = asf(s[0]) + asf(s[1]); }
  float y[2];
#pragma unroll
  for (int k = 0; k < 2; ++k) { const float p = t[k] + dpp<ROR8>(t[k]), q = t[k + 2] + dpp<ROR8>(t[k + 2]); y[k] = (lane & 8) ? q : p; }
#pragma unroll
  for (int k = 0; k < 2; ++k) { y[k] += dpp<XOR1>(y[k]); y[k] += dpp<XOR2>(y[k]); y[k] += dpp<HMIR>(y[k]); }
  return (lane & 4) ? y[1] : y[0];
}

struct Tile { v4u k[8], v[8]; };

template <bool REFILL>
__device__ __forceinline__ void tile_step(Tile &t, v4u q, rsrc_t rk, rsrc_t rv, int koff, int voff, int snext, unsigned mk0, unsigned mk1,
                                          float &m2, float &lsum, f32x4 &acc0, f32x4 &acc1) {
  float s[8];
  const f32x4 z = {0.f, 0.f, 0.f, 0.f};
  f32x4 d[8];
#pragma unroll
  for (int g = 0; g < 8; ++g) {
    d[g] = mfma32(t.k[g], q, z);
    if (REFILL) t.k[g] = ld16<AUX_NT>(rk, koff, snext + g * 1024);
    SB();
  }
#pragma unroll
  for (int g = 0; g < 8; ++g) {
    s[g] = ((dpp<0x00>(d[g][0]) + dpp<0x55>(d[g][1])) + dpp<0xAA>(d[g][2])) + dpp<0xFF>(d[g][3]);
    asm("" : "+v"(s[g]));
  }
#pragma unroll
  for (int g = 0; g < 8; g += 2) {
    const unsigned a = cvtpk(s[g], s[g + 1]);
    const unsigned b = cvtpk(lo16(a) * SCALE, hi16(a) * SCALE);
    s[g] = lo16(b); s[g + 1] = hi16(b);
  }
  float mx = fmaxf(fmaxf(fmaxf(s[0], s[1]), fmaxf(s[2], s[3])), fmaxf(fmaxf(s[4], s[5]), fmaxf(s[6], s[7])));
  mx = xr16_max(mx);
  const float mn = fmaxf(m2, mx * LOG2E);
  const float alpha = __builtin_amdgcn_exp2f(m2 - mn);
  m2 = mn;
  lsum *= alpha;
  int ai = __builtin_bit_cast(int, alpha), ar;
  ar = __builtin_amdgcn_update_dpp(ai, ai, 0x150, 0x1, 0xf, false);
  ar = __builtin_amdgcn_update_dpp(ar, ai, 0x154, 0x2, 0xf, false);
  ar = __builtin_amdgcn_update_dpp(ar, ai, 0x158, 0x4, 0xf, false);
  ar = __builtin_amdgcn_update_dpp(ar, ai, 0x15c, 0x8, 0xf, false);
  const float al = __builtin_bit_cast(float, ar);
  acc0 *= al; acc1 *= al;
#pragma unroll
  for (int g = 0; g < 8; ++g) {
    const float p = __builtin_amdgcn_exp2f(fmaf(s[g], LOG2E, -mn));
    lsum += p;
    const unsigned pp = cvtpk(p, p);
    const v2u a = {pp & mk0, pp & mk1};
    acc0 = mfma16(a, t.v[g][0], t.v[g][1], acc0);
    acc1 = mfma16(a, t.v[g][2], t.v[g][3], acc1);
    if (REFILL) t.v[g] = ld16<AUX_NT>(rv, voff, snext + g * 1024);
    SB();
  }
}

template <int N>
__device__ __forceinline__ void gather(rsrc_t rs, const int (&off)[N], unsigned tag, v4u (&x)[N]) {
  for (unsigned spins = 0;; ++spins) {
    bool ok = true;
#pragma unroll
    for (int i = 0; i < N; ++i) x[i] = ld16<AUX_SC1>(rs, off[i], 0);
#pragma unroll
    for (int i = 0; i < N; ++i) ok = ok && x[i][1] == tag && x[i][3] == tag;
    if (__all(ok) || spins > SPIN_CAP) break;
    __builtin_amdgcn_s_sleep(1);
  }
}
template <int S> __device__ __forceinline__ float strided_sum(float x) {
  if (S <= 1) x += dpp<XOR1>(x);
  if (S <= 2) x += dpp<XOR2>(x);
  if (S <= 8) x += dpp<ROR8>(x);
  if (S <= 4) x += dpp<ROR4>(x);
  return xr16_sum(x);
}
template <int C>
__device__ __forceinline__ void publish512(rsrc_t rs, int base, int nprod, int p, f32x4 a0, f32x4 a1, LAS float *tr, int w, int lane, unsigned tag) {
  if (lane < 16) {
    *(LAS f32x4 *)(tr + w * 128 + 8 * lane) = a0;
    *(LAS f32x4 *)(tr + w * 128 + 8 * lane + 4) = a1;
  }
  LDS_WAIT();
  const f32x2 y = *(LAS f32x2 *)(tr + w * 128 + 2 * lane);
  const int col = 128 * w + 2 * lane, j = col / C, within = col % C;
  const v4u g = {asu(y[0]), tag, asu(y[1]), tag};
  st16(rs, base + ((j * nprod + p) * C + within) * 8, g);
}
template <int C, int NP>
__device__ __forceinline__ f32x2 reduce_cols(rsrc_t rs, int base, int j, unsigned tag, LAS float *red, int tid, int w, int lane) {
  constexpr int S = C / 2, NPIECE = NP * S, N = (NPIECE + NTHR - 1) / NTHR;
  int off[N]; v4u x[N];
#pragma unroll
  for (int i = 0; i < N; ++i) { const int pc = i * NTHR + tid; off[i] = base + j * (NP * C * 8) + (pc < NPIECE ? pc : tid) * 16; }
  gather<N>(rs, off, tag, x);
  float s0 = 0.f, s1 = 0.f;
#pragma unroll
  for (int i = 0; i < N; ++i) { const bool live = i * NTHR + tid < NPIECE; s0 += live ? asf(x[i][0]) : 0.f; s1 += live ? asf(x[i][2]) : 0.f; }
  s0 = strided_sum<S>(s0); s1 = strided_sum<S>(s1);
  if (lane < S) { f32x2 y = {s0, s1}; *(LAS f32x2 *)(red + w * 64 + 2 * lane) = y; }
  WG_BARRIER();
  f32x2 r = {0.f, 0.f};
#pragma unroll
  for (int ww = 0; ww < 4; ++ww) r += *(LAS f32x2 *)(red + ww * 64 + 2 * (lane % S));
  return r;
}

#define STAMP_AT(i) do { } while (0)
#define STAMP_FLUSH() do { } while (0)

constexpr int L_MRG = 0;
constexpr int L_ML = L_MRG + 2048;
constexpr int L_RED = L_ML + 32;
constexpr int L_RED2 = L_RED + 256;
constexpr int L_TR = L_RED2 + 256;
constexpr int L_Q = L_TR + 512;
constexpr int L_XN = L_Q + 256;
constexpr int L_END = L_XN + 256;
constexpr int LDS_BYTES = 84 * 1024;

__global__ void __launch_bounds__(NTHR, 2) fused_decode_kernel(Params p) {
  extern __shared__ __attribute__((aligned(16))) float smem[];
  LAS float *L = (LAS float *)smem;
  const int tid = threadIdx.x, lane = tid & 63, w = __builtin_amdgcn_readfirstlane(tid >> 6), b = blockIdx.x;
  STAMP_AT(0);
  const int kh = b >> 5, sp = b & 31;
  const rsrc_t rws = __builtin_amdgcn_make_buffer_rsrc(p.ws, 0, WS_USED, RS3);
  const rsrc_t rk = __builtin_amdgcn_make_buffer_rsrc((char *)p.k + ((size_t)kh * SK + sp * 2048) * 256, 0, 2048 * 256, RS3);
  const rsrc_t rv = __builtin_amdgcn_make_buffer_rsrc((char *)p.v + ((size_t)kh * SK + sp * 2048) * 256, 0, 2048 * 256, RS3);
  const rsrc_t rx = __builtin_amdgcn_make_buffer_rsrc((void *)p.x, 0, DM * 2, RS3);
  const rsrc_t rwq = __builtin_amdgcn_make_buffer_rsrc((void *)p.wq, 0, DM * 4096 * 2, RS3);
  const unsigned epoch = *(const __attribute__((address_space(4))) unsigned *)((const char *)p.ws + WS_EPOCH);
  const v4u xr = ld16<0>(rx, lane * 16, 0);
  const int qf = (b & 7) * 32 + (b >> 3);
  const unsigned xraw = __builtin_amdgcn_raw_buffer_load_b32(rx, (64 * w + lane) * 4, 0, 0);
  const int wqoff = (128 * w + 2 * lane) * 8192 + qf * 32;
  const v4u wa0 = ld16<AUX_NT>(rwq, wqoff, 0), wa1 = ld16<AUX_NT>(rwq, wqoff, 8192);
  const v4u wb0 = ld16<AUX_NT>(rwq, wqoff, 16), wb1 = ld16<AUX_NT>(rwq, wqoff, 8192 + 16);
  SB();
  asm volatile("s_barrier" ::: "memory");
  SB();
  const int g4 = lane >> 4, a4 = (lane >> 2) & 3, u4 = lane & 3;
  const int koff = a4 * 256 + (4 * g4 + u4) * 16, voff = lane * 16;
  Tile ta, tb;
#pragma unroll
  for (int g = 0; g < 8; ++g) ta.k[g] = ld16<AUX_NT>(rk, koff, w * 8192 + g * 1024);
#pragma unroll
  for (int g = 0; g < 8; ++g) ta.v[g] = ld16<AUX_NT>(rv, voff, w * 8192 + g * 1024);
  SB();
  const unsigned tag = epoch + 1u;
  {
    unsigned xsel = ln_pair(xraw, ln_stats(xr));
    const float r = gemv16(xsel, wa0, wa1, wb0, wb1, lane);
    if ((lane & 3) == 0) L[L_RED + w * 64 + (lane >> 2)] = r;
    WG_BARRIER();
    if (w == 0) {
      const int i = lane & 7;
      float s0 = 0.f, s1 = 0.f;
#pragma unroll
      for (int ww = 0; ww < 4; ++ww) { const f32x2 y = *(LAS f32x2 *)(L + L_RED + ww * 64 + 2 * i); s0 += y[0]; s1 += y[1]; }
      const v2u g = {cvtpk(s0, s1), tag};
      if (lane < 8 * QREP) st8(rws, WS_QG + (lane >> 3) * 16384 + ((b & 7) * 256 + (b >> 3) * 8 + i) * 8, g);
    }
  }
  STAMP_AT(1);
  asm volatile("s_waitcnt vmcnt(9)" ::: "memory");
  v4u q;
  {
    const int qoff = WS_QG + (b & (QREP - 1)) * 16384 + (kh * 256 + w * 64) * 8 + (lane & 31) * 16;
    v4u x0 = ld16<AUX_SC1>(rws, qoff, 0);
    SB();
#pragma unroll
    for (int g = 0; g < 8; ++g) tb.k[g] = ld16<AUX_NT>(rk, koff, (4 + w) * 8192 + g * 1024);
#pragma unroll
    for (int g = 0; g < 8; ++g) tb.v[g] = ld16<AUX_NT>(rv, voff, (4 + w) * 8192 + g * 1024);
    SB();
    bool ok = x0[1] == tag && x0[3] == tag;
    for (unsigned spins = 0; !__all(ok) && spins < SPIN_CAP; ++spins) {
      x0 = ld16<AUX_SC1>(rws, qoff, 0);
      ok = x0[1] == tag && x0[3] == tag;
    }
    if (lane < 32) *(LAS v2u *)(L + L_Q + w * 64 + 2 * lane) = v2u{x0[0], x0[2]};
    WG_BARRIER();
    q = *(LAS v4u *)(L + L_Q + a4 * 64 + (4 * g4 + u4) * 4);
  }
  STAMP_AT(2);
  const int pf = (b & 7) * 28 + (b >> 3);
  const bool hasf = (b >> 3) < 28;
  const rsrc_t rwg = __builtin_amdgcn_make_buffer_rsrc((void *)p.wg, 0, hasf ? DM * FF * 2 : 0, RS3);
  const rsrc_t rwu = __builtin_amdgcn_make_buffer_rsrc((void *)p.wu, 0, hasf ? DM * FF * 2 : 0, RS3);
  const int go = (128 * w + 2 * lane) * (FF * 2) + pf * 16;
  const v4u wg0 = ld16<AUX_NT>(rwg, go, 0), wg1 = ld16<AUX_NT>(rwg, go, FF * 2);
  const v4u wu0 = ld16<AUX_NT>(rwu, go, 0), wu1 = ld16<AUX_NT>(rwu, go, FF * 2);
  SB();
  const unsigned mk0 = u4 == 0 ? 0xFFFFu : u4 == 1 ? 0xFFFF0000u : 0u;
  const unsigned mk1 = u4 == 2 ? 0xFFFFu : u4 == 3 ? 0xFFFF0000u : 0u;
  float m2 = -3.0e38f, lsum = 0.f;
  f32x4 acc0 = {0.f, 0.f, 0.f, 0.f}, acc1 = acc0;
  for (int it = 0; it < 7; ++it) {
    const int s0 = ((2 * it + 2) * 4 + w) * 8192;
    tile_step<true>(ta, q, rk, rv, koff, voff, s0, mk0, mk1, m2, lsum, acc0, acc1);
    tile_step<true>(tb, q, rk, rv, koff, voff, s0 + 4 * 8192, mk0, mk1, m2, lsum, acc0, acc1);
  }
  const rsrc_t rwo = __builtin_amdgcn_make_buffer_rsrc((char *)p.wo + (size_t)b * 16384, 0, 16384, RS3);
  const rsrc_t rwd = __builtin_amdgcn_make_buffer_rsrc((char *)p.wd + (size_t)(hasf ? pf : 0) * 8192, 0, hasf ? 8192 : 0, RS3);
  v4u wo[4], wd[2];
  {
    const int o = g4 * 1024 + w * 256 + (lane & 15) * 16;
#pragma unroll
    for (int i = 0; i < 4; ++i) wo[i] = ld16<AUX_NT>(rwo, o, i * 4096);
#pragma unroll
    for (int i = 0; i < 2; ++i) wd[i] = ld16<AUX_NT>(rwd, o, i * 4096);
    SB();
  }
  tile_step<false>(ta, q, rk, rv, koff, voff, 0, mk0, mk1, m2, lsum, acc0, acc1);
  tile_step<false>(tb, q, rk, rv, koff, voff, 0, mk0, mk1, m2, lsum, acc0, acc1);
  STAMP_AT(3);
  {
    const float lw = xr16_sum(lsum);
    *(LAS f32x4 *)(L + L_MRG + w * 512 + g4 * 128 + 8 * (lane & 15)) = acc0;
    *(LAS f32x4 *)(L + L_MRG + w * 512 + g4 * 128 + 8 * (lane & 15) + 4) = acc1;
    if (lane < 16 && u4 == 0) { f32x2 y = {m2, lw}; *(LAS f32x2 *)(L + L_ML + (w * 4 + a4) * 2) = y; }
    WG_BARRIER();
    f32x2 ml[4];
#pragma unroll
    for (int ww = 0; ww < 4; ++ww) ml[ww] = *(LAS f32x2 *)(L + L_ML + (ww * 4 + w) * 2);
    const float M = fmaxf(fmaxf(ml[0][0], ml[1][0]), fmaxf(ml[2][0], ml[3][0]));
    float Ls = 0.f; f32x2 o = {0.f, 0.f};
#pragma unroll
    for (int ww = 0; ww < 4; ++ww) {
      const float e = __builtin_amdgcn_exp2f(ml[ww][0] - M);
      Ls = fmaf(e, ml[ww][1], Ls);
      o += e * *(LAS f32x2 *)(L + L_MRG + ww * 512 + w * 128 + 2 * lane);
    }
    const int head = 4 * kh + w;
    st16(rws, WS_OP + (((head * 8 + (lane >> 3)) * 32 + sp) * 16 + 2 * (lane & 7)) * 8, v4u{asu(o[0]), tag, asu(o[1]), tag});
    if (lane < 8) st16(rws, WS_ML + ((head * 8 + lane) * 32 + sp) * 16, v4u{asu(M), tag, asu(Ls), tag});
  }
  STAMP_AT(4);
  {
    const int off[2] = {WS_OP + b * 4096 + tid * 16, WS_ML + (b * 32 + 8 * w + (lane >> 3)) * 16};
    v4u x[2];
    gather<2>(rws, off, tag, x);
    STAMP_AT(5);
    const float ms = asf(x[1][0]), ls = asf(x[1][2]);
    const float Mw = xr16_max(fmaxf(ms, dpp<ROR8>(ms)));
    const float e = __builtin_amdgcn_exp2f(ms - Mw);
    const float v0 = strided_sum<8>(e * asf(x[0][0])), v1 = strided_sum<8>(e * asf(x[0][2])), lw = strided_sum<8>(e * ls);
    if (lane < 8) {
      const int d = 2 * lane;
      L[L_RED2 + w * 64 + (d & 3) * 4 + (d >> 2)] = v0;
      L[L_RED2 + w * 64 + ((d + 1) & 3) * 4 + (d >> 2)] = v1;
      if (lane == 0) *(LAS f32x2 *)(L + L_RED2 + w * 64 + 32) = f32x2{Mw, lw};
    }
    WG_BARRIER();
    f32x2 ml[4];
#pragma unroll
    for (int ww = 0; ww < 4; ++ww) ml[ww] = *(LAS f32x2 *)(L + L_RED2 + ww * 64 + 32);
    const float M = fmaxf(fmaxf(ml[0][0], ml[1][0]), fmaxf(ml[2][0], ml[3][0]));
    f32x4 o = {0.f, 0.f, 0.f, 0.f};
    float Lt = 0.f;
#pragma unroll
    for (int ww = 0; ww < 4; ++ww) {
      const float ew = __builtin_amdgcn_exp2f(ml[ww][0] - M);
      Lt = fmaf(ew, ml[ww][1], Lt);
      o += ew * *(LAS f32x4 *)(L + L_RED2 + ww * 64 + g4 * 4);
    }
    o *= __builtin_amdgcn_rcpf(Lt);
    f32x4 c0 = {0.f, 0.f, 0.f, 0.f}, c1 = c0;
#pragma unroll
    for (int i = 0; i < 4; ++i) {
      const unsigned pp = cvtpk(o[i], o[i]);
      const v2u a = {pp & mk0, pp & mk1};
      c0 = mfma16(a, wo[i][0], wo[i][1], c0);
      c1 = mfma16(a, wo[i][2], wo[i][3], c1);
    }
    publish512<512 / NB1>(rws, WS_PB, 256, b, c0, c1, L + L_TR, w, lane, tag);
  }
  STAMP_AT(6);
  if (b < NB1) {
    constexpr int C = 512 / NB1, S = C / 2;
    const unsigned xp = __builtin_amdgcn_raw_buffer_load_b32(rx, (b * C + 2 * (lane % S)) * 2, 0, 0);
    const f32x2 r = reduce_cols<C, 256>(rws, WS_PB, b, tag, L + L_RED, tid, w, lane);
    STAMP_AT(7);
    const unsigned hp = cvtpk(lo16(xp) + rbf(r[0]), hi16(xp) + rbf(r[1]));
    if (tid < S * REP) st8(rws, WS_HG + (tid / S) * 2048 + (b * S + tid % S) * 8, v2u{hp, tag});
  }
  STAMP_AT(8);
  if (w == 0) {
    const int base = WS_HG + (b % REP) * 2048 + lane * 32;
    const int off[2] = {base, base + 16};
    v4u x[2];
    gather<2>(rws, off, tag, x);
    const v4u hb = v4u{x[0][0], x[0][2], x[1][0], x[1][2]};
    *(LAS v4u *)(L + L_Q + 4 * lane) = hb;
    *(LAS v4u *)(L + L_XN + 4 * lane) = layernorm8(hb);
  }
  WG_BARRIER();
  STAMP_AT(9);
  if (hasf) {
    const unsigned xsel = ((LAS unsigned *)L)[L_XN + 64 * w + lane];
    const float r = gemv16(xsel, wg0, wg1, wu0, wu1, lane);
    if ((lane & 3) == 0) { const int vi = lane >> 2, col = vi & 7; L[L_RED2 + w * 64 + 16 + (col & 3) * 4 + (vi >> 3) * 2 + (col >> 2)] = r; }
    WG_BARRIER();
    f32x4 gu = {0.f, 0.f, 0.f, 0.f};
#pragma unroll
    for (int ww = 0; ww < 4; ++ww) gu += *(LAS f32x4 *)(L + L_RED2 + ww * 64 + 16 + g4 * 4);
    f32x4 c0 = {0.f, 0.f, 0.f, 0.f}, c1 = c0;
#pragma unroll
    for (int i = 0; i < 2; ++i) {
      const float hv = rbf(gu[i]) * rbf(gu[2 + i]);
      const unsigned pp = cvtpk(hv, hv);
      const v2u a = {pp & mk0, pp & mk1};
      c0 = mfma16(a, wd[i][0], wd[i][1], c0);
      c1 = mfma16(a, wd[i][2], wd[i][3], c1);
    }
    publish512<512 / ND>(rws, WS_PD, NPF, pf, c0, c1, L + L_TR, w, lane, tag);
  }
  STAMP_AT(10);
  if (b >= NWG - ND) {
    constexpr int C = 512 / ND, S = C / 2;
    const int j = b - (NWG - ND);
    const unsigned hp = ((LAS unsigned *)L)[L_Q + j * S + lane % S];
    WG_BARRIER();
    const f32x2 r = reduce_cols<C, NPF>(rws, WS_PD, j, tag, L + L_RED, tid, w, lane);
    const unsigned op = cvtpk(lo16(hp) + rbf(r[0]), hi16(hp) + rbf(r[1]));
    if (w == 0 && lane < S) ((unsigned *)p.out)[j * S + lane] = op;
    if (j == 0 && tid == 0) __builtin_amdgcn_raw_buffer_store_b32(tag, rws, WS_EPOCH, 0, AUX_SC1);
  }
  STAMP_AT(11);
  STAMP_FLUSH();
}
}

extern "C" void kernel_launch(void *const *d_in, const int *in_sizes, int n_in, void *d_out, int out_size, void *d_ws, size_t ws_size,
                              hipStream_t stream) {
  Params p;
  p.x = d_in[0]; p.k = d_in[1]; p.v = d_in[2]; p.wq = d_in[3]; p.wo = d_in[4];
  p.wg = d_in[6]; p.wu = d_in[7]; p.wd = d_in[8];
  p.out = d_out; p.ws = d_ws;
  (void)hipFuncSetAttribute((const void *)fused_decode_kernel, hipFuncAttributeMaxDynamicSharedMemorySize, LDS_BYTES);
  fused_decode_kernel<<<NWG, NTHR, LDS_BYTES, stream>>>(p);
}
```

```cpp
#include <hip/hip_runtime.h>
#include <stdint.h>

#define STAMP 0
#define NB1 64
#define ND 32
#define REP 32
#define KAUX 2
#define VAUX 0
#define WAUX 0
#define QREP 4

namespace {
typedef unsigned v4u __attribute__((ext_vector_type(4)));
typedef unsigned v2u __attribute__((ext_vector_type(2)));
typedef float f32x4 __attribute__((ext_vector_type(4)));
typedef float f32x2 __attribute__((ext_vector_type(2)));
typedef short s16x4 __attribute__((ext_vector_type(4)));
typedef __bf16 bf16x8 __attribute__((ext_vector_type(8)));
typedef __bf16 bf16x2 __attribute__((ext_vector_type(2)));
typedef __amdgpu_buffer_rsrc_t rsrc_t;
#define LAS __attribute__((address_space(3)))

constexpr int DM = 512, SK = 65536, DH = 128, FF = 1792;
constexpr int NWG = 256, NTHR = 256;
constexpr int NPF = 224;
constexpr int AUX_NT = 2, AUX_SC1 = 16;
constexpr int RS3 = 0x00020000;
constexpr int WS_EPOCH = 0;
constexpr int WS_QG = 4096;
constexpr int WS_ML = WS_QG + 8 * 16384;
constexpr int WS_HG = WS_ML + 131072;
constexpr int WS_OP = 1 << 20;
constexpr int WS_PB = 2 << 20;
constexpr int WS_PD = 3 << 20;
constexpr int WS_USED = 4 << 20;
constexpr int WS_STAMP = 8 << 20;
constexpr unsigned SPIN_CAP = 1u << 14;
constexpr float LOG2E = 1.4426950408889634f;
constexpr float SCALE = 0.08838834764831845f;

struct Params {
  const void *x, *k, *v, *wq, *wo, *wg, *wu, *wd;
  void *out, *ws;
};

__device__ __forceinline__ float asf(unsigned u) { return __builtin_bit_cast(float, u); }
__device__ __forceinline__ unsigned asu(float f) { return __builtin_bit_cast(unsigned, f); }
template <int C> __device__ __forceinline__ float dpp(float x) {
  return __builtin_bit_cast(float, __builtin_amdgcn_mov_dpp(__builtin_bit_cast(int, x), C, 0xf, 0xf, true));
}
constexpr int XOR1 = 0xB1, XOR2 = 0x4E, HMIR = 0x141, MIR = 0x140, ROR4 = 0x124, ROR8 = 0x128;
__device__ __forceinline__ unsigned cvtpk(float lo, float hi) {
  f32x2 v = {lo, hi};
  bf16x2 r = __builtin_convertvector(v, bf16x2);
  return __builtin_bit_cast(unsigned, r);
}
__device__ __forceinline__ float lo16(unsigned u) { return asf(u << 16); }
__device__ __forceinline__ float hi16(unsigned u) { return asf(u & 0xffff0000u); }
__device__ __forceinline__ float rbf(float x) { return lo16(cvtpk(x, x)); }
__device__ __forceinline__ float xr16_max(float x) {
  auto s = __builtin_amdgcn_permlane16_swap(asu(x), asu(x), false, false);
  x = fmaxf(asf(s[0]), asf(s[1]));
  auto t = __builtin_amdgcn_permlane32_swap(asu(x), asu(x), false, false);
  return fmaxf(asf(t[0]), asf(t[1]));
}
__device__ __forceinline__ float xr16_sum(float x) {
  auto s = __builtin_amdgcn_permlane16_swap(asu(x), asu(x), false, false);
  x = asf(s[0]) + asf(s[1]);
  auto t = __builtin_amdgcn_permlane32_swap(asu(x), asu(x), false, false);
  return asf(t[0]) + asf(t[1]);
}
__device__ __forceinline__ float row_sum(float x) {
  x += dpp<XOR1>(x); x += dpp<XOR2>(x); x += dpp<HMIR>(x); x += dpp<MIR>(x); return x;
}
__device__ __forceinline__ float row_max(float x) {
  x = fmaxf(x, dpp<XOR1>(x)); x = fmaxf(x, dpp<XOR2>(x)); x = fmaxf(x, dpp<HMIR>(x)); x = fmaxf(x, dpp<MIR>(x)); return x;
}
__device__ __forceinline__ float wave_sum(float x) { return xr16_sum(row_sum(x)); }
__device__ __forceinline__ f32x4 mfma32(v4u a, v4u b, f32x4 c) {
  return __builtin_amdgcn_mfma_f32_16x16x32_bf16(__builtin_bit_cast(bf16x8, a), __builtin_bit_cast(bf16x8, b), c, 0, 0, 0);
}
__device__ __forceinline__ f32x4 mfma16(v2u a, unsigned b0, unsigned b1, f32x4 c) {
  v2u b = {b0, b1};
  return __builtin_amdgcn_mfma_f32_16x16x16bf16_1k(__builtin_bit_cast(s16x4, a), __builtin_bit_cast(s16x4, b), c, 0, 0, 0);
}
template <int AUX> __device__ __forceinline__ v4u ld16(rsrc_t r, int voff, int soff) {
  return __builtin_amdgcn_raw_buffer_load_b128(r, voff, soff, AUX);
}
__device__ __forceinline__ void st16(rsrc_t r, int voff, v4u v) { __builtin_amdgcn_raw_buffer_store_b128(v, r, voff, 0, AUX_SC1); }
__device__ __forceinline__ void st8(rsrc_t r, int voff, v2u v) { __builtin_amdgcn_raw_buffer_store_b64(v, r, voff, 0, AUX_SC1); }
#define WG_BARRIER() asm volatile("s_waitcnt lgkmcnt(0)\n\ts_barrier" ::: "memory")
#define LDS_WAIT() asm volatile("s_waitcnt lgkmcnt(0)" ::: "memory")
#define SB() __builtin_amdgcn_sched_barrier(0)

__device__ __forceinline__ v4u layernorm8(v4u p) {
  float x[8];
#pragma unroll
  for (int i = 0; i < 4; ++i) { x[2 * i] = lo16(p[i]); x[2 * i + 1] = hi16(p[i]); }
  float s = ((x[0] + x[1]) + (x[2] + x[3])) + ((x[4] + x[5]) + (x[6] + x[7]));
  const float mean = wave_sum(s) * (1.f / DM);
  float q = 0.f;
#pragma unroll
  for (int i = 0; i < 8; ++i) { const float d = x[i] - mean; q = fmaf(d, d, q); }
  const float var = wave_sum(q) * (1.f / DM);
  const float mb = rbf(mean);
  const float den = rbf(sqrtf(rbf(rbf(var) + 1e-5f)));
  v4u r;
#pragma unroll
  for (int i = 0; i < 4; ++i) r[i] = cvtpk(rbf(x[2 * i] - mb) / den, rbf(x[2 * i + 1] - mb) / den);
  return r;
}
struct LnStat { float mb, rden; float den; };
__device__ __forceinline__ LnStat ln_stats(v4u p) {
  float x[8];
#pragma unroll
  for (int i = 0; i < 4; ++i) { x[2 * i] = lo16(p[i]); x[2 * i + 1] = hi16(p[i]); }
  float s = ((x[0] + x[1]) + (x[2] + x[3])) + ((x[4] + x[5]) + (x[6] + x[7]));
  const float mean = wave_sum(s) * (1.f / DM);
  float q = 0.f;
#pragma unroll
  for (int i = 0; i < 8; ++i) { const float d = x[i] - mean; q = fmaf(d, d, q); }
  const float var = wave_sum(q) * (1.f / DM);
  LnStat r; r.mb = rbf(mean); r.den = rbf(sqrtf(rbf(rbf(var) + 1e-5f))); r.rden = 0.f;
  return r;
}
__device__ __forceinline__ unsigned ln_pair(unsigned p, LnStat st) { return cvtpk(rbf(lo16(p) - st.mb) / st.den, rbf(hi16(p) - st.mb) / st.den); }
__device__ __forceinline__ unsigned pick4(v4u v, int w) {
  const unsigned a = (w & 1) ? v[1] : v[0], b = (w & 1) ? v[3] : v[2];
  return (w & 2) ? b : a;
}
__device__ __forceinline__ float gemv16(unsigned xp, v4u a0, v4u a1, v4u b0, v4u b1, int lane) {
  float v[16];
  const bf16x2 xb = __builtin_bit_cast(bf16x2, xp);
#pragma unroll
  for (int i = 0; i < 4; ++i) {
    const unsigned alo = __builtin_amdgcn_perm(a1[i], a0[i], 0x05040100u), ahi = __builtin_amdgcn_perm(a1[i], a0[i], 0x07060302u);
    const unsigned blo = __builtin_amdgcn_perm(b1[i], b0[i], 0x05040100u), bhi = __builtin_amdgcn_perm(b1[i], b0[i], 0x07060302u);
    v[2 * i] = __builtin_amdgcn_fdot2_f32_bf16(__builtin_bit_cast(bf16x2, alo), xb, 0.f, false);
    v[2 * i + 1] = __builtin_amdgcn_fdot2_f32_bf16(__builtin_bit_cast(bf16x2, ahi), xb, 0.f, false);
    v[8 + 2 * i] = __builtin_amdgcn_fdot2_f32_bf16(__builtin_bit_cast(bf16x2, blo), xb, 0.f, false);
    v[8 + 2 * i + 1] = __builtin_amdgcn_fdot2_f32_bf16(__builtin_bit_cast(bf16x2, bhi), xb, 0.f, false);
  }
  float u[8];
#pragma unroll
  for (int k = 0; k < 8; ++k) { auto s = __builtin_amdgcn_permlane32_swap(asu(v[k]), asu(v[k + 8]), false, false); u[k] = asf(s[0]) + asf(s[1]); }
  float t[4];
#pragma unroll
  for (int k = 0; k < 4; ++k) { auto s = __builtin_amdgcn_permlane16_swap(asu(u[k]), asu(u[k + 4]), false, false); t[k] = asf(s[0]) + asf(s[1]); }
  float y[2];
#pragma unroll
  for (int k = 0; k < 2; ++k) { const float p = t[k] + dpp<ROR8>(t[k]), q = t[k + 2] + dpp<ROR8>(t[k + 2]); y[k] = (lane & 8) ? q : p; }
#pragma unroll
  for (int k = 0; k < 2; ++k) { y[k] += dpp<XOR1>(y[k]); y[k] += dpp<XOR2>(y[k]); y[k] += dpp<HMIR>(y[k]); }
  return (lane & 4) ? y[1] : y[0];
}

struct Tile { v4u k[8], v[8]; };

template <bool REFILL>
__device__ __forceinline__ void tile_step(Tile &t, v4u q, rsrc_t rk, rsrc_t rv, int koff, int voff, int snext, unsigned mk0, unsigned mk1,
                                          float &m2, float &lsum, f32x4 &acc0, f32x4 &acc1) {
  float s[8];
  const f32x4 z = {0.f, 0.f, 0.f, 0.f};
  f32x4 d[8];
#pragma unroll
  for (int g = 0; g < 8; ++g) {
    d[g] = mfma32(t.k[g], q, z);
    if (REFILL) t.k[g] = ld16<KAUX>(rk, koff, snext + g * 1024);
    SB();
  }
#pragma unroll
  for (int g = 0; g < 8; ++g) {
    s[g] = ((dpp<0x00>(d[g][0]) + dpp<0x55>(d[g][1])) + dpp<0xAA>(d[g][2])) + dpp<0xFF>(d[g][3]);
    asm("" : "+v"(s[g]));
  }
#pragma unroll
  for (int g = 0; g < 8; g += 2) {
    const unsigned a = cvtpk(s[g], s[g + 1]);
    const unsigned b = cvtpk(lo16(a) * SCALE, hi16(a) * SCALE);
    s[g] = lo16(b); s[g + 1] = hi16(b);
  }
  float mx = fmaxf(fmaxf(fmaxf(s[0], s[1]), fmaxf(s[2], s[3])), fmaxf(fmaxf(s[4], s[5]), fmaxf(s[6], s[7])));
  mx = xr16_max(mx);
  const float mn = fmaxf(m2, mx * LOG2E);
  const float alpha = __builtin_amdgcn_exp2f(m2 - mn);
  m2 = mn;
  lsum *= alpha;
  int ai = __builtin_bit_cast(int, alpha), ar;
  ar = __builtin_amdgcn_update_dpp(ai, ai, 0x150, 0x1, 0xf, false);
  ar = __builtin_amdgcn_update_dpp(ar, ai, 0x154, 0x2, 0xf, false);
  ar = __builtin_amdgcn_update_dpp(ar, ai, 0x158, 0x4, 0xf, false);
  ar = __builtin_amdgcn_update_dpp(ar, ai, 0x15c, 0x8, 0xf, false);
  const float al = __builtin_bit_cast(float, ar);
  acc0 *= al; acc1 *= al;
#pragma unroll
  for (int g = 0; g < 8; ++g) {
    const float p = __builtin_amdgcn_exp2f(fmaf(s[g], LOG2E, -mn));
    lsum += p;
    const unsigned pp = cvtpk(p, p);
    const v2u a = {pp & mk0, pp & mk1};
    acc0 = mfma16(a, t.v[g][0], t.v[g][1], acc0);
    acc1 = mfma16(a, t.v[g][2], t.v[g][3], acc1);
    if (REFILL) t.v[g] = ld16<VAUX>(rv, voff, snext + g * 1024);
    SB();
  }
}

template <int N>
__device__ __forceinline__ void gather(rsrc_t rs, const int (&off)[N], unsigned tag, v4u (&x)[N]) {
  for (unsigned spins = 0;; ++spins) {
    bool ok = true;
#pragma unroll
    for (int i = 0; i < N; ++i) x[i] = ld16<AUX_SC1>(rs, off[i], 0);
#pragma unroll
    for (int i = 0; i < N; ++i) ok = ok && x[i][1] == tag && x[i][3] == tag;
    if (__all(ok) || spins > SPIN_CAP) break;
    __builtin_amdgcn_s_sleep(1);
  }
}
template <int S> __device__ __forceinline__ float strided_sum(float x) {
  if (S <= 1) x += dpp<XOR1>(x);
  if (S <= 2) x += dpp<XOR2>(x);
  if (S <= 8) x += dpp<ROR8>(x);
  if (S <= 4) x += dpp<ROR4>(x);
  return xr16_sum(x);
}
template <int C>
__device__ __forceinline__ void publish512(rsrc_t rs, int base, int nprod, int p, f32x4 a0, f32x4 a1, LAS float *tr, int w, int lane, unsigned tag) {
  if (lane < 16) {
    *(LAS f32x4 *)(tr + w * 128 + 8 * lane) = a0;
    *(LAS f32x4 *)(tr + w * 128 + 8 * lane + 4) = a1;
  }
  LDS_WAIT();
  const f32x2 y = *(LAS f32x2 *)(tr + w * 128 + 2 * lane);
  const int col = 128 * w + 2 * lane, j = col / C, within = col % C;
  const v4u g = {asu(y[0]), tag, asu(y[1]), tag};
  st16(rs, base + ((j * nprod + p) * C + within) * 8, g);
}
template <int C, int NP>
__device__ __forceinline__ f32x2 reduce_cols(rsrc_t rs, int base, int j, unsigned tag, LAS float *red, int tid, int w, int lane) {
  constexpr int S = C / 2, NPIECE = NP * S, N = (NPIECE + NTHR - 1) / NTHR;
  int off[N]; v4u x[N];
#pragma unroll
  for (int i = 0; i < N; ++i) { const int pc = i * NTHR + tid; off[i] = base + j * (NP * C * 8) + (pc < NPIECE ? pc : tid) * 16; }
  gather<N>(rs, off, tag, x);
  float s0 = 0.f, s1 = 0.f;
#pragma unroll
  for (int i = 0; i < N; ++i) { const bool live = i * NTHR + tid < NPIECE; s0 += live ? asf(x[i][0]) : 0.f; s1 += live ? asf(x[i][2]) : 0.f; }
  s0 = strided_sum<S>(s0); s1 = strided_sum<S>(s1);
  if (lane < S) { f32x2 y = {s0, s1}; *(LAS f32x2 *)(red + w * 64 + 2 * lane) = y; }
  WG_BARRIER();
  f32x2 r = {0.f, 0.f};
#pragma unroll
  for (int ww = 0; ww < 4; ++ww) r += *(LAS f32x2 *)(red + ww * 64 + 2 * (lane % S));
  return r;
}

#define STAMP_AT(i) do { } while (0)
#define STAMP_FLUSH() do { } while (0)

constexpr int L_MRG = 0;
constexpr int L_ML = L_MRG + 2048;
constexpr int L_RED = L_ML + 32;
constexpr int L_RED2 = L_RED + 256;
constexpr int L_TR = L_RED2 + 256;
constexpr int L_Q = L_TR + 512;
constexpr int L_XN = L_Q + 256;
constexpr int L_END = L_XN + 256;
constexpr int LDS_BYTES = 84 * 1024;

__global__ void __launch_bounds__(NTHR, 2) fused_decode_kernel(Params p) {
  extern __shared__ __attribute__((aligned(16))) float smem[];
  LAS float *L = (LAS float *)smem;
  const int tid = threadIdx.x, lane = tid & 63, w = __builtin_amdgcn_readfirstlane(tid >> 6), b = blockIdx.x;
  STAMP_AT(0);
  const int kh = b >> 5, sp = b & 31;
  const rsrc_t rws = __builtin_amdgcn_make_buffer_rsrc(p.ws, 0, WS_USED, RS3);
  const rsrc_t rk = __builtin_amdgcn_make_buffer_rsrc((char *)p.k + ((size_t)kh * SK + sp * 2048) * 256, 0, 2048 * 256, RS3);
  const rsrc_t rv = __builtin_amdgcn_make_buffer_rsrc((char *)p.v + ((size_t)kh * SK + sp * 2048) * 256, 0, 2048 * 256, RS3);
  const rsrc_t rx = __builtin_amdgcn_make_buffer_rsrc((void *)p.x, 0, DM * 2, RS3);
  const rsrc_t rwq = __builtin_amdgcn_make_buffer_rsrc((void *)p.wq, 0, DM * 4096 * 2, RS3);
  const unsigned epoch = *(const __attribute__((address_space(4))) unsigned *)((const char *)p.ws + WS_EPOCH);
  const v4u xr = ld16<0>(rx, lane * 16, 0);
  const int qf = (b & 7) * 32 + (b >> 3);
  const unsigned xraw = __builtin_amdgcn_raw_buffer_load_b32(rx, (64 * w + lane) * 4, 0, 0);
  const int wqoff = (128 * w + 2 * lane) * 8192 + qf * 32;
  const v4u wa0 = ld16<WAUX>(rwq, wqoff, 0), wa1 = ld16<WAUX>(rwq, wqoff, 8192);
  const v4u wb0 = ld16<WAUX>(rwq, wqoff, 16), wb1 = ld16<WAUX>(rwq, wqoff, 8192 + 16);
  SB();
  asm volatile("s_barrier" ::: "memory");
  SB();
  const int g4 = lane >> 4, a4 = (lane >> 2) & 3, u4 = lane & 3;
  const int koff = a4 * 256 + (4 * g4 + u4) * 16, voff = lane * 16;
  Tile ta, tb;
#pragma unroll
  for (int g = 0; g < 8; ++g) ta.k[g] = ld16<KAUX>(rk, koff, w * 8192 + g * 1024);
#pragma unroll
  for (int g = 0; g < 8; ++g) ta.v[g] = ld16<VAUX>(rv, voff, w * 8192 + g * 1024);
  SB();
  const unsigned tag = epoch + 1u;
  {
    unsigned xsel = ln_pair(xraw, ln_stats(xr));
    const float r = gemv16(xsel, wa0, wa1, wb0, wb1, lane);
    if ((lane & 3) == 0) L[L_RED + w * 64 + (lane >> 2)] = r;
    WG_BARRIER();
    if (w == 0) {
      const int i = lane & 7;
      float s0 = 0.f, s1 = 0.f;
#pragma unroll
      for (int ww = 0; ww < 4; ++ww) { const f32x2 y = *(LAS f32x2 *)(L + L_RED + ww * 64 + 2 * i); s0 += y[0]; s1 += y[1]; }
      const v2u g = {cvtpk(s0, s1), tag};
      if (lane < 8 * QREP) st8(rws, WS_QG + (lane >> 3) * 16384 + ((b & 7) * 256 + (b >> 3) * 8 + i) * 8, g);
    }
  }
  STAMP_AT(1);
  asm volatile("s_waitcnt vmcnt(9)" ::: "memory");
  v4u q;
  {
    const int qoff = WS_QG + (b & (QREP - 1)) * 16384 + (kh * 256 + w * 64) * 8 + (lane & 31) * 16;
    v4u x0 = ld16<AUX_SC1>(rws, qoff, 0);
    SB();
#pragma unroll
    for (int g = 0; g < 8; ++g) tb.k[g] = ld16<KAUX>(rk, koff, (4 + w) * 8192 + g * 1024);
#pragma unroll
    for (int g = 0; g < 8; ++g) tb.v[g] = ld16<VAUX>(rv, voff, (4 + w) * 8192 + g * 1024);
    SB();
    bool ok = x0[1] == tag && x0[3] == tag;
    for (unsigned spins = 0; !__all(ok) && spins < SPIN_CAP; ++spins) {
      x0 = ld16<AUX_SC1>(rws, qoff, 0);
      ok = x0[1] == tag && x0[3] == tag;
    }
    if (lane < 32) *(LAS v2u *)(L + L_Q + w * 64 + 2 * lane) = v2u{x0[0], x0[2]};
    WG_BARRIER();
    q = *(LAS v4u *)(L + L_Q + a4 * 64 + (4 * g4 + u4) * 4);
  }
  STAMP_AT(2);
  const int pf = (b & 7) * 28 + (b >> 3);
  const bool hasf = (b >> 3) < 28;
  const rsrc_t rwg = __builtin_amdgcn_make_buffer_rsrc((void *)p.wg, 0, hasf ? DM * FF * 2 : 0, RS3);
  const rsrc_t rwu = __builtin_amdgcn_make_buffer_rsrc((void *)p.wu, 0, hasf ? DM * FF * 2 : 0, RS3);
  const int go = (128 * w + 2 * lane) * (FF * 2) + pf * 16;
  const v4u wg0 = ld16<WAUX>(rwg, go, 0), wg1 = ld16<WAUX>(rwg, go, FF * 2);
  const v4u wu0 = ld16<WAUX>(rwu, go, 0), wu1 = ld16<WAUX>(rwu, go, FF * 2);
  SB();
  const unsigned mk0 = u4 == 0 ? 0xFFFFu : u4 == 1 ? 0xFFFF0000u : 0u;
  const unsigned mk1 = u4 == 2 ? 0xFFFFu : u4 == 3 ? 0xFFFF0000u : 0u;
  float m2 = -3.0e38f, lsum = 0.f;
  f32x4 acc0 = {0.f, 0.f, 0.f, 0.f}, acc1 = acc0;
  for (int it = 0; it < 7; ++it) {
    const int s0 = ((2 * it + 2) * 4 + w) * 8192;
    tile_step<true>(ta, q, rk, rv, koff, voff, s0, mk0, mk1, m2, lsum, acc0, acc1);
    tile_step<true>(tb, q, rk, rv, koff, voff, s0 + 4 * 8192, mk0, mk1, m2, lsum, acc0, acc1);
  }
  const rsrc_t rwo = __builtin_amdgcn_make_buffer_rsrc((char *)p.wo + (size_t)b * 16384, 0, 16384, RS3);
  const rsrc_t rwd = __builtin_amdgcn_make_buffer_rsrc((char *)p.wd + (size_t)(hasf ? pf : 0) * 8192, 0, hasf ? 8192 : 0, RS3);
  v4u wo[4], wd[2];
  {
    const int o = g4 * 1024 + w * 256 + (lane & 15) * 16;
#pragma unroll
    for (int i = 0; i < 4; ++i) wo[i] = ld16<WAUX>(rwo, o, i * 4096);
#pragma unroll
    for (int i = 0; i < 2; ++i) wd[i] = ld16<WAUX>(rwd, o, i * 4096);
    SB();
  }
  tile_step<false>(ta, q, rk, rv, koff, voff, 0, mk0, mk1, m2, lsum, acc0, acc1);
  tile_step<false>(tb, q, rk, rv, koff, voff, 0, mk0, mk1, m2, lsum, acc0, acc1);
  STAMP_AT(3);
  {
    const float lw = xr16_sum(lsum);
    *(LAS f32x4 *)(L + L_MRG + w * 512 + g4 * 128 + 8 * (lane & 15)) = acc0;
    *(LAS f32x4 *)(L + L_MRG + w * 512 + g4 * 128 + 8 * (lane & 15) + 4) = acc1;
    if (lane < 16 && u4 == 0) { f32x2 y = {m2, lw}; *(LAS f32x2 *)(L + L_ML + (w * 4 + a4) * 2) = y; }
    WG_BARRIER();
    f32x2 ml[4];
#pragma unroll
    for (int ww = 0; ww < 4; ++ww) ml[ww] = *(LAS f32x2 *)(L + L_ML + (ww * 4 + w) * 2);
    const float M = fmaxf(fmaxf(ml[0][0], ml[1][0]), fmaxf(ml[2][0], ml[3][0]));
    float Ls = 0.f; f32x2 o = {0.f, 0.f};
#pragma unroll
    for (int ww = 0; ww < 4; ++ww) {
      const float e = __builtin_amdgcn_exp2f(ml[ww][0] - M);
      Ls = fmaf(e, ml[ww][1], Ls);
      o += e * *(LAS f32x2 *)(L + L_MRG + ww * 512 + w * 128 + 2 * lane);
    }
    const int head = 4 * kh + w;
    st16(rws, WS_OP + (((head * 8 + (lane >> 3)) * 32 + sp) * 16 + 2 * (lane & 7)) * 8, v4u{asu(o[0]), tag, asu(o[1]), tag});
    if (lane < 8) st16(rws, WS_ML + ((head * 8 + lane) * 32 + sp) * 16, v4u{asu(M), tag, asu(Ls), tag});
  }
  STAMP_AT(4);
  {
    const int off[2] = {WS_OP + b * 4096 + tid * 16, WS_ML + (b * 32 + 8 * w + (lane >> 3)) * 16};
    v4u x[2];
    gather<2>(rws, off, tag, x);
    STAMP_AT(5);
    const float ms = asf(x[1][0]), ls = asf(x[1][2]);
    const float Mw = xr16_max(fmaxf(ms, dpp<ROR8>(ms)));
    const float e = __builtin_amdgcn_exp2f(ms - Mw);
    const float v0 = strided_sum<8>(e * asf(x[0][0])), v1 = strided_sum<8>(e * asf(x[0][2])), lw = strided_sum<8>(e * ls);
    if (lane < 8) {
      const int d = 2 * lane;
      L[L_RED2 + w * 64 + (d & 3) * 4 + (d >> 2)] = v0;
      L[L_RED2 + w * 64 + ((d + 1) & 3) * 4 + (d >> 2)] = v1;
      if (lane == 0) *(LAS f32x2 *)(L + L_RED2 + w * 64 + 32) = f32x2{Mw, lw};
    }
    WG_BARRIER();
    f32x2 ml[4];
#pragma unroll
    for (int ww = 0; ww < 4; ++ww) ml[ww] = *(LAS f32x2 *)(L + L_RED2 + ww * 64 + 32);
    const float M = fmaxf(fmaxf(ml[0][0], ml[1][0]), fmaxf(ml[2][0], ml[3][0]));
    f32x4 o = {0.f, 0.f, 0.f, 0.f};
    float Lt = 0.f;
#pragma unroll
    for (int ww = 0; ww < 4; ++ww) {
      const float ew = __builtin_amdgcn_exp2f(ml[ww][0] - M);
      Lt = fmaf(ew, ml[ww][1], Lt);
      o += ew * *(LAS f32x4 *)(L + L_RED2 + ww * 64 + g4 * 4);
    }
    o *= __builtin_amdgcn_rcpf(Lt);
    f32x4 c0 = {0.f, 0.f, 0.f, 0.f}, c1 = c0;
#pragma unroll
    for (int i = 0; i < 4; ++i) {
      const unsigned pp = cvtpk(o[i], o[i]);
      const v2u a = {pp & mk0, pp & mk1};
      c0 = mfma16(a, wo[i][0], wo[i][1], c0);
      c1 = mfma16(a, wo[i][2], wo[i][3], c1);
    }
    publish512<512 / NB1>(rws, WS_PB, 256, b, c0, c1, L + L_TR, w, lane, tag);
  }
  STAMP_AT(6);
  if (b < NB1) {
    constexpr int C = 512 / NB1, S = C / 2;
    const unsigned xp = __builtin_amdgcn_raw_buffer_load_b32(rx, (b * C + 2 * (lane % S)) * 2, 0, 0);
    const f32x2 r = reduce_cols<C, 256>(rws, WS_PB, b, tag, L + L_RED, tid, w, lane);
    STAMP_AT(7);
    const unsigned hp = cvtpk(lo16(xp) + rbf(r[0]), hi16(xp) + rbf(r[1]));
    if (tid < S * REP) st8(rws, WS_HG + (tid / S) * 2048 + (b * S + tid % S) * 8, v2u{hp, tag});
  }
  STAMP_AT(8);
  if (w == 0) {
    const int base = WS_HG + (b % REP) * 2048 + lane * 32;
    const int off[2] = {base, base + 16};
    v4u x[2];
    gather<2>(rws, off, tag, x);
    const v4u hb = v4u{x[0][0], x[0][2], x[1][0], x[1][2]};
    *(LAS v4u *)(L + L_Q + 4 * lane) = hb;
    *(LAS v4u *)(L + L_XN + 4 * lane) = layernorm8(hb);
  }
  WG_BARRIER();
  STAMP_AT(9);
  if (hasf) {
    const unsigned xsel = ((LAS unsigned *)L)[L_XN + 64 * w + lane];
    const float r = gemv16(xsel, wg0, wg1, wu0, wu1, lane);
    if ((lane & 3) == 0) { const int vi = lane >> 2, col = vi & 7; L[L_RED2 + w * 64 + 16 + (col & 3) * 4 + (vi >> 3) * 2 + (col >> 2)] = r; }
    WG_BARRIER();
    f32x4 gu = {0.f, 0.f, 0.f, 0.f};
#pragma unroll
    for (int ww = 0; ww < 4; ++ww) gu += *(LAS f32x4 *)(L + L_RED2 + ww * 64 + 16 + g4 * 4);
    f32x4 c0 = {0.f, 0.f, 0.f, 0.f}, c1 = c0;
#pragma unroll
    for (int i = 0; i < 2; ++i) {
      const float hv = rbf(gu[i]) * rbf(gu[2 + i]);
      const unsigned pp = cvtpk(hv, hv);
      const v2u a = {pp & mk0, pp & mk1};
      c0 = mfma16(a, wd[i][0], wd[i][1], c0);
      c1 = mfma16(a, wd[i][2], wd[i][3], c1);
    }
    publish512<512 / ND>(rws, WS_PD, NPF, pf, c0, c1, L + L_TR, w, lane, tag);
  }
  STAMP_AT(10);
  if (b >= NWG - ND) {
    constexpr int C = 512 / ND, S = C / 2;
    const int j = b - (NWG - ND);
    const unsigned hp = ((LAS unsigned *)L)[L_Q + j * S + lane % S];
    WG_BARRIER();
    const f32x2 r = reduce_cols<C, NPF>(rws, WS_PD, j, tag, L + L_RED, tid, w, lane);
    const unsigned op = cvtpk(lo16(hp) + rbf(r[0]), hi16(hp) + rbf(r[1]));
    if (w == 0 && lane < S) ((unsigned *)p.out)[j * S + lane] = op;
    if (j == 0 && tid == 0) __builtin_amdgcn_raw_buffer_store_b32(tag, rws, WS_EPOCH, 0, AUX_SC1);
  }
  STAMP_AT(11);
  STAMP_FLUSH();
}
}

extern "C" void kernel_launch(void *const *d_in, const int *in_sizes, int n_in, void *d_out, int out_size, void *d_ws, size_t ws_size,
                              hipStream_t stream) {
  Params p;
  p.x = d_in[0]; p.k = d_in[1]; p.v = d_in[2]; p.wq = d_in[3]; p.wo = d_in[4];
  p.wg = d_in[6]; p.wu = d_in[7]; p.wd = d_in[8];
  p.out = d_out; p.ws = d_ws;
  (void)hipFuncSetAttribute((const void *)fused_decode_kernel, hipFuncAttributeMaxDynamicSharedMemorySize, LDS_BYTES);
  fused_decode_kernel<<<NWG, NTHR, LDS_BYTES, stream>>>(p);
}
```

```cpp
#include <hip/hip_runtime.h>
#include <stdint.h>

#define STAMP 0
#define NB1 64
#define ND 32
#define REP 32
#define KAUX 2
#define VAUX 2
#define WAUX 0
#define KNTMASK 0xf0
#define VNTMASK 0xf0
#define QREP 4

namespace {
typedef unsigned v4u __attribute__((ext_vector_type(4)));
typedef unsigned v2u __attribute__((ext_vector_type(2)));
typedef float f32x4 __attribute__((ext_vector_type(4)));
typedef float f32x2 __attribute__((ext_vector_type(2)));
typedef short s16x4 __attribute__((ext_vector_type(4)));
typedef __bf16 bf16x8 __attribute__((ext_vector_type(8)));
typedef __bf16 bf16x2 __attribute__((ext_vector_type(2)));
typedef __amdgpu_buffer_rsrc_t rsrc_t;
#define LAS __attribute__((address_space(3)))

constexpr int DM = 512, SK = 65536, DH = 128, FF = 1792;
constexpr int NWG = 256, NTHR = 256;
constexpr int NPF = 224;
constexpr int AUX_NT = 2, AUX_SC1 = 16;
constexpr int RS3 = 0x00020000;
constexpr int WS_EPOCH = 0;
constexpr int WS_QG = 4096;
constexpr int WS_ML = WS_QG + 8 * 16384;
constexpr int WS_HG = WS_ML + 131072;
constexpr int WS_OP = 1 << 20;
constexpr int WS_PB = 2 << 20;
constexpr int WS_PD = 3 << 20;
constexpr int WS_USED = 4 << 20;
constexpr int WS_STAMP = 8 << 20;
constexpr unsigned SPIN_CAP = 1u << 14;
constexpr float LOG2E = 1.4426950408889634f;
constexpr float SCALE = 0.08838834764831845f;

struct Params {
  const void *x, *k, *v, *wq, *wo, *wg, *wu, *wd;
  void *out, *ws;
};

__device__ __forceinline__ float asf(unsigned u) { return __builtin_bit_cast(float, u); }
__device__ __forceinline__ unsigned asu(float f) { return __builtin_bit_cast(unsigned, f); }
template <int C> __device__ __forceinline__ float dpp(float x) {
  return __builtin_bit_cast(float, __builtin_amdgcn_mov_dpp(__builtin_bit_cast(int, x), C, 0xf, 0xf, true));
}
constexpr int XOR1 = 0xB1, XOR2 = 0x4E, HMIR = 0x141, MIR = 0x140, ROR4 = 0x124, ROR8 = 0x128;
__device__ __forceinline__ unsigned cvtpk(float lo, float hi) {
  f32x2 v = {lo, hi};
  bf16x2 r = __builtin_convertvector(v, bf16x2);
  return __builtin_bit_cast(unsigned, r);
}
__device__ __forceinline__ float lo16(unsigned u) { return asf(u << 16); }
__device__ __forceinline__ float hi16(unsigned u) { return asf(u & 0xffff0000u); }
__device__ __forceinline__ float rbf(float x) { return lo16(cvtpk(x, x)); }
__device__ __forceinline__ float xr16_max(float x) {
  auto s = __builtin_amdgcn_permlane16_swap(asu(x), asu(x), false, false);
  x = fmaxf(asf(s[0]), asf(s[1]));
  auto t = __builtin_amdgcn_permlane32_swap(asu(x), asu(x), false, false);
  return fmaxf(asf(t[0]), asf(t[1]));
}
__device__ __forceinline__ float xr16_sum(float x) {
  auto s = __builtin_amdgcn_permlane16_swap(asu(x), asu(x), false, false);
  x = asf(s[0]) + asf(s[1]);
  auto t = __builtin_amdgcn_permlane32_swap(asu(x), asu(x), false, false);
  return asf(t[0]) + asf(t[1]);
}
__device__ __forceinline__ float row_sum(float x) {
  x += dpp<XOR1>(x); x += dpp<XOR2>(x); x += dpp<HMIR>(x); x += dpp<MIR>(x); return x;
}
__device__ __forceinline__ float row_max(float x) {
  x = fmaxf(x, dpp<XOR1>(x)); x = fmaxf(x, dpp<XOR2>(x)); x = fmaxf(x, dpp<HMIR>(x)); x = fmaxf(x, dpp<MIR>(x)); return x;
}
__device__ __forceinline__ float wave_sum(float x) { return xr16_sum(row_sum(x)); }
__device__ __forceinline__ f32x4 mfma32(v4u a, v4u b, f32x4 c) {
  return __builtin_amdgcn_mfma_f32_16x16x32_bf16(__builtin_bit_cast(bf16x8, a), __builtin_bit_cast(bf16x8, b), c, 0, 0, 0);
}
__device__ __forceinline__ f32x4 mfma16(v2u a, unsigned b0, unsigned b1, f32x4 c) {
  v2u b = {b0, b1};
  return __builtin_amdgcn_mfma_f32_16x16x16bf16_1k(__builtin_bit_cast(s16x4, a), __builtin_bit_cast(s16x4, b), c, 0, 0, 0);
}
template <int AUX> __device__ __forceinline__ v4u ld16(rsrc_t r, int voff, int soff) {
  return __builtin_amdgcn_raw_buffer_load_b128(r, voff, soff, AUX);
}
__device__ __forceinline__ void st16(rsrc_t r, int voff, v4u v) { __builtin_amdgcn_raw_buffer_store_b128(v, r, voff, 0, AUX_SC1); }
__device__ __forceinline__ void st8(rsrc_t r, int voff, v2u v) { __builtin_amdgcn_raw_buffer_store_b64(v, r, voff, 0, AUX_SC1); }
#define WG_BARRIER() asm volatile("s_waitcnt lgkmcnt(0)\n\ts_barrier" ::: "memory")
#define LDS_WAIT() asm volatile("s_waitcnt lgkmcnt(0)" ::: "memory")
#define SB() __builtin_amdgcn_sched_barrier(0)

__device__ __forceinline__ v4u layernorm8(v4u p) {
  float x[8];
#pragma unroll
  for (int i = 0; i < 4; ++i) { x[2 * i] = lo16(p[i]); x[2 * i + 1] = hi16(p[i]); }
  float s = ((x[0] + x[1]) + (x[2] + x[3])) + ((x[4] + x[5]) + (x[6] + x[7]));
  const float mean = wave_sum(s) * (1.f / DM);
  float q = 0.f;
#pragma unroll
  for (int i = 0; i < 8; ++i) { const float d = x[i] - mean; q = fmaf(d, d, q); }
  const float var = wave_sum(q) * (1.f / DM);
  const float mb = rbf(mean);
  const float den = rbf(sqrtf(rbf(rbf(var) + 1e-5f)));
  v4u r;
#pragma unroll
  for (int i = 0; i < 4; ++i) r[i] = cvtpk(rbf(x[2 * i] - mb) / den, rbf(x[2 * i + 1] - mb) / den);
  return r;
}
struct LnStat { float mb, rden; float den; };
__device__ __forceinline__ LnStat ln_stats(v4u p) {
  float x[8];
#pragma unroll
  for (int i = 0; i < 4; ++i) { x[2 * i] = lo16(p[i]); x[2 * i + 1] = hi16(p[i]); }
  float s = ((x[0] + x[1]) + (x[2] + x[3])) + ((x[4] + x[5]) + (x[6] + x[7]));
  const float mean = wave_sum(s) * (1.f / DM);
  float q = 0.f;
#pragma unroll
  for (int i = 0; i < 8; ++i) { const float d = x[i] - mean; q = fmaf(d, d, q); }
  const float var = wave_sum(q) * (1.f / DM);
  LnStat r; r.mb = rbf(mean); r.den = rbf(sqrtf(rbf(rbf(var) + 1e-5f))); r.rden = 0.f;
  return r;
}
__device__ __forceinline__ unsigned ln_pair(unsigned p, LnStat st) { return cvtpk(rbf(lo16(p) - st.mb) / st.den, rbf(hi16(p) - st.mb) / st.den); }
__device__ __forceinline__ unsigned pick4(v4u v, int w) {
  const unsigned a = (w & 1) ? v[1] : v[0], b = (w & 1) ? v[3] : v[2];
  return (w & 2) ? b : a;
}
__device__ __forceinline__ float gemv16(unsigned xp, v4u a0, v4u a1, v4u b0, v4u b1, int lane) {
  float v[16];
  const bf16x2 xb = __builtin_bit_cast(bf16x2, xp);
#pragma unroll
  for (int i = 0; i < 4; ++i) {
    const unsigned alo = __builtin_amdgcn_perm(a1[i], a0[i], 0x05040100u), ahi = __builtin_amdgcn_perm(a1[i], a0[i], 0x07060302u);
    const unsigned blo = __builtin_amdgcn_perm(b1[i], b0[i], 0x05040100u), bhi = __builtin_amdgcn_perm(b1[i], b0[i], 0x07060302u);
    v[2 * i] = __builtin_amdgcn_fdot2_f32_bf16(__builtin_bit_cast(bf16x2, alo), xb, 0.f, false);
    v[2 * i + 1] = __builtin_amdgcn_fdot2_f32_bf16(__builtin_bit_cast(bf16x2, ahi), xb, 0.f, false);
    v[8 + 2 * i] = __builtin_amdgcn_fdot2_f32_bf16(__builtin_bit_cast(bf16x2, blo), xb, 0.f, false);
    v[8 + 2 * i + 1] = __builtin_amdgcn_fdot2_f32_bf16(__builtin_bit_cast(bf16x2, bhi), xb, 0.f, false);
  }
  float u[8];
#pragma unroll
  for (int k = 0; k < 8; ++k) { auto s = __builtin_amdgcn_permlane32_swap(asu(v[k]), asu(v[k + 8]), false, false); u[k] = asf(s[0]) + asf(s[1]); }
  float t[4];
#pragma unroll
  for (int k = 0; k < 4; ++k) { auto s = __builtin_amdgcn_permlane16_swap(asu(u[k]), asu(u[k + 4]), false, false); t[k] = asf(s[0]) + asf(s[1]); }
  float y[2];
#pragma unroll
  for (int k = 0; k < 2; ++k) { const float p = t[k] + dpp<ROR8>(t[k]), q = t[k + 2] + dpp<ROR8>(t[k + 2]); y[k] = (lane & 8) ? q : p; }
#pragma unroll
  for (int k = 0; k < 2; ++k) { y[k] += dpp<XOR1>(y[k]); y[k] += dpp<XOR2>(y[k]); y[k] += dpp<HMIR>(y[k]); }
  return (lane & 4) ? y[1] : y[0];
}

struct Tile { v4u k[8], v[8]; };

template <bool REFILL, int KA, int VA>
__device__ __forceinline__ void tile_step(Tile &t, v4u q, rsrc_t rk, rsrc_t rv, int koff, int voff, int snext, unsigned mk0, unsigned mk1,
                                          float &m2, float &lsum, f32x4 &acc0, f32x4 &acc1) {
  float s[8];
  const f32x4 z = {0.f, 0.f, 0.f, 0.f};
  f32x4 d[8];
#pragma unroll
  for (int g = 0; g < 8; ++g) {
    d[g] = mfma32(t.k[g], q, z);
    if (REFILL) t.k[g] = ld16<KA>(rk, koff, snext + g * 1024);
    SB();
  }
#pragma unroll
  for (int g = 0; g < 8; ++g) {
    s[g] = ((dpp<0x00>(d[g][0]) + dpp<0x55>(d[g][1])) + dpp<0xAA>(d[g][2])) + dpp<0xFF>(d[g][3]);
    asm("" : "+v"(s[g]));
  }
#pragma unroll
  for (int g = 0; g < 8; g += 2) {
    const unsigned a = cvtpk(s[g], s[g + 1]);
    const unsigned b = cvtpk(lo16(a) * SCALE, hi16(a) * SCALE);
    s[g] = lo16(b); s[g + 1] = hi16(b);
  }
  float mx = fmaxf(fmaxf(fmaxf(s[0], s[1]), fmaxf(s[2], s[3])), fmaxf(fmaxf(s[4], s[5]), fmaxf(s[6], s[7])));
  mx = xr16_max(mx);
  const float mn = fmaxf(m2, mx * LOG2E);
  const float alpha = __builtin_amdgcn_exp2f(m2 - mn);
  m2 = mn;
  lsum *= alpha;
  int ai = __builtin_bit_cast(int, alpha), ar;
  ar = __builtin_amdgcn_update_dpp(ai, ai, 0x150, 0x1, 0xf, false);
  ar = __builtin_amdgcn_update_dpp(ar, ai, 0x154, 0x2, 0xf, false);
  ar = __builtin_amdgcn_update_dpp(ar, ai, 0x158, 0x4, 0xf, false);
  ar = __builtin_amdgcn_update_dpp(ar, ai, 0x15c, 0x8, 0xf, false);
  const float al = __builtin_bit_cast(float, ar);
  acc0 *= al; acc1 *= al;
#pragma unroll
  for (int g = 0; g < 8; ++g) {
    const float p = __builtin_amdgcn_exp2f(fmaf(s[g], LOG2E, -mn));
    lsum += p;
    const unsigned pp = cvtpk(p, p);
    const v2u a = {pp & mk0, pp & mk1};
    acc0 = mfma16(a, t.v[g][0], t.v[g][1], acc0);
    acc1 = mfma16(a, t.v[g][2], t.v[g][3], acc1);
    if (REFILL) t.v[g] = ld16<VA>(rv, voff, snext + g * 1024);
    SB();
  }
}

template <int N>
__device__ __forceinline__ void gather(rsrc_t rs, const int (&off)[N], unsigned tag, v4u (&x)[N]) {
  for (unsigned spins = 0;; ++spins) {
    bool ok = true;
#pragma unroll
    for (int i = 0; i < N; ++i) x[i] = ld16<AUX_SC1>(rs, off[i], 0);
#pragma unroll
    for (int i = 0; i < N; ++i) ok = ok && x[i][1] == tag && x[i][3] == tag;
    if (__all(ok) || spins > SPIN_CAP) break;
    __builtin_amdgcn_s_sleep(1);
  }
}
template <int S> __device__ __forceinline__ float strided_sum(float x) {
  if (S <= 1) x += dpp<XOR1>(x);
  if (S <= 2) x += dpp<XOR2>(x);
  if (S <= 8) x += dpp<ROR8>(x);
  if (S <= 4) x += dpp<ROR4>(x);
  return xr16_sum(x);
}
template <int C>
__device__ __forceinline__ void publish512(rsrc_t rs, int base, int nprod, int p, f32x4 a0, f32x4 a1, LAS float *tr, int w, int lane, unsigned tag) {
  if (lane < 16) {
    *(LAS f32x4 *)(tr + w * 128 + 8 * lane) = a0;
    *(LAS f32x4 *)(tr + w * 128 + 8 * lane + 4) = a1;
  }
  LDS_WAIT();
  const f32x2 y = *(LAS f32x2 *)(tr + w * 128 + 2 * lane);
  const int col = 128 * w + 2 * lane, j = col / C, within = col % C;
  const v4u g = {asu(y[0]), tag, asu(y[1]), tag};
  st16(rs, base + ((j * nprod + p) * C + within) * 8, g);
}
template <int C, int NP>
__device__ __forceinline__ f32x2 reduce_cols(rsrc_t rs, int base, int j, unsigned tag, LAS float *red, int tid, int w, int lane) {
  constexpr int S = C / 2, NPIECE = NP * S, N = (NPIECE + NTHR - 1) / NTHR;
  int off[N]; v4u x[N];
#pragma unroll
  for (int i = 0; i < N; ++i) { const int pc = i * NTHR + tid; off[i] = base + j * (NP * C * 8) + (pc < NPIECE ? pc : tid) * 16; }
  gather<N>(rs, off, tag, x);
  float s0 = 0.f, s1 = 0.f;
#pragma unroll
  for (int i = 0; i < N; ++i) { const bool live = i * NTHR + tid < NPIECE; s0 += live ? asf(x[i][0]) : 0.f; s1 += live ? asf(x[i][2]) : 0.f; }
  s0 = strided_sum<S>(s0); s1 = strided_sum<S>(s1);
  if (lane < S) { f32x2 y = {s0, s1}; *(LAS f32x2 *)(red + w * 64 + 2 * lane) = y; }
  WG_BARRIER();
  f32x2 r = {0.f, 0.f};
#pragma unroll
  for (int ww = 0; ww < 4; ++ww) r += *(LAS f32x2 *)(red + ww * 64 + 2 * (lane % S));
  return r;
}

#define STAMP_AT(i) do { } while (0)
#define STAMP_FLUSH() do { } while (0)

constexpr int L_MRG = 0;
constexpr int L_ML = L_MRG + 2048;
constexpr int L_RED = L_ML + 32;
constexpr int L_RED2 = L_RED + 256;
constexpr int L_TR = L_RED2 + 256;
constexpr int L_Q = L_TR + 512;
constexpr int L_XN = L_Q + 256;
constexpr int L_END = L_XN + 256;
constexpr int LDS_BYTES = 84 * 1024;

template <int KA, int VA>
__device__ __forceinline__ void body(const Params &p, LAS float *L) {
  const int tid = threadIdx.x, lane = tid & 63, w = __builtin_amdgcn_readfirstlane(tid >> 6), b = blockIdx.x;
  STAMP_AT(0);
  const int kh = b >> 5, sp = b & 31;
  const rsrc_t rws = __builtin_amdgcn_make_buffer_rsrc(p.ws, 0, WS_USED, RS3);
  const rsrc_t rk = __builtin_amdgcn_make_buffer_rsrc((char *)p.k + ((size_t)kh * SK + sp * 2048) * 256, 0, 2048 * 256, RS3);
  const rsrc_t rv = __builtin_amdgcn_make_buffer_rsrc((char *)p.v + ((size_t)kh * SK + sp * 2048) * 256, 0, 2048 * 256, RS3);
  const rsrc_t rx = __builtin_amdgcn_make_buffer_rsrc((void *)p.x, 0, DM * 2, RS3);
  const rsrc_t rwq = __builtin_amdgcn_make_buffer_rsrc((void *)p.wq, 0, DM * 4096 * 2, RS3);
  const unsigned epoch = *(const __attribute__((address_space(4))) unsigned *)((const char *)p.ws + WS_EPOCH);
  const v4u xr = ld16<0>(rx, lane * 16, 0);
  const int qf = (b & 7) * 32 + (b >> 3);
  const unsigned xraw = __builtin_amdgcn_raw_buffer_load_b32(rx, (64 * w + lane) * 4, 0, 0);
  const int wqoff = (128 * w + 2 * lane) * 8192 + qf * 32;
  const v4u wa0 = ld16<WAUX>(rwq, wqoff, 0), wa1 = ld16<WAUX>(rwq, wqoff, 8192);
  const v4u wb0 = ld16<WAUX>(rwq, wqoff, 16), wb1 = ld16<WAUX>(rwq, wqoff, 8192 + 16);
  SB();
  asm volatile("s_barrier" ::: "memory");
  SB();
  const int g4 = lane >> 4, a4 = (lane >> 2) & 3, u4 = lane & 3;
  const int koff = a4 * 256 + (4 * g4 + u4) * 16, voff = lane * 16;
  Tile ta, tb;
#pragma unroll
  for (int g = 0; g < 8; ++g) ta.k[g] = ld16<KA>(rk, koff, w * 8192 + g * 1024);
#pragma unroll
  for (int g = 0; g < 8; ++g) ta.v[g] = ld16<VA>(rv, voff, w * 8192 + g * 1024);
  SB();
  const unsigned tag = epoch + 1u;
  {
    unsigned xsel = ln_pair(xraw, ln_stats(xr));
    const float r = gemv16(xsel, wa0, wa1, wb0, wb1, lane);
    if ((lane & 3) == 0) L[L_RED + w * 64 + (lane >> 2)] = r;
    WG_BARRIER();
    if (w == 0) {
      const int i = lane & 7;
      float s0 = 0.f, s1 = 0.f;
#pragma unroll
      for (int ww = 0; ww < 4; ++ww) { const f32x2 y = *(LAS f32x2 *)(L + L_RED + ww * 64 + 2 * i); s0 += y[0]; s1 += y[1]; }
      const v2u g = {cvtpk(s0, s1), tag};
      if (lane < 8 * QREP) st8(rws, WS_QG + (lane >> 3) * 16384 + ((b & 7) * 256 + (b >> 3) * 8 + i) * 8, g);
    }
  }
  STAMP_AT(1);
  asm volatile("s_waitcnt vmcnt(9)" ::: "memory");
  v4u q;
  {
    const int qoff = WS_QG + (b & (QREP - 1)) * 16384 + (kh * 256 + w * 64) * 8 + (lane & 31) * 16;
    v4u x0 = ld16<AUX_SC1>(rws, qoff, 0);
    SB();
#pragma unroll
    for (int g = 0; g < 8; ++g) tb.k[g] = ld16<KA>(rk, koff, (4 + w) * 8192 + g * 1024);
#pragma unroll
    for (int g = 0; g < 8; ++g) tb.v[g] = ld16<VA>(rv, voff, (4 + w) * 8192 + g * 1024);
    SB();
    bool ok = x0[1] == tag && x0[3] == tag;
    for (unsigned spins = 0; !__all(ok) && spins < SPIN_CAP; ++spins) {
      x0 = ld16<AUX_SC1>(rws, qoff, 0);
      ok = x0[1] == tag && x0[3] == tag;
    }
    if (lane < 32) *(LAS v2u *)(L + L_Q + w * 64 + 2 * lane) = v2u{x0[0], x0[2]};
    WG_BARRIER();
    q = *(LAS v4u *)(L + L_Q + a4 * 64 + (4 * g4 + u4) * 4);
  }
  STAMP_AT(2);
  const int pf = (b & 7) * 28 + (b >> 3);
  const bool hasf = (b >> 3) < 28;
  const rsrc_t rwg = __builtin_amdgcn_make_buffer_rsrc((void *)p.wg, 0, hasf ? DM * FF * 2 : 0, RS3);
  const rsrc_t rwu = __builtin_amdgcn_make_buffer_rsrc((void *)p.wu, 0, hasf ? DM * FF * 2 : 0, RS3);
  const int go = (128 * w + 2 * lane) * (FF * 2) + pf * 16;
  const v4u wg0 = ld16<WAUX>(rwg, go, 0), wg1 = ld16<WAUX>(rwg, go, FF * 2);
  const v4u wu0 = ld16<WAUX>(rwu, go, 0), wu1 = ld16<WAUX>(rwu, go, FF * 2);
  SB();
  const unsigned mk0 = u4 == 0 ? 0xFFFFu : u4 == 1 ? 0xFFFF0000u : 0u;
  const unsigned mk1 = u4 == 2 ? 0xFFFFu : u4 == 3 ? 0xFFFF0000u : 0u;
  float m2 = -3.0e38f, lsum = 0.f;
  f32x4 acc0 = {0.f, 0.f, 0.f, 0.f}, acc1 = acc0;
  for (int it = 0; it < 7; ++it) {
    const int s0 = ((2 * it + 2) * 4 + w) * 8192;
    tile_step<true, KA, VA>(ta, q, rk, rv, koff, voff, s0, mk0, mk1, m2, lsum, acc0, acc1);
    tile_step<true, KA, VA>(tb, q, rk, rv, koff, voff, s0 + 4 * 8192, mk0, mk1, m2, lsum, acc0, acc1);
  }
  const rsrc_t rwo = __builtin_amdgcn_make_buffer_rsrc((char *)p.wo + (size_t)b * 16384, 0, 16384, RS3);
  const rsrc_t rwd = __builtin_amdgcn_make_buffer_rsrc((char *)p.wd + (size_t)(hasf ? pf : 0) * 8192, 0, hasf ? 8192 : 0, RS3);
  v4u wo[4], wd[2];
  {
    const int o = g4 * 1024 + w * 256 + (lane & 15) * 16;
#pragma unroll
    for (int i = 0; i < 4; ++i) wo[i] = ld16<WAUX>(rwo, o, i * 4096);
#pragma unroll
    for (int i = 0; i < 2; ++i) wd[i] = ld16<WAUX>(rwd, o, i * 4096);
    SB();
  }
  tile_step<false, KA, VA>(ta, q, rk, rv, koff, voff, 0, mk0, mk1, m2, lsum, acc0, acc1);
  tile_step<false, KA, VA>(tb, q, rk, rv, koff, voff, 0, mk0, mk1, m2, lsum, acc0, acc1);
  STAMP_AT(3);
  {
    const float lw = xr16_sum(lsum);
    *(LAS f32x4 *)(L + L_MRG + w * 512 + g4 * 128 + 8 * (lane & 15)) = acc0;
    *(LAS f32x4 *)(L + L_MRG + w * 512 + g4 * 128 + 8 * (lane & 15) + 4) = acc1;
    if (lane < 16 && u4 == 0) { f32x2 y = {m2, lw}; *(LAS f32x2 *)(L + L_ML + (w * 4 + a4) * 2) = y; }
    WG_BARRIER();
    f32x2 ml[4];
#pragma unroll
    for (int ww = 0; ww < 4; ++ww) ml[ww] = *(LAS f32x2 *)(L + L_ML + (ww * 4 + w) * 2);
    const float M = fmaxf(fmaxf(ml[0][0], ml[1][0]), fmaxf(ml[2][0], ml[3][0]));
    float Ls = 0.f; f32x2 o = {0.f, 0.f};
#pragma unroll
    for (int ww = 0; ww < 4; ++ww) {
      const float e = __builtin_amdgcn_exp2f(ml[ww][0] - M);
      Ls = fmaf(e, ml[ww][1], Ls);
      o += e * *(LAS f32x2 *)(L + L_MRG + ww * 512 + w * 128 + 2 * lane);
    }
    const int head = 4 * kh + w;
    st16(rws, WS_OP + (((head * 8 + (lane >> 3)) * 32 + sp) * 16 + 2 * (lane & 7)) * 8, v4u{asu(o[0]), tag, asu(o[1]), tag});
    if (lane < 8) st16(rws, WS_ML + ((head * 8 + lane) * 32 + sp) * 16, v4u{asu(M), tag, asu(Ls), tag});
  }
  STAMP_AT(4);
  {
    const int off[2] = {WS_OP + b * 4096 + tid * 16, WS_ML + (b * 32 + 8 * w + (lane >> 3)) * 16};
    v4u x[2];
    gather<2>(rws, off, tag, x);
    STAMP_AT(5);
    const float ms = asf(x[1][0]), ls = asf(x[1][2]);
    const float Mw = xr16_max(fmaxf(ms, dpp<ROR8>(ms)));
    const float e = __builtin_amdgcn_exp2f(ms - Mw);
    const float v0 = strided_sum<8>(e * asf(x[0][0])), v1 = strided_sum<8>(e * asf(x[0][2])), lw = strided_sum<8>(e * ls);
    if (lane < 8) {
      const int d = 2 * lane;
      L[L_RED2 + w * 64 + (d & 3) * 4 + (d >> 2)] = v0;
      L[L_RED2 + w * 64 + ((d + 1) & 3) * 4 + (d >> 2)] = v1;
      if (lane == 0) *(LAS f32x2 *)(L + L_RED2 + w * 64 + 32) = f32x2{Mw, lw};
    }
    WG_BARRIER();
    f32x2 ml[4];
#pragma unroll
    for (int ww = 0; ww < 4; ++ww) ml[ww] = *(LAS f32x2 *)(L + L_RED2 + ww * 64 + 32);
    const float M = fmaxf(fmaxf(ml[0][0], ml[1][0]), fmaxf(ml[2][0], ml[3][0]));
    f32x4 o = {0.f, 0.f, 0.f, 0.f};
    float Lt = 0.f;
#pragma unroll
    for (int ww = 0; ww < 4; ++ww) {
      const float ew = __builtin_amdgcn_exp2f(ml[ww][0] - M);
      Lt = fmaf(ew, ml[ww][1], Lt);
      o += ew * *(LAS f32x4 *)(L + L_RED2 + ww * 64 + g4 * 4);
    }
    o *= __builtin_amdgcn_rcpf(Lt);
    f32x4 c0 = {0.f, 0.f, 0.f, 0.f}, c1 = c0;
#pragma unroll
    for (int i = 0; i < 4; ++i) {
      const unsigned pp = cvtpk(o[i], o[i]);
      const v2u a = {pp & mk0, pp & mk1};
      c0 = mfma16(a, wo[i][0], wo[i][1], c0);
      c1 = mfma16(a, wo[i][2], wo[i][3], c1);
    }
    publish512<512 / NB1>(rws, WS_PB, 256, b, c0, c1, L + L_TR, w, lane, tag);
  }
  STAMP_AT(6);
  if (b < NB1) {
    constexpr int C = 512 / NB1, S = C / 2;
    const unsigned xp = __builtin_amdgcn_raw_buffer_load_b32(rx, (b * C + 2 * (lane % S)) * 2, 0, 0);
    const f32x2 r = reduce_cols<C, 256>(rws, WS_PB, b, tag, L + L_RED, tid, w, lane);
    STAMP_AT(7);
    const unsigned hp = cvtpk(lo16(xp) + rbf(r[0]), hi16(xp) + rbf(r[1]));
    if (tid < S * REP) st8(rws, WS_HG + (tid / S) * 2048 + (b * S + tid % S) * 8, v2u{hp, tag});
  }
  STAMP_AT(8);
  if (w == 0) {
    const int base = WS_HG + (b % REP) * 2048 + lane * 32;
    const int off[2] = {base, base + 16};
    v4u x[2];
    gather<2>(rws, off, tag, x);
    const v4u hb = v4u{x[0][0], x[0][2], x[1][0], x[1][2]};
    *(LAS v4u *)(L + L_Q + 4 * lane) = hb;
    *(LAS v4u *)(L + L_XN + 4 * lane) = layernorm8(hb);
  }
  WG_BARRIER();
  STAMP_AT(9);
  if (hasf) {
    const unsigned xsel = ((LAS unsigned *)L)[L_XN + 64 * w + lane];
    const float r = gemv16(xsel, wg0, wg1, wu0, wu1, lane);
    if ((lane & 3) == 0) { const int vi = lane >> 2, col = vi & 7; L[L_RED2 + w * 64 + 16 + (col & 3) * 4 + (vi >> 3) * 2 + (col >> 2)] = r; }
    WG_BARRIER();
    f32x4 gu = {0.f, 0.f, 0.f, 0.f};
#pragma unroll
    for (int ww = 0; ww < 4; ++ww) gu += *(LAS f32x4 *)(L + L_RED2 + ww * 64 + 16 + g4 * 4);
    f32x4 c0 = {0.f, 0.f, 0.f, 0.f}, c1 = c0;
#pragma unroll
    for (int i = 0; i < 2; ++i) {
      const float hv = rbf(gu[i]) * rbf(gu[2 + i]);
      const unsigned pp = cvtpk(hv, hv);
      const v2u a = {pp & mk0, pp & mk1};
      c0 = mfma16(a, wd[i][0], wd[i][1], c0);
      c1 = mfma16(a, wd[i][2], wd[i][3], c1);
    }
    publish512<512 / ND>(rws, WS_PD, NPF, pf, c0, c1, L + L_TR, w, lane, tag);
  }
  STAMP_AT(10);
  if (b >= NWG - ND) {
    constexpr int C = 512 / ND, S = C / 2;
    const int j = b - (NWG - ND);
    const unsigned hp = ((LAS unsigned *)L)[L_Q + j * S + lane % S];
    WG_BARRIER();
    const f32x2 r = reduce_cols<C, NPF>(rws, WS_PD, j, tag, L + L_RED, tid, w, lane);
    const unsigned op = cvtpk(lo16(hp) + rbf(r[0]), hi16(hp) + rbf(r[1]));
    if (w == 0 && lane < S) ((unsigned *)p.out)[j * S + lane] = op;
    if (j == 0 && tid == 0) __builtin_amdgcn_raw_buffer_store_b32(tag, rws, WS_EPOCH, 0, AUX_SC1);
  }
  STAMP_AT(11);
  STAMP_FLUSH();
}
__global__ void __launch_bounds__(NTHR, 2) fused_decode_kernel(Params p) {
  extern __shared__ __attribute__((aligned(16))) float smem[];
  LAS float *L = (LAS float *)smem;
  const int kh = blockIdx.x >> 5;
  const bool knt = (KNTMASK >> kh) & 1, vnt = (VNTMASK >> kh) & 1;
  if (knt) { if (vnt) body<2, 2>(p, L); else body<2, 0>(p, L); }
  else { if (vnt) body<0, 2>(p, L); else body<0, 0>(p, L); }
}
}

extern "C" void kernel_launch(void *const *d_in, const int *in_sizes, int n_in, void *d_out, int out_size, void *d_ws, size_t ws_size,
                              hipStream_t stream) {
  Params p;
  p.x = d_in[0]; p.k = d_in[1]; p.v = d_in[2]; p.wq = d_in[3]; p.wo = d_in[4];
  p.wg = d_in[6]; p.wu = d_in[7]; p.wd = d_in[8];
  p.out = d_out; p.ws = d_ws;
  (void)hipFuncSetAttribute((const void *)fused_decode_kernel, hipFuncAttributeMaxDynamicSharedMemorySize, LDS_BYTES);
  fused_decode_kernel<<<NWG, NTHR, LDS_BYTES, stream>>>(p);
}
```

```cpp
#include <hip/hip_runtime.h>
#include <stdint.h>

#define STAMP 0
#define NB1 64
#define ND 32
#define REP 32
#define KAUX 2
#define VAUX 2
#define WAUX 2
#define TOUCH 65536
#define QREP 4

namespace {
typedef unsigned v4u __attribute__((ext_vector_type(4)));
typedef unsigned v2u __attribute__((ext_vector_type(2)));
typedef float f32x4 __attribute__((ext_vector_type(4)));
typedef float f32x2 __attribute__((ext_vector_type(2)));
typedef short s16x4 __attribute__((ext_vector_type(4)));
typedef __bf16 bf16x8 __attribute__((ext_vector_type(8)));
typedef __bf16 bf16x2 __attribute__((ext_vector_type(2)));
typedef __amdgpu_buffer_rsrc_t rsrc_t;
#define LAS __attribute__((address_space(3)))

constexpr int DM = 512, SK = 65536, DH = 128, FF = 1792;
constexpr int NWG = 256, NTHR = 256;
constexpr int NPF = 224;
constexpr int AUX_NT = 2, AUX_SC1 = 16;
constexpr int RS3 = 0x00020000;
constexpr int WS_EPOCH = 0;
constexpr int WS_QG = 4096;
constexpr int WS_ML = WS_QG + 8 * 16384;
constexpr int WS_HG = WS_ML + 131072;
constexpr int WS_OP = 1 << 20;
constexpr int WS_PB = 2 << 20;
constexpr int WS_PD = 3 << 20;
constexpr int WS_USED = 4 << 20;
constexpr int WS_STAMP = 8 << 20;
constexpr unsigned SPIN_CAP = 1u << 14;
constexpr float LOG2E = 1.4426950408889634f;
constexpr float SCALE = 0.08838834764831845f;

struct Params {
  const void *x, *k, *v, *wq, *wo, *wg, *wu, *wd;
  void *out, *ws;
};

__device__ __forceinline__ float asf(unsigned u) { return __builtin_bit_cast(float, u); }
__device__ __forceinline__ unsigned asu(float f) { return __builtin_bit_cast(unsigned, f); }
template <int C> __device__ __forceinline__ float dpp(float x) {
  return __builtin_bit_cast(float, __builtin_amdgcn_mov_dpp(__builtin_bit_cast(int, x), C, 0xf, 0xf, true));
}
constexpr int XOR1 = 0xB1, XOR2 = 0x4E, HMIR = 0x141, MIR = 0x140, ROR4 = 0x124, ROR8 = 0x128;
__device__ __forceinline__ unsigned cvtpk(float lo, float hi) {
  f32x2 v = {lo, hi};
  bf16x2 r = __builtin_convertvector(v, bf16x2);
  return __builtin_bit_cast(unsigned, r);
}
__device__ __forceinline__ float lo16(unsigned u) { return asf(u << 16); }
__device__ __forceinline__ float hi16(unsigned u) { return asf(u & 0xffff0000u); }
__device__ __forceinline__ float rbf(float x) { return lo16(cvtpk(x, x)); }
__device__ __forceinline__ float xr16_max(float x) {
  auto s = __builtin_amdgcn_permlane16_swap(asu(x), asu(x), false, false);
  x = fmaxf(asf(s[0]), asf(s[1]));
  auto t = __builtin_amdgcn_permlane32_swap(asu(x), asu(x), false, false);
  return fmaxf(asf(t[0]), asf(t[1]));
}
__device__ __forceinline__ float xr16_sum(float x) {
  auto s = __builtin_amdgcn_permlane16_swap(asu(x), asu(x), false, false);
  x = asf(s[0]) + asf(s[1]);
  auto t = __builtin_amdgcn_permlane32_swap(asu(x), asu(x), false, false);
  return asf(t[0]) + asf(t[1]);
}
__device__ __forceinline__ float row_sum(float x) {
  x += dpp<XOR1>(x); x += dpp<XOR2>(x); x += dpp<HMIR>(x); x += dpp<MIR>(x); return x;
}
__device__ __forceinline__ float row_max(float x) {
  x = fmaxf(x, dpp<XOR1>(x)); x = fmaxf(x, dpp<XOR2>(x)); x = fmaxf(x, dpp<HMIR>(x)); x = fmaxf(x, dpp<MIR>(x)); return x;
}
__device__ __forceinline__ float wave_sum(float x) { return xr16_sum(row_sum(x)); }
__device__ __forceinline__ f32x4 mfma32(v4u a, v4u b, f32x4 c) {
  return __builtin_amdgcn_mfma_f32_16x16x32_bf16(__builtin_bit_cast(bf16x8, a), __builtin_bit_cast(bf16x8, b), c, 0, 0, 0);
}
__device__ __forceinline__ f32x4 mfma16(v2u a, unsigned b0, unsigned b1, f32x4 c) {
  v2u b = {b0, b1};
  return __builtin_amdgcn_mfma_f32_16x16x16bf16_1k(__builtin_bit_cast(s16x4, a), __builtin_bit_cast(s16x4, b), c, 0, 0, 0);
}
template <int AUX> __device__ __forceinline__ v4u ld16(rsrc_t r, int voff, int soff) {
  return __builtin_amdgcn_raw_buffer_load_b128(r, voff, soff, AUX);
}
__device__ __forceinline__ void st16(rsrc_t r, int voff, v4u v) { __builtin_amdgcn_raw_buffer_store_b128(v, r, voff, 0, AUX_SC1); }
__device__ __forceinline__ void st8(rsrc_t r, int voff, v2u v) { __builtin_amdgcn_raw_buffer_store_b64(v, r, voff, 0, AUX_SC1); }
#define WG_BARRIER() asm volatile("s_waitcnt lgkmcnt(0)\n\ts_barrier" ::: "memory")
#define LDS_WAIT() asm volatile("s_waitcnt lgkmcnt(0)" ::: "memory")
#define SB() __builtin_amdgcn_sched_barrier(0)

__device__ __forceinline__ v4u layernorm8(v4u p) {
  float x[8];
#pragma unroll
  for (int i = 0; i < 4; ++i) { x[2 * i] = lo16(p[i]); x[2 * i + 1] = hi16(p[i]); }
  float s = ((x[0] + x[1]) + (x[2] + x[3])) + ((x[4] + x[5]) + (x[6] + x[7]));
  const float mean = wave_sum(s) * (1.f / DM);
  float q = 0.f;
#pragma unroll
  for (int i = 0; i < 8; ++i) { const float d = x[i] - mean; q = fmaf(d, d, q); }
  const float var = wave_sum(q) * (1.f / DM);
  const float mb = rbf(mean);
  const float den = rbf(sqrtf(rbf(rbf(var) + 1e-5f)));
  v4u r;
#pragma unroll
  for (int i = 0; i < 4; ++i) r[i] = cvtpk(rbf(x[2 * i] - mb) / den, rbf(x[2 * i + 1] - mb) / den);
  return r;
}
struct LnStat { float mb, rden; float den; };
__device__ __forceinline__ LnStat ln_stats(v4u p) {
  float x[8];
#pragma unroll
  for (int i = 0; i < 4; ++i) { x[2 * i] = lo16(p[i]); x[2 * i + 1] = hi16(p[i]); }
  float s = ((x[0] + x[1]) + (x[2] + x[3])) + ((x[4] + x[5]) + (x[6] + x[7]));
  const float mean = wave_sum(s) * (1.f / DM);
  float q = 0.f;
#pragma unroll
  for (int i = 0; i < 8; ++i) { const float d = x[i] - mean; q = fmaf(d, d, q); }
  const float var = wave_sum(q) * (1.f / DM);
  LnStat r; r.mb = rbf(mean); r.den = rbf(sqrtf(rbf(rbf(var) + 1e-5f))); r.rden = 0.f;
  return r;
}
__device__ __forceinline__ unsigned ln_pair(unsigned p, LnStat st) { return cvtpk(rbf(lo16(p) - st.mb) / st.den, rbf(hi16(p) - st.mb) / st.den); }
__device__ __forceinline__ unsigned pick4(v4u v, int w) {
  const unsigned a = (w & 1) ? v[1] : v[0], b = (w & 1) ? v[3] : v[2];
  return (w & 2) ? b : a;
}
__device__ __forceinline__ float gemv16(unsigned xp, v4u a0, v4u a1, v4u b0, v4u b1, int lane) {
  float v[16];
  const bf16x2 xb = __builtin_bit_cast(bf16x2, xp);
#pragma unroll
  for (int i = 0; i < 4; ++i) {
    const unsigned alo = __builtin_amdgcn_perm(a1[i], a0[i], 0x05040100u), ahi = __builtin_amdgcn_perm(a1[i], a0[i], 0x07060302u);
    const unsigned blo = __builtin_amdgcn_perm(b1[i], b0[i], 0x05040100u), bhi = __builtin_amdgcn_perm(b1[i], b0[i], 0x07060302u);
    v[2 * i] = __builtin_amdgcn_fdot2_f32_bf16(__builtin_bit_cast(bf16x2, alo), xb, 0.f, false);
    v[2 * i + 1] = __builtin_amdgcn_fdot2_f32_bf16(__builtin_bit_cast(bf16x2, ahi), xb, 0.f, false);
    v[8 + 2 * i] = __builtin_amdgcn_fdot2_f32_bf16(__builtin_bit_cast(bf16x2, blo), xb, 0.f, false);
    v[8 + 2 * i + 1] = __builtin_amdgcn_fdot2_f32_bf16(__builtin_bit_cast(bf16x2, bhi), xb, 0.f, false);
  }
  float u[8];
#pragma unroll
  for (int k = 0; k < 8; ++k) { auto s = __builtin_amdgcn_permlane32_swap(asu(v[k]), asu(v[k + 8]), false, false); u[k] = asf(s[0]) + asf(s[1]); }
  float t[4];
#pragma unroll
  for (int k = 0; k < 4; ++k) { auto s = __builtin_amdgcn_permlane16_swap(asu(u[k]), asu(u[k + 4]), false, false); t[k] = asf(s[0]) + asf(s[1]); }
  float y[2];
#pragma unroll
  for (int k = 0; k < 2; ++k) { const float p = t[k] + dpp<ROR8>(t[k]), q = t[k + 2] + dpp<ROR8>(t[k + 2]); y[k] = (lane & 8) ? q : p; }
#pragma unroll
  for (int k = 0; k < 2; ++k) { y[k] += dpp<XOR1>(y[k]); y[k] += dpp<XOR2>(y[k]); y[k] += dpp<HMIR>(y[k]); }
  return (lane & 4) ? y[1] : y[0];
}

struct Tile { v4u k[8], v[8]; };

template <bool REFILL>
__device__ __forceinline__ void tile_step(Tile &t, v4u q, rsrc_t rk, rsrc_t rv, int koff, int voff, int snext, unsigned mk0, unsigned mk1,
                                          float &m2, float &lsum, f32x4 &acc0, f32x4 &acc1) {
  float s[8];
  const f32x4 z = {0.f, 0.f, 0.f, 0.f};
  f32x4 d[8];
#pragma unroll
  for (int g = 0; g < 8; ++g) {
    d[g] = mfma32(t.k[g], q, z);
    if (REFILL) t.k[g] = ld16<KAUX>(rk, koff, snext + g * 1024);
    SB();
  }
#pragma unroll
  for (int g = 0; g < 8; ++g) {
    s[g] = ((dpp<0x00>(d[g][0]) + dpp<0x55>(d[g][1])) + dpp<0xAA>(d[g][2])) + dpp<0xFF>(d[g][3]);
    asm("" : "+v"(s[g]));
  }
#pragma unroll
  for (int g = 0; g < 8; g += 2) {
    const unsigned a = cvtpk(s[g], s[g + 1]);
    const unsigned b = cvtpk(lo16(a) * SCALE, hi16(a) * SCALE);
    s[g] = lo16(b); s[g + 1] = hi16(b);
  }
  float mx = fmaxf(fmaxf(fmaxf(s[0], s[1]), fmaxf(s[2], s[3])), fmaxf(fmaxf(s[4], s[5]), fmaxf(s[6], s[7])));
  mx = xr16_max(mx);
  const float mn = fmaxf(m2, mx * LOG2E);
  const float alpha = __builtin_amdgcn_exp2f(m2 - mn);
  m2 = mn;
  lsum *= alpha;
  int ai = __builtin_bit_cast(int, alpha), ar;
  ar = __builtin_amdgcn_update_dpp(ai, ai, 0x150, 0x1, 0xf, false);
  ar = __builtin_amdgcn_update_dpp(ar, ai, 0x154, 0x2, 0xf, false);
  ar = __builtin_amdgcn_update_dpp(ar, ai, 0x158, 0x4, 0xf, false);
  ar = __builtin_amdgcn_update_dpp(ar, ai, 0x15c, 0x8, 0xf, false);
  const float al = __builtin_bit_cast(float, ar);
  acc0 *= al; acc1 *= al;
#pragma unroll
  for (int g = 0; g < 8; ++g) {
    const float p = __builtin_amdgcn_exp2f(fmaf(s[g], LOG2E, -mn));
    lsum += p;
    const unsigned pp = cvtpk(p, p);
    const v2u a = {pp & mk0, pp & mk1};
    acc0 = mfma16(a, t.v[g][0], t.v[g][1], acc0);
    acc1 = mfma16(a, t.v[g][2], t.v[g][3], acc1);
    if (REFILL) t.v[g] = ld16<VAUX>(rv, voff, snext + g * 1024);
    SB();
  }
}

template <int N>
__device__ __forceinline__ void gather(rsrc_t rs, const int (&off)[N], unsigned tag, v4u (&x)[N]) {
  for (unsigned spins = 0;; ++spins) {
    bool ok = true;
#pragma unroll
    for (int i = 0; i < N; ++i) x[i] = ld16<AUX_SC1>(rs, off[i], 0);
#pragma unroll
    for (int i = 0; i < N; ++i) ok = ok && x[i][1] == tag && x[i][3] == tag;
    if (__all(ok) || spins > SPIN_CAP) break;
    __builtin_amdgcn_s_sleep(1);
  }
}
template <int S> __device__ __forceinline__ float strided_sum(float x) {
  if (S <= 1) x += dpp<XOR1>(x);
  if (S <= 2) x += dpp<XOR2>(x);
  if (S <= 8) x += dpp<ROR8>(x);
  if (S <= 4) x += dpp<ROR4>(x);
  return xr16_sum(x);
}
template <int C>
__device__ __forceinline__ void publish512(rsrc_t rs, int base, int nprod, int p, f32x4 a0, f32x4 a1, LAS float *tr, int w, int lane, unsigned tag) {
  if (lane < 16) {
    *(LAS f32x4 *)(tr + w * 128 + 8 * lane) = a0;
    *(LAS f32x4 *)(tr + w * 128 + 8 * lane + 4) = a1;
  }
  LDS_WAIT();
  const f32x2 y = *(LAS f32x2 *)(tr + w * 128 + 2 * lane);
  const int col = 128 * w + 2 * lane, j = col / C, within = col % C;
  const v4u g = {asu(y[0]), tag, asu(y[1]), tag};
  st16(rs, base + ((j * nprod + p) * C + within) * 8, g);
}
template <int C, int NP>
__device__ __forceinline__ f32x2 reduce_cols(rsrc_t rs, int base, int j, unsigned tag, LAS float *red, int tid, int w, int lane) {
  constexpr int S = C / 2, NPIECE = NP * S, N = (NPIECE + NTHR - 1) / NTHR;
  int off[N]; v4u x[N];
#pragma unroll
  for (int i = 0; i < N; ++i) { const int pc = i * NTHR + tid; off[i] = base + j * (NP * C * 8) + (pc < NPIECE ? pc : tid) * 16; }
  gather<N>(rs, off, tag, x);
  float s0 = 0.f, s1 = 0.f;
#pragma unroll
  for (int i = 0; i < N; ++i) { const bool live = i * NTHR + tid < NPIECE; s0 += live ? asf(x[i][0]) : 0.f; s1 += live ? asf(x[i][2]) : 0.f; }
  s0 = strided_sum<S>(s0); s1 = strided_sum<S>(s1);
  if (lane < S) { f32x2 y = {s0, s1}; *(LAS f32x2 *)(red + w * 64 + 2 * lane) = y; }
  WG_BARRIER();
  f32x2 r = {0.f, 0.f};
#pragma unroll
  for (int ww = 0; ww < 4; ++ww) r += *(LAS f32x2 *)(red + ww * 64 + 2 * (lane % S));
  return r;
}

#define STAMP_AT(i) do { } while (0)
#define STAMP_FLUSH() do { } while (0)

constexpr int L_MRG = 0;
constexpr int L_ML = L_MRG + 2048;
constexpr int L_RED = L_ML + 32;
constexpr int L_RED2 = L_RED + 256;
constexpr int L_TR = L_RED2 + 256;
constexpr int L_Q = L_TR + 512;
constexpr int L_XN = L_Q + 256;
constexpr int L_END = L_XN + 256;
constexpr int LDS_BYTES = 84 * 1024;

__global__ void __launch_bounds__(NTHR, 2) fused_decode_kernel(Params p) {
  extern __shared__ __attribute__((aligned(16))) float smem[];
  LAS float *L = (LAS float *)smem;
  const int tid = threadIdx.x, lane = tid & 63, w = __builtin_amdgcn_readfirstlane(tid >> 6), b = blockIdx.x;
  STAMP_AT(0);
  const int kh = b >> 5, sp = b & 31;
  const rsrc_t rws = __builtin_amdgcn_make_buffer_rsrc(p.ws, 0, WS_USED, RS3);
  const rsrc_t rk = __builtin_amdgcn_make_buffer_rsrc((char *)p.k + ((size_t)kh * SK + sp * 2048) * 256, 0, 2048 * 256, RS3);
  const rsrc_t rv = __builtin_amdgcn_make_buffer_rsrc((char *)p.v + ((size_t)kh * SK + sp * 2048) * 256, 0, 2048 * 256, RS3);
  const rsrc_t rx = __builtin_amdgcn_make_buffer_rsrc((void *)p.x, 0, DM * 2, RS3);
  const rsrc_t rwq = __builtin_amdgcn_make_buffer_rsrc((void *)p.wq, 0, DM * 4096 * 2, RS3);
  const unsigned epoch = *(const __attribute__((address_space(4))) unsigned *)((const char *)p.ws + WS_EPOCH);
  const v4u xr = ld16<0>(rx, lane * 16, 0);
  const int qf = (b & 7) * 32 + (b >> 3);
  const unsigned xraw = __builtin_amdgcn_raw_buffer_load_b32(rx, (64 * w + lane) * 4, 0, 0);
  const int wqoff = (128 * w + 2 * lane) * 8192 + qf * 32;
  const v4u wa0 = ld16<WAUX>(rwq, wqoff, 0), wa1 = ld16<WAUX>(rwq, wqoff, 8192);
  const v4u wb0 = ld16<WAUX>(rwq, wqoff, 16), wb1 = ld16<WAUX>(rwq, wqoff, 8192 + 16);
  SB();
  asm volatile("s_barrier" ::: "memory");
  SB();
  const int g4 = lane >> 4, a4 = (lane >> 2) & 3, u4 = lane & 3;
  const int koff = a4 * 256 + (4 * g4 + u4) * 16, voff = lane * 16;
  Tile ta, tb;
#pragma unroll
  for (int g = 0; g < 8; ++g) ta.k[g] = ld16<KAUX>(rk, koff, w * 8192 + g * 1024);
#pragma unroll
  for (int g = 0; g < 8; ++g) ta.v[g] = ld16<VAUX>(rv, voff, w * 8192 + g * 1024);
  SB();
  unsigned tch = 0;
  {
    const int idx = tid & 127;
    if (idx * TOUCH < 524288) tch = __builtin_amdgcn_raw_buffer_load_b32(w < 2 ? rk : rv, idx * TOUCH + 128, 0, 2);
  }
  SB();
  const unsigned tag = epoch + 1u;
  {
    unsigned xsel = ln_pair(xraw, ln_stats(xr));
    const float r = gemv16(xsel, wa0, wa1, wb0, wb1, lane);
    if ((lane & 3) == 0) L[L_RED + w * 64 + (lane >> 2)] = r;
    WG_BARRIER();
    if (w == 0) {
      const int i = lane & 7;
      float s0 = 0.f, s1 = 0.f;
#pragma unroll
      for (int ww = 0; ww < 4; ++ww) { const f32x2 y = *(LAS f32x2 *)(L + L_RED + ww * 64 + 2 * i); s0 += y[0]; s1 += y[1]; }
      const v2u g = {cvtpk(s0, s1), tag};
      if (lane < 8 * QREP) st8(rws, WS_QG + (lane >> 3) * 16384 + ((b & 7) * 256 + (b >> 3) * 8 + i) * 8, g);
    }
  }
  STAMP_AT(1);
  asm volatile("s_waitcnt vmcnt(9)" ::: "memory");
  v4u q;
  {
    const int qoff = WS_QG + (b & (QREP - 1)) * 16384 + (kh * 256 + w * 64) * 8 + (lane & 31) * 16;
    v4u x0 = ld16<AUX_SC1>(rws, qoff, 0);
    SB();
#pragma unroll
    for (int g = 0; g < 8; ++g) tb.k[g] = ld16<KAUX>(rk, koff, (4 + w) * 8192 + g * 1024);
#pragma unroll
    for (int g = 0; g < 8; ++g) tb.v[g] = ld16<VAUX>(rv, voff, (4 + w) * 8192 + g * 1024);
    SB();
    bool ok = x0[1] == tag && x0[3] == tag;
    for (unsigned spins = 0; !__all(ok) && spins < SPIN_CAP; ++spins) {
      x0 = ld16<AUX_SC1>(rws, qoff, 0);
      ok = x0[1] == tag && x0[3] == tag;
    }
    if (lane < 32) *(LAS v2u *)(L + L_Q + w * 64 + 2 * lane) = v2u{x0[0], x0[2]};
    WG_BARRIER();
    q = *(LAS v4u *)(L + L_Q + a4 * 64 + (4 * g4 + u4) * 4);
  }
  STAMP_AT(2);
  const int pf = (b & 7) * 28 + (b >> 3);
  const bool hasf = (b >> 3) < 28;
  const rsrc_t rwg = __builtin_amdgcn_make_buffer_rsrc((void *)p.wg, 0, hasf ? DM * FF * 2 : 0, RS3);
  const rsrc_t rwu = __builtin_amdgcn_make_buffer_rsrc((void *)p.wu, 0, hasf ? DM * FF * 2 : 0, RS3);
  const int go = (128 * w + 2 * lane) * (FF * 2) + pf * 16;
  const v4u wg0 = ld16<WAUX>(rwg, go, 0), wg1 = ld16<WAUX>(rwg, go, FF * 2);
  const v4u wu0 = ld16<WAUX>(rwu, go, 0), wu1 = ld16<WAUX>(rwu, go, FF * 2);
  SB();
  const unsigned mk0 = u4 == 0 ? 0xFFFFu : u4 == 1 ? 0xFFFF0000u : 0u;
  const unsigned mk1 = u4 == 2 ? 0xFFFFu : u4 == 3 ? 0xFFFF0000u : 0u;
  float m2 = -3.0e38f, lsum = 0.f;
  asm volatile("" :: "v"(tch));
  f32x4 acc0 = {0.f, 0.f, 0.f, 0.f}, acc1 = acc0;
  for (int it = 0; it < 7; ++it) {
    const int s0 = ((2 * it + 2) * 4 + w) * 8192;
    tile_step<true>(ta, q, rk, rv, koff, voff, s0, mk0, mk1, m2, lsum, acc0, acc1);
    tile_step<true>(tb, q, rk, rv, koff, voff, s0 + 4 * 8192, mk0, mk1, m2, lsum, acc0, acc1);
  }
  const rsrc_t rwo = __builtin_amdgcn_make_buffer_rsrc((char *)p.wo + (size_t)b * 16384, 0, 16384, RS3);
  const rsrc_t rwd = __builtin_amdgcn_make_buffer_rsrc((char *)p.wd + (size_t)(hasf ? pf : 0) * 8192, 0, hasf ? 8192 : 0, RS3);
  v4u wo[4], wd[2];
  {
    const int o = g4 * 1024 + w * 256 + (lane & 15) * 16;
#pragma unroll
    for (int i = 0; i < 4; ++i) wo[i] = ld16<WAUX>(rwo, o, i * 4096);
#pragma unroll
    for (int i = 0; i < 2; ++i) wd[i] = ld16<WAUX>(rwd, o, i * 4096);
    SB();
  }
  tile_step<false>(ta, q, rk, rv, koff, voff, 0, mk0, mk1, m2, lsum, acc0, acc1);
  tile_step<false>(tb, q, rk, rv, koff, voff, 0, mk0, mk1, m2, lsum, acc0, acc1);
  STAMP_AT(3);
  {
    const float lw = xr16_sum(lsum);
    *(LAS f32x4 *)(L + L_MRG + w * 512 + g4 * 128 + 8 * (lane & 15)) = acc0;
    *(LAS f32x4 *)(L + L_MRG + w * 512 + g4 * 128 + 8 * (lane & 15) + 4) = acc1;
    if (lane < 16 && u4 == 0) { f32x2 y = {m2, lw}; *(LAS f32x2 *)(L + L_ML + (w * 4 + a4) * 2) = y; }
    WG_BARRIER();
    f32x2 ml[4];
#pragma unroll
    for (int ww = 0; ww < 4; ++ww) ml[ww] = *(LAS f32x2 *)(L + L_ML + (ww * 4 + w) * 2);
    const float M = fmaxf(fmaxf(ml[0][0], ml[1][0]), fmaxf(ml[2][0], ml[3][0]));
    float Ls = 0.f; f32x2 o = {0.f, 0.f};
#pragma unroll
    for (int ww = 0; ww < 4; ++ww) {
      const float e = __builtin_amdgcn_exp2f(ml[ww][0] - M);
      Ls = fmaf(e, ml[ww][1], Ls);
      o += e * *(LAS f32x2 *)(L + L_MRG + ww * 512 + w * 128 + 2 * lane);
    }
    const int head = 4 * kh + w;
    st16(rws, WS_OP + (((head * 8 + (lane >> 3)) * 32 + sp) * 16 + 2 * (lane & 7)) * 8, v4u{asu(o[0]), tag, asu(o[1]), tag});
    if (lane < 8) st16(rws, WS_ML + ((head * 8 + lane) * 32 + sp) * 16, v4u{asu(M), tag, asu(Ls), tag});
  }
  STAMP_AT(4);
  {
    const int off[2] = {WS_OP + b * 4096 + tid * 16, WS_ML + (b * 32 + 8 * w + (lane >> 3)) * 16};
    v4u x[2];
    gather<2>(rws, off, tag, x);
    STAMP_AT(5);
    const float ms = asf(x[1][0]), ls = asf(x[1][2]);
    const float Mw = xr16_max(fmaxf(ms, dpp<ROR8>(ms)));
    const float e = __builtin_amdgcn_exp2f(ms - Mw);
    const float v0 = strided_sum<8>(e * asf(x[0][0])), v1 = strided_sum<8>(e * asf(x[0][2])), lw = strided_sum<8>(e * ls);
    if (lane < 8) {
      const int d = 2 * lane;
      L[L_RED2 + w * 64 + (d & 3) * 4 + (d >> 2)] = v0;
      L[L_RED2 + w * 64 + ((d + 1) & 3) * 4 + (d >> 2)] = v1;
      if (lane == 0) *(LAS f32x2 *)(L + L_RED2 + w * 64 + 32) = f32x2{Mw, lw};
    }
    WG_BARRIER();
    f32x2 ml[4];
#pragma unroll
    for (int ww = 0; ww < 4; ++ww) ml[ww] = *(LAS f32x2 *)(L + L_RED2 + ww * 64 + 32);
    const float M = fmaxf(fmaxf(ml[0][0], ml[1][0]), fmaxf(ml[2][0], ml[3][0]));
    f32x4 o = {0.f, 0.f, 0.f, 0.f};
    float Lt = 0.f;
#pragma unroll
    for (int ww = 0; ww < 4; ++ww) {
      const float ew = __builtin_amdgcn_exp2f(ml[ww][0] - M);
      Lt = fmaf(ew, ml[ww][1], Lt);
      o += ew * *(LAS f32x4 *)(L + L_RED2 + ww * 64 + g4 * 4);
    }
    o *= __builtin_amdgcn_rcpf(Lt);
    f32x4 c0 = {0.f, 0.f, 0.f, 0.f}, c1 = c0;
#pragma unroll
    for (int i = 0; i < 4; ++i) {
      const unsigned pp = cvtpk(o[i], o[i]);
      const v2u a = {pp & mk0, pp & mk1};
      c0 = mfma16(a, wo[i][0], wo[i][1], c0);
      c1 = mfma16(a, wo[i][2], wo[i][3], c1);
    }
    publish512<512 / NB1>(rws, WS_PB, 256, b, c0, c1, L + L_TR, w, lane, tag);
  }
  STAMP_AT(6);
  if (b < NB1) {
    constexpr int C = 512 / NB1, S = C / 2;
    const unsigned xp = __builtin_amdgcn_raw_buffer_load_b32(rx, (b * C + 2 * (lane % S)) * 2, 0, 0);
    const f32x2 r = reduce_cols<C, 256>(rws, WS_PB, b, tag, L + L_RED, tid, w, lane);
    STAMP_AT(7);
    const unsigned hp = cvtpk(lo16(xp) + rbf(r[0]), hi16(xp) + rbf(r[1]));
    if (tid < S * REP) st8(rws, WS_HG + (tid / S) * 2048 + (b * S + tid % S) * 8, v2u{hp, tag});
  }
  STAMP_AT(8);
  if (w == 0) {
    const int base = WS_HG + (b % REP) * 2048 + lane * 32;
    const int off[2] = {base, base + 16};
    v4u x[2];
    gather<2>(rws, off, tag, x);
    const v4u hb = v4u{x[0][0], x[0][2], x[1][0], x[1][2]};
    *(LAS v4u *)(L + L_Q + 4 * lane) = hb;
    *(LAS v4u *)(L + L_XN + 4 * lane) = layernorm8(hb);
  }
  WG_BARRIER();
  STAMP_AT(9);
  if (hasf) {
    const unsigned xsel = ((LAS unsigned *)L)[L_XN + 64 * w + lane];
    const float r = gemv16(xsel, wg0, wg1, wu0, wu1, lane);
    if ((lane & 3) == 0) { const int vi = lane >> 2, col = vi & 7; L[L_RED2 + w * 64 + 16 + (col & 3) * 4 + (vi >> 3) * 2 + (col >> 2)] = r; }
    WG_BARRIER();
    f32x4 gu = {0.f, 0.f, 0.f, 0.f};
#pragma unroll
    for (int ww = 0; ww < 4; ++ww) gu += *(LAS f32x4 *)(L + L_RED2 + ww * 64 + 16 + g4 * 4);
    f32x4 c0 = {0.f, 0.f, 0.f, 0.f}, c1 = c0;
#pragma unroll
    for (int i = 0; i < 2; ++i) {
      const float hv = rbf(gu[i]) * rbf(gu[2 + i]);
      const unsigned pp = cvtpk(hv, hv);
      const v2u a = {pp & mk0, pp & mk1};
      c0 = mfma16(a, wd[i][0], wd[i][1], c0);
      c1 = mfma16(a, wd[i][2], wd[i][3], c1);
    }
    publish512<512 / ND>(rws, WS_PD, NPF, pf, c0, c1, L + L_TR, w, lane, tag);
  }
  STAMP_AT(10);
  if (b >= NWG - ND) {
    constexpr int C = 512 / ND, S = C / 2;
    const int j = b - (NWG - ND);
    const unsigned hp = ((LAS unsigned *)L)[L_Q + j * S + lane % S];
    WG_BARRIER();
    const f32x2 r = reduce_cols<C, NPF>(rws, WS_PD, j, tag, L + L_RED, tid, w, lane);
    const unsigned op = cvtpk(lo16(hp) + rbf(r[0]), hi16(hp) + rbf(r[1]));
    if (w == 0 && lane < S) ((unsigned *)p.out)[j * S + lane] = op;
    if (j == 0 && tid == 0) __builtin_amdgcn_raw_buffer_store_b32(tag, rws, WS_EPOCH, 0, AUX_SC1);
  }
  STAMP_AT(11);
  STAMP_FLUSH();
}
}

extern "C" void kernel_launch(void *const *d_in, const int *in_sizes, int n_in, void *d_out, int out_size, void *d_ws, size_t ws_size,
                              hipStream_t stream) {
  Params p;
  p.x = d_in[0]; p.k = d_in[1]; p.v = d_in[2]; p.wq = d_in[3]; p.wo = d_in[4];
  p.wg = d_in[6]; p.wu = d_in[7]; p.wd = d_in[8];
  p.out = d_out; p.ws = d_ws;
  (void)hipFuncSetAttribute((const void *)fused_decode_kernel, hipFuncAttributeMaxDynamicSharedMemorySize, LDS_BYTES);
  fused_decode_kernel<<<NWG, NTHR, LDS_BYTES, stream>>>(p);
}
```

```cpp
#include <hip/hip_runtime.h>
#include <stdint.h>

#define STAMP 0
#define NB1 64
#define ND 32
#define REP 32
#define KAUX 2
#define VAUX 2
#define WAUX 2
#define SPINTO 8000
#define QREP 4

namespace {
typedef unsigned v4u __attribute__((ext_vector_type(4)));
typedef unsigned v2u __attribute__((ext_vector_type(2)));
typedef float f32x4 __attribute__((ext_vector_type(4)));
typedef float f32x2 __attribute__((ext_vector_type(2)));
typedef short s16x4 __attribute__((ext_vector_type(4)));
typedef __bf16 bf16x8 __attribute__((ext_vector_type(8)));
typedef __bf16 bf16x2 __attribute__((ext_vector_type(2)));
typedef __amdgpu_buffer_rsrc_t rsrc_t;
#define LAS __attribute__((address_space(3)))

constexpr int DM = 512, SK = 65536, DH = 128, FF = 1792;
constexpr int NWG = 256, NTHR = 256;
constexpr int NPF = 224;
constexpr int AUX_NT = 2, AUX_SC1 = 16;
constexpr int RS3 = 0x00020000;
constexpr int WS_EPOCH = 0;
constexpr int WS_QG = 4096;
constexpr int WS_ML = WS_QG + 8 * 16384;
constexpr int WS_HG = WS_ML + 131072;
constexpr int WS_OP = 1 << 20;
constexpr int WS_PB = 2 << 20;
constexpr int WS_PD = 3 << 20;
constexpr int WS_USED = 4 << 20;
constexpr int WS_STAMP = 8 << 20;
constexpr unsigned SPIN_CAP = 1u << 14;
constexpr float LOG2E = 1.4426950408889634f;
constexpr float SCALE = 0.08838834764831845f;

struct Params {
  const void *x, *k, *v, *wq, *wo, *wg, *wu, *wd;
  void *out, *ws;
};

__device__ __forceinline__ float asf(unsigned u) { return __builtin_bit_cast(float, u); }
__device__ __forceinline__ unsigned asu(float f) { return __builtin_bit_cast(unsigned, f); }
template <int C> __device__ __forceinline__ float dpp(float x) {
  return __builtin_bit_cast(float, __builtin_amdgcn_mov_dpp(__builtin_bit_cast(int, x), C, 0xf, 0xf, true));
}
constexpr int XOR1 = 0xB1, XOR2 = 0x4E, HMIR = 0x141, MIR = 0x140, ROR4 = 0x124, ROR8 = 0x128;
__device__ __forceinline__ unsigned cvtpk(float lo, float hi) {
  f32x2 v = {lo, hi};
  bf16x2 r = __builtin_convertvector(v, bf16x2);
  return __builtin_bit_cast(unsigned, r);
}
__device__ __forceinline__ float lo16(unsigned u) { return asf(u << 16); }
__device__ __forceinline__ float hi16(unsigned u) { return asf(u & 0xffff0000u); }
__device__ __forceinline__ float rbf(float x) { return lo16(cvtpk(x, x)); }
__device__ __forceinline__ float xr16_max(float x) {
  auto s = __builtin_amdgcn_permlane16_swap(asu(x), asu(x), false, false);
  x = fmaxf(asf(s[0]), asf(s[1]));
  auto t = __builtin_amdgcn_permlane32_swap(asu(x), asu(x), false, false);
  return fmaxf(asf(t[0]), asf(t[1]));
}
__device__ __forceinline__ float xr16_sum(float x) {
  auto s = __builtin_amdgcn_permlane16_swap(asu(x), asu(x), false, false);
  x = asf(s[0]) + asf(s[1]);
  auto t = __builtin_amdgcn_permlane32_swap(asu(x), asu(x), false, false);
  return asf(t[0]) + asf(t[1]);
}
__device__ __forceinline__ float row_sum(float x) {
  x += dpp<XOR1>(x); x += dpp<XOR2>(x); x += dpp<HMIR>(x); x += dpp<MIR>(x); return x;
}
__device__ __forceinline__ float row_max(float x) {
  x = fmaxf(x, dpp<XOR1>(x)); x = fmaxf(x, dpp<XOR2>(x)); x = fmaxf(x, dpp<HMIR>(x)); x = fmaxf(x, dpp<MIR>(x)); return x;
}
__device__ __forceinline__ float wave_sum(float x) { return xr16_sum(row_sum(x)); }
__device__ __forceinline__ f32x4 mfma32(v4u a, v4u b, f32x4 c) {
  return __builtin_amdgcn_mfma_f32_16x16x32_bf16(__builtin_bit_cast(bf16x8, a), __builtin_bit_cast(bf16x8, b), c, 0, 0, 0);
}
__device__ __forceinline__ f32x4 mfma16(v2u a, unsigned b0, unsigned b1, f32x4 c) {
  v2u b = {b0, b1};
  return __builtin_amdgcn_mfma_f32_16x16x16bf16_1k(__builtin_bit_cast(s16x4, a), __builtin_bit_cast(s16x4, b), c, 0, 0, 0);
}
template <int AUX> __device__ __forceinline__ v4u ld16(rsrc_t r, int voff, int soff) {
  return __builtin_amdgcn_raw_buffer_load_b128(r, voff, soff, AUX);
}
__device__ __forceinline__ void st16(rsrc_t r, int voff, v4u v) { __builtin_amdgcn_raw_buffer_store_b128(v, r, voff, 0, AUX_SC1); }
__device__ __forceinline__ void st8(rsrc_t r, int voff, v2u v) { __builtin_amdgcn_raw_buffer_store_b64(v, r, voff, 0, AUX_SC1); }
#define WG_BARRIER() asm volatile("s_waitcnt lgkmcnt(0)\n\ts_barrier" ::: "memory")
#define LDS_WAIT() asm volatile("s_waitcnt lgkmcnt(0)" ::: "memory")
#define SB() __builtin_amdgcn_sched_barrier(0)

__device__ __forceinline__ v4u layernorm8(v4u p) {
  float x[8];
#pragma unroll
  for (int i = 0; i < 4; ++i) { x[2 * i] = lo16(p[i]); x[2 * i + 1] = hi16(p[i]); }
  float s = ((x[0] + x[1]) + (x[2] + x[3])) + ((x[4] + x[5]) + (x[6] + x[7]));
  const float mean = wave_sum(s) * (1.f / DM);
  float q = 0.f;
#pragma unroll
  for (int i = 0; i < 8; ++i) { const float d = x[i] - mean; q = fmaf(d, d, q); }
  const float var = wave_sum(q) * (1.f / DM);
  const float mb = rbf(mean);
  const float den = rbf(sqrtf(rbf(rbf(var) + 1e-5f)));
  v4u r;
#pragma unroll
  for (int i = 0; i < 4; ++i) r[i] = cvtpk(rbf(x[2 * i] - mb) / den, rbf(x[2 * i + 1] - mb) / den);
  return r;
}
struct LnStat { float mb, rden; float den; };
__device__ __forceinline__ LnStat ln_stats(v4u p) {
  float x[8];
#pragma unroll
  for (int i = 0; i < 4; ++i) { x[2 * i] = lo16(p[i]); x[2 * i + 1] = hi16(p[i]); }
  float s = ((x[0] + x[1]) + (x[2] + x[3])) + ((x[4] + x[5]) + (x[6] + x[7]));
  const float mean = wave_sum(s) * (1.f / DM);
  float q = 0.f;
#pragma unroll
  for (int i = 0; i < 8; ++i) { const float d = x[i] - mean; q = fmaf(d, d, q); }
  const float var = wave_sum(q) * (1.f / DM);
  LnStat r; r.mb = rbf(mean); r.den = rbf(sqrtf(rbf(rbf(var) + 1e-5f))); r.rden = 0.f;
  return r;
}
__device__ __forceinline__ unsigned ln_pair(unsigned p, LnStat st) { return cvtpk(rbf(lo16(p) - st.mb) / st.den, rbf(hi16(p) - st.mb) / st.den); }
__device__ __forceinline__ unsigned pick4(v4u v, int w) {
  const unsigned a = (w & 1) ? v[1] : v[0], b = (w & 1) ? v[3] : v[2];
  return (w & 2) ? b : a;
}
__device__ __forceinline__ float gemv16(unsigned xp, v4u a0, v4u a1, v4u b0, v4u b1, int lane) {
  float v[16];
  const bf16x2 xb = __builtin_bit_cast(bf16x2, xp);
#pragma unroll
  for (int i = 0; i < 4; ++i) {
    const unsigned alo = __builtin_amdgcn_perm(a1[i], a0[i], 0x05040100u), ahi = __builtin_amdgcn_perm(a1[i], a0[i], 0x07060302u);
    const unsigned blo = __builtin_amdgcn_perm(b1[i], b0[i], 0x05040100u), bhi = __builtin_amdgcn_perm(b1[i], b0[i], 0x07060302u);
    v[2 * i] = __builtin_amdgcn_fdot2_f32_bf16(__builtin_bit_cast(bf16x2, alo), xb, 0.f, false);
    v[2 * i + 1] = __builtin_amdgcn_fdot2_f32_bf16(__builtin_bit_cast(bf16x2, ahi), xb, 0.f, false);
    v[8 + 2 * i] = __builtin_amdgcn_fdot2_f32_bf16(__builtin_bit_cast(bf16x2, blo), xb, 0.f, false);
    v[8 + 2 * i + 1] = __builtin_amdgcn_fdot2_f32_bf16(__builtin_bit_cast(bf16x2, bhi), xb, 0.f, false);
  }
  float u[8];
#pragma unroll
  for (int k = 0; k < 8; ++k) { auto s = __builtin_amdgcn_permlane32_swap(asu(v[k]), asu(v[k + 8]), false, false); u[k] = asf(s[0]) + asf(s[1]); }
  float t[4];
#pragma unroll
  for (int k = 0; k < 4; ++k) { auto s = __builtin_amdgcn_permlane16_swap(asu(u[k]), asu(u[k + 4]), false, false); t[k] = asf(s[0]) + asf(s[1]); }
  float y[2];
#pragma unroll
  for (int k = 0; k < 2; ++k) { const float p = t[k] + dpp<ROR8>(t[k]), q = t[k + 2] + dpp<ROR8>(t[k + 2]); y[k] = (lane & 8) ? q : p; }
#pragma unroll
  for (int k = 0; k < 2; ++k) { y[k] += dpp<XOR1>(y[k]); y[k] += dpp<XOR2>(y[k]); y[k] += dpp<HMIR>(y[k]); }
  return (lane & 4) ? y[1] : y[0];
}

struct Tile { v4u k[8], v[8]; };

template <bool REFILL>
__device__ __forceinline__ void tile_step(Tile &t, v4u q, rsrc_t rk, rsrc_t rv, int koff, int voff, int snext, unsigned mk0, unsigned mk1,
                                          float &m2, float &lsum, f32x4 &acc0, f32x4 &acc1) {
  float s[8];
  const f32x4 z = {0.f, 0.f, 0.f, 0.f};
  f32x4 d[8];
#pragma unroll
  for (int g = 0; g < 8; ++g) {
    d[g] = mfma32(t.k[g], q, z);
    if (REFILL) t.k[g] = ld16<KAUX>(rk, koff, snext + g * 1024);
    SB();
  }
#pragma unroll
  for (int g = 0; g < 8; ++g) {
    s[g] = ((dpp<0x00>(d[g][0]) + dpp<0x55>(d[g][1])) + dpp<0xAA>(d[g][2])) + dpp<0xFF>(d[g][3]);
    asm("" : "+v"(s[g]));
  }
#pragma unroll
  for (int g = 0; g < 8; g += 2) {
    const unsigned a = cvtpk(s[g], s[g + 1]);
    const unsigned b = cvtpk(lo16(a) * SCALE, hi16(a) * SCALE);
    s[g] = lo16(b); s[g + 1] = hi16(b);
  }
  float mx = fmaxf(fmaxf(fmaxf(s[0], s[1]), fmaxf(s[2], s[3])), fmaxf(fmaxf(s[4], s[5]), fmaxf(s[6], s[7])));
  mx = xr16_max(mx);
  const float mn = fmaxf(m2, mx * LOG2E);
  const float alpha = __builtin_amdgcn_exp2f(m2 - mn);
  m2 = mn;
  lsum *= alpha;
  int ai = __builtin_bit_cast(int, alpha), ar;
  ar = __builtin_amdgcn_update_dpp(ai, ai, 0x150, 0x1, 0xf, false);
  ar = __builtin_amdgcn_update_dpp(ar, ai, 0x154, 0x2, 0xf, false);
  ar = __builtin_amdgcn_update_dpp(ar, ai, 0x158, 0x4, 0xf, false);
  ar = __builtin_amdgcn_update_dpp(ar, ai, 0x15c, 0x8, 0xf, false);
  const float al = __builtin_bit_cast(float, ar);
  acc0 *= al; acc1 *= al;
#pragma unroll
  for (int g = 0; g < 8; ++g) {
    const float p = __builtin_amdgcn_exp2f(fmaf(s[g], LOG2E, -mn));
    lsum += p;
    const unsigned pp = cvtpk(p, p);
    const v2u a = {pp & mk0, pp & mk1};
    acc0 = mfma16(a, t.v[g][0], t.v[g][1], acc0);
    acc1 = mfma16(a, t.v[g][2], t.v[g][3], acc1);
    if (REFILL) t.v[g] = ld16<VAUX>(rv, voff, snext + g * 1024);
    SB();
  }
}

template <int N>
__device__ __forceinline__ void gather(rsrc_t rs, const int (&off)[N], unsigned tag, v4u (&x)[N]) {
  for (unsigned spins = 0;; ++spins) {
    bool ok = true;
#pragma unroll
    for (int i = 0; i < N; ++i) x[i] = ld16<AUX_SC1>(rs, off[i], 0);
#pragma unroll
    for (int i = 0; i < N; ++i) ok = ok && x[i][1] == tag && x[i][3] == tag;
    if (__all(ok) || spins > SPIN_CAP) break;
    __builtin_amdgcn_s_sleep(1);
  }
}
template <int S> __device__ __forceinline__ float strided_sum(float x) {
  if (S <= 1) x += dpp<XOR1>(x);
  if (S <= 2) x += dpp<XOR2>(x);
  if (S <= 8) x += dpp<ROR8>(x);
  if (S <= 4) x += dpp<ROR4>(x);
  return xr16_sum(x);
}
template <int C>
__device__ __forceinline__ void publish512(rsrc_t rs, int base, int nprod, int p, f32x4 a0, f32x4 a1, LAS float *tr, int w, int lane, unsigned tag) {
  if (lane < 16) {
    *(LAS f32x4 *)(tr + w * 128 + 8 * lane) = a0;
    *(LAS f32x4 *)(tr + w * 128 + 8 * lane + 4) = a1;
  }
  LDS_WAIT();
  const f32x2 y = *(LAS f32x2 *)(tr + w * 128 + 2 * lane);
  const int col = 128 * w + 2 * lane, j = col / C, within = col % C;
  const v4u g = {asu(y[0]), tag, asu(y[1]), tag};
  st16(rs, base + ((j * nprod + p) * C + within) * 8, g);
}
template <int C, int NP>
__device__ __forceinline__ f32x2 reduce_cols(rsrc_t rs, int base, int j, unsigned tag, LAS float *red, int tid, int w, int lane) {
  constexpr int S = C / 2, NPIECE = NP * S, N = (NPIECE + NTHR - 1) / NTHR;
  int off[N]; v4u x[N];
#pragma unroll
  for (int i = 0; i < N; ++i) { const int pc = i * NTHR + tid; off[i] = base + j * (NP * C * 8) + (pc < NPIECE ? pc : tid) * 16; }
  gather<N>(rs, off, tag, x);
  float s0 = 0.f, s1 = 0.f;
#pragma unroll
  for (int i = 0; i < N; ++i) { const bool live = i * NTHR + tid < NPIECE; s0 += live ? asf(x[i][0]) : 0.f; s1 += live ? asf(x[i][2]) : 0.f; }
  s0 = strided_sum<S>(s0); s1 = strided_sum<S>(s1);
  if (lane < S) { f32x2 y = {s0, s1}; *(LAS f32x2 *)(red + w * 64 + 2 * lane) = y; }
  WG_BARRIER();
  f32x2 r = {0.f, 0.f};
#pragma unroll
  for (int ww = 0; ww < 4; ++ww) r += *(LAS f32x2 *)(red + ww * 64 + 2 * (lane % S));
  return r;
}

#define STAMP_AT(i) do { } while (0)
#define STAMP_FLUSH() do { } while (0)

constexpr int L_MRG = 0;
constexpr int L_ML = L_MRG + 2048;
constexpr int L_RED = L_ML + 32;
constexpr int L_RED2 = L_RED + 256;
constexpr int L_TR = L_RED2 + 256;
constexpr int L_Q = L_TR + 512;
constexpr int L_XN = L_Q + 256;
constexpr int L_END = L_XN + 256;
constexpr int LDS_BYTES = 84 * 1024;

__global__ void __launch_bounds__(NTHR, 2) fused_decode_kernel(Params p) {
  extern __shared__ __attribute__((aligned(16))) float smem[];
  LAS float *L = (LAS float *)smem;
  const int tid = threadIdx.x, lane = tid & 63, w = __builtin_amdgcn_readfirstlane(tid >> 6), b = blockIdx.x;
  const unsigned long long tstart = __builtin_amdgcn_s_memrealtime();
  STAMP_AT(0);
  const int kh = b >> 5, sp = b & 31;
  const rsrc_t rws = __builtin_amdgcn_make_buffer_rsrc(p.ws, 0, WS_USED, RS3);
  const rsrc_t rk = __builtin_amdgcn_make_buffer_rsrc((char *)p.k + ((size_t)kh * SK + sp * 2048) * 256, 0, 2048 * 256, RS3);
  const rsrc_t rv = __builtin_amdgcn_make_buffer_rsrc((char *)p.v + ((size_t)kh * SK + sp * 2048) * 256, 0, 2048 * 256, RS3);
  const rsrc_t rx = __builtin_amdgcn_make_buffer_rsrc((void *)p.x, 0, DM * 2, RS3);
  const rsrc_t rwq = __builtin_amdgcn_make_buffer_rsrc((void *)p.wq, 0, DM * 4096 * 2, RS3);
  const unsigned epoch = *(const __attribute__((address_space(4))) unsigned *)((const char *)p.ws + WS_EPOCH);
  const v4u xr = ld16<0>(rx, lane * 16, 0);
  const int qf = (b & 7) * 32 + (b >> 3);
  const unsigned xraw = __builtin_amdgcn_raw_buffer_load_b32(rx, (64 * w + lane) * 4, 0, 0);
  const int wqoff = (128 * w + 2 * lane) * 8192 + qf * 32;
  const v4u wa0 = ld16<WAUX>(rwq, wqoff, 0), wa1 = ld16<WAUX>(rwq, wqoff, 8192);
  const v4u wb0 = ld16<WAUX>(rwq, wqoff, 16), wb1 = ld16<WAUX>(rwq, wqoff, 8192 + 16);
  SB();
  asm volatile("s_barrier" ::: "memory");
  SB();
  const int g4 = lane >> 4, a4 = (lane >> 2) & 3, u4 = lane & 3;
  const int koff = a4 * 256 + (4 * g4 + u4) * 16, voff = lane * 16;
  Tile ta, tb;
#pragma unroll
  for (int g = 0; g < 8; ++g) ta.k[g] = ld16<KAUX>(rk, koff, w * 8192 + g * 1024);
#pragma unroll
  for (int g = 0; g < 8; ++g) ta.v[g] = ld16<VAUX>(rv, voff, w * 8192 + g * 1024);
  SB();
  const unsigned tag = epoch + 1u;
  {
    unsigned xsel = ln_pair(xraw, ln_stats(xr));
    const float r = gemv16(xsel, wa0, wa1, wb0, wb1, lane);
    if ((lane & 3) == 0) L[L_RED + w * 64 + (lane >> 2)] = r;
    WG_BARRIER();
    if (w == 0) {
      const int i = lane & 7;
      float s0 = 0.f, s1 = 0.f;
#pragma unroll
      for (int ww = 0; ww < 4; ++ww) { const f32x2 y = *(LAS f32x2 *)(L + L_RED + ww * 64 + 2 * i); s0 += y[0]; s1 += y[1]; }
      const v2u g = {cvtpk(s0, s1), tag};
      if (lane < 8 * QREP) st8(rws, WS_QG + (lane >> 3) * 16384 + ((b & 7) * 256 + (b >> 3) * 8 + i) * 8, g);
    }
  }
  STAMP_AT(1);
  asm volatile("s_waitcnt vmcnt(9)" ::: "memory");
  v4u q;
  {
    const int qoff = WS_QG + (b & (QREP - 1)) * 16384 + (kh * 256 + w * 64) * 8 + (lane & 31) * 16;
    v4u x0 = ld16<AUX_SC1>(rws, qoff, 0);
    SB();
#pragma unroll
    for (int g = 0; g < 8; ++g) tb.k[g] = ld16<KAUX>(rk, koff, (4 + w) * 8192 + g * 1024);
#pragma unroll
    for (int g = 0; g < 8; ++g) tb.v[g] = ld16<VAUX>(rv, voff, (4 + w) * 8192 + g * 1024);
    SB();
    bool ok = x0[1] == tag && x0[3] == tag;
    for (unsigned spins = 0; !__all(ok) && spins < SPIN_CAP; ++spins) {
      x0 = ld16<AUX_SC1>(rws, qoff, 0);
      ok = x0[1] == tag && x0[3] == tag;
    }
    if (lane < 32) *(LAS v2u *)(L + L_Q + w * 64 + 2 * lane) = v2u{x0[0], x0[2]};
    WG_BARRIER();
    q = *(LAS v4u *)(L + L_Q + a4 * 64 + (4 * g4 + u4) * 4);
  }
  STAMP_AT(2);
  const int pf = (b & 7) * 28 + (b >> 3);
  const bool hasf = (b >> 3) < 28;
  const rsrc_t rwg = __builtin_amdgcn_make_buffer_rsrc((void *)p.wg, 0, hasf ? DM * FF * 2 : 0, RS3);
  const rsrc_t rwu = __builtin_amdgcn_make_buffer_rsrc((void *)p.wu, 0, hasf ? DM * FF * 2 : 0, RS3);
  const int go = (128 * w + 2 * lane) * (FF * 2) + pf * 16;
  const v4u wg0 = ld16<WAUX>(rwg, go, 0), wg1 = ld16<WAUX>(rwg, go, FF * 2);
  const v4u wu0 = ld16<WAUX>(rwu, go, 0), wu1 = ld16<WAUX>(rwu, go, FF * 2);
  SB();
  const unsigned mk0 = u4 == 0 ? 0xFFFFu : u4 == 1 ? 0xFFFF0000u : 0u;
  const unsigned mk1 = u4 == 2 ? 0xFFFFu : u4 == 3 ? 0xFFFF0000u : 0u;
  float m2 = -3.0e38f, lsum = 0.f;
  f32x4 acc0 = {0.f, 0.f, 0.f, 0.f}, acc1 = acc0;
  for (int it = 0; it < 7; ++it) {
    const int s0 = ((2 * it + 2) * 4 + w) * 8192;
    tile_step<true>(ta, q, rk, rv, koff, voff, s0, mk0, mk1, m2, lsum, acc0, acc1);
    tile_step<true>(tb, q, rk, rv, koff, voff, s0 + 4 * 8192, mk0, mk1, m2, lsum, acc0, acc1);
  }
  const rsrc_t rwo = __builtin_amdgcn_make_buffer_rsrc((char *)p.wo + (size_t)b * 16384, 0, 16384, RS3);
  const rsrc_t rwd = __builtin_amdgcn_make_buffer_rsrc((char *)p.wd + (size_t)(hasf ? pf : 0) * 8192, 0, hasf ? 8192 : 0, RS3);
  v4u wo[4], wd[2];
  {
    const int o = g4 * 1024 + w * 256 + (lane & 15) * 16;
#pragma unroll
    for (int i = 0; i < 4; ++i) wo[i] = ld16<WAUX>(rwo, o, i * 4096);
#pragma unroll
    for (int i = 0; i < 2; ++i) wd[i] = ld16<WAUX>(rwd, o, i * 4096);
    SB();
  }
  tile_step<false>(ta, q, rk, rv, koff, voff, 0, mk0, mk1, m2, lsum, acc0, acc1);
  tile_step<false>(tb, q, rk, rv, koff, voff, 0, mk0, mk1, m2, lsum, acc0, acc1);
  STAMP_AT(3);
  {
    const float lw = xr16_sum(lsum);
    *(LAS f32x4 *)(L + L_MRG + w * 512 + g4 * 128 + 8 * (lane & 15)) = acc0;
    *(LAS f32x4 *)(L + L_MRG + w * 512 + g4 * 128 + 8 * (lane & 15) + 4) = acc1;
    if (lane < 16 && u4 == 0) { f32x2 y = {m2, lw}; *(LAS f32x2 *)(L + L_ML + (w * 4 + a4) * 2) = y; }
    WG_BARRIER();
    f32x2 ml[4];
#pragma unroll
    for (int ww = 0; ww < 4; ++ww) ml[ww] = *(LAS f32x2 *)(L + L_ML + (ww * 4 + w) * 2);
    const float M = fmaxf(fmaxf(ml[0][0], ml[1][0]), fmaxf(ml[2][0], ml[3][0]));
    float Ls = 0.f; f32x2 o = {0.f, 0.f};
#pragma unroll
    for (int ww = 0; ww < 4; ++ww) {
      const float e = __builtin_amdgcn_exp2f(ml[ww][0] - M);
      Ls = fmaf(e, ml[ww][1], Ls);
      o += e * *(LAS f32x2 *)(L + L_MRG + ww * 512 + w * 128 + 2 * lane);
    }
    const int head = 4 * kh + w;
    st16(rws, WS_OP + (((head * 8 + (lane >> 3)) * 32 + sp) * 16 + 2 * (lane & 7)) * 8, v4u{asu(o[0]), tag, asu(o[1]), tag});
    if (lane < 8) st16(rws, WS_ML + ((head * 8 + lane) * 32 + sp) * 16, v4u{asu(M), tag, asu(Ls), tag});
  }
  STAMP_AT(4);
  {
    const int off[2] = {WS_OP + b * 4096 + tid * 16, WS_ML + (b * 32 + 8 * w + (lane >> 3)) * 16};
    v4u x[2];
    gather<2>(rws, off, tag, x);
    STAMP_AT(5);
    const float ms = asf(x[1][0]), ls = asf(x[1][2]);
    const float Mw = xr16_max(fmaxf(ms, dpp<ROR8>(ms)));
    const float e = __builtin_amdgcn_exp2f(ms - Mw);
    const float v0 = strided_sum<8>(e * asf(x[0][0])), v1 = strided_sum<8>(e * asf(x[0][2])), lw = strided_sum<8>(e * ls);
    if (lane < 8) {
      const int d = 2 * lane;
      L[L_RED2 + w * 64 + (d & 3) * 4 + (d >> 2)] = v0;
      L[L_RED2 + w * 64 + ((d + 1) & 3) * 4 + (d >> 2)] = v1;
      if (lane == 0) *(LAS f32x2 *)(L + L_RED2 + w * 64 + 32) = f32x2{Mw, lw};
    }
    WG_BARRIER();
    f32x2 ml[4];
#pragma unroll
    for (int ww = 0; ww < 4; ++ww) ml[ww] = *(LAS f32x2 *)(L + L_RED2 + ww * 64 + 32);
    const float M = fmaxf(fmaxf(ml[0][0], ml[1][0]), fmaxf(ml[2][0], ml[3][0]));
    f32x4 o = {0.f, 0.f, 0.f, 0.f};
    float Lt = 0.f;
#pragma unroll
    for (int ww = 0; ww < 4; ++ww) {
      const float ew = __builtin_amdgcn_exp2f(ml[ww][0] - M);
      Lt = fmaf(ew, ml[ww][1], Lt);
      o += ew * *(LAS f32x4 *)(L + L_RED2 + ww * 64 + g4 * 4);
    }
    o *= __builtin_amdgcn_rcpf(Lt);
    f32x4 c0 = {0.f, 0.f, 0.f, 0.f}, c1 = c0;
#pragma unroll
    for (int i = 0; i < 4; ++i) {
      const unsigned pp = cvtpk(o[i], o[i]);
      const v2u a = {pp & mk0, pp & mk1};
      c0 = mfma16(a, wo[i][0], wo[i][1], c0);
      c1 = mfma16(a, wo[i][2], wo[i][3], c1);
    }
    publish512<512 / NB1>(rws, WS_PB, 256, b, c0, c1, L + L_TR, w, lane, tag);
  }
  STAMP_AT(6);
  if (b < NB1) {
    constexpr int C = 512 / NB1, S = C / 2;
    const unsigned xp = __builtin_amdgcn_raw_buffer_load_b32(rx, (b * C + 2 * (lane % S)) * 2, 0, 0);
    const f32x2 r = reduce_cols<C, 256>(rws, WS_PB, b, tag, L + L_RED, tid, w, lane);
    STAMP_AT(7);
    const unsigned hp = cvtpk(lo16(xp) + rbf(r[0]), hi16(xp) + rbf(r[1]));
    if (tid < S * REP) st8(rws, WS_HG + (tid / S) * 2048 + (b * S + tid % S) * 8, v2u{hp, tag});
  }
  STAMP_AT(8);
  if (w == 0) {
    const int base = WS_HG + (b % REP) * 2048 + lane * 32;
    const int off[2] = {base, base + 16};
    v4u x[2];
    gather<2>(rws, off, tag, x);
    const v4u hb = v4u{x[0][0], x[0][2], x[1][0], x[1][2]};
    *(LAS v4u *)(L + L_Q + 4 * lane) = hb;
    *(LAS v4u *)(L + L_XN + 4 * lane) = layernorm8(hb);
  }
  WG_BARRIER();
  STAMP_AT(9);
  if (hasf) {
    const unsigned xsel = ((LAS unsigned *)L)[L_XN + 64 * w + lane];
    const float r = gemv16(xsel, wg0, wg1, wu0, wu1, lane);
    if ((lane & 3) == 0) { const int vi = lane >> 2, col = vi & 7; L[L_RED2 + w * 64 + 16 + (col & 3) * 4 + (vi >> 3) * 2 + (col >> 2)] = r; }
    WG_BARRIER();
    f32x4 gu = {0.f, 0.f, 0.f, 0.f};
#pragma unroll
    for (int ww = 0; ww < 4; ++ww) gu += *(LAS f32x4 *)(L + L_RED2 + ww * 64 + 16 + g4 * 4);
    f32x4 c0 = {0.f, 0.f, 0.f, 0.f}, c1 = c0;
#pragma unroll
    for (int i = 0; i < 2; ++i) {
      const float hv = rbf(gu[i]) * rbf(gu[2 + i]);
      const unsigned pp = cvtpk(hv, hv);
      const v2u a = {pp & mk0, pp & mk1};
      c0 = mfma16(a, wd[i][0], wd[i][1], c0);
      c1 = mfma16(a, wd[i][2], wd[i][3], c1);
    }
    publish512<512 / ND>(rws, WS_PD, NPF, pf, c0, c1, L + L_TR, w, lane, tag);
  }
  STAMP_AT(10);
  if (b >= NWG - ND) {
    constexpr int C = 512 / ND, S = C / 2;
    const int j = b - (NWG - ND);
    const unsigned hp = ((LAS unsigned *)L)[L_Q + j * S + lane % S];
    WG_BARRIER();
    const f32x2 r = reduce_cols<C, NPF>(rws, WS_PD, j, tag, L + L_RED, tid, w, lane);
    const unsigned op = cvtpk(lo16(hp) + rbf(r[0]), hi16(hp) + rbf(r[1]));
    if (w == 0 && lane < S) ((unsigned *)p.out)[j * S + lane] = op;
    if (j == 0 && tid == 0) __builtin_amdgcn_raw_buffer_store_b32(tag, rws, WS_EPOCH, 0, AUX_SC1);
  }
  STAMP_AT(11);
  STAMP_FLUSH();
  if (b == 0) { while (__builtin_amdgcn_s_memrealtime() - tstart < SPINTO) __builtin_amdgcn_s_sleep(2); }
}
}

extern "C" void kernel_launch(void *const *d_in, const int *in_sizes, int n_in, void *d_out, int out_size, void *d_ws, size_t ws_size,
                              hipStream_t stream) {
  Params p;
  p.x = d_in[0]; p.k = d_in[1]; p.v = d_in[2]; p.wq = d_in[3]; p.wo = d_in[4];
  p.wg = d_in[6]; p.wu = d_in[7]; p.wd = d_in[8];
  p.out = d_out; p.ws = d_ws;
  (void)hipFuncSetAttribute((const void *)fused_decode_kernel, hipFuncAttributeMaxDynamicSharedMemorySize, LDS_BYTES);
  fused_decode_kernel<<<NWG, NTHR, LDS_BYTES, stream>>>(p);
}
```

```cpp
#include <hip/hip_runtime.h>
#include <stdint.h>

#define STAMP 0
#define NB1 64
#define ND 32
#define REP 32
#define KAUX 2
#define VAUX 2
#define WAUX 2
#define SPINTO 10000
#define QREP 4

namespace {
typedef unsigned v4u __attribute__((ext_vector_type(4)));
typedef unsigned v2u __attribute__((ext_vector_type(2)));
typedef float f32x4 __attribute__((ext_vector_type(4)));
typedef float f32x2 __attribute__((ext_vector_type(2)));
typedef short s16x4 __attribute__((ext_vector_type(4)));
typedef __bf16 bf16x8 __attribute__((ext_vector_type(8)));
typedef __bf16 bf16x2 __attribute__((ext_vector_type(2)));
typedef __amdgpu_buffer_rsrc_t rsrc_t;
#define LAS __attribute__((address_space(3)))

constexpr int DM = 512, SK = 65536, DH = 128, FF = 1792;
constexpr int NWG = 256, NTHR = 256;
constexpr int NPF = 224;
constexpr int AUX_NT = 2, AUX_SC1 = 16;
constexpr int RS3 = 0x00020000;
constexpr int WS_EPOCH = 0;
constexpr int WS_QG = 4096;
constexpr int WS_ML = WS_QG + 8 * 16384;
constexpr int WS_HG = WS_ML + 131072;
constexpr int WS_OP = 1 << 20;
constexpr int WS_PB = 2 << 20;
constexpr int WS_PD = 3 << 20;
constexpr int WS_USED = 4 << 20;
constexpr int WS_STAMP = 8 << 20;
constexpr unsigned SPIN_CAP = 1u << 14;
constexpr float LOG2E = 1.4426950408889634f;
constexpr float SCALE = 0.08838834764831845f;

struct Params {
  const void *x, *k, *v, *wq, *wo, *wg, *wu, *wd;
  void *out, *ws;
};

__device__ __forceinline__ float asf(unsigned u) { return __builtin_bit_cast(float, u); }
__device__ __forceinline__ unsigned asu(float f) { return __builtin_bit_cast(unsigned, f); }
template <int C> __device__ __forceinline__ float dpp(float x) {
  return __builtin_bit_cast(float, __builtin_amdgcn_mov_dpp(__builtin_bit_cast(int, x), C, 0xf, 0xf, true));
}
constexpr int XOR1 = 0xB1, XOR2 = 0x4E, HMIR = 0x141, MIR = 0x140, ROR4 = 0x124, ROR8 = 0x128;
__device__ __forceinline__ unsigned cvtpk(float lo, float hi) {
  f32x2 v = {lo, hi};
  bf16x2 r = __builtin_convertvector(v, bf16x2);
  return __builtin_bit_cast(unsigned, r);
}
__device__ __forceinline__ float lo16(unsigned u) { return asf(u << 16); }
__device__ __forceinline__ float hi16(unsigned u) { return asf(u & 0xffff0000u); }
__device__ __forceinline__ float rbf(float x) { return lo16(cvtpk(x, x)); }
__device__ __forceinline__ float xr16_max(float x) {
  auto s = __builtin_amdgcn_permlane16_swap(asu(x), asu(x), false, false);
  x = fmaxf(asf(s[0]), asf(s[1]));
  auto t = __builtin_amdgcn_permlane32_swap(asu(x), asu(x), false, false);
  return fmaxf(asf(t[0]), asf(t[1]));
}
__device__ __forceinline__ float xr16_sum(float x) {
  auto s = __builtin_amdgcn_permlane16_swap(asu(x), asu(x), false, false);
  x = asf(s[0]) + asf(s[1]);
  auto t = __builtin_amdgcn_permlane32_swap(asu(x), asu(x), false, false);
  return asf(t[0]) + asf(t[1]);
}
__device__ __forceinline__ float row_sum(float x) {
  x += dpp<XOR1>(x); x += dpp<XOR2>(x); x += dpp<HMIR>(x); x += dpp<MIR>(x); return x;
}
__device__ __forceinline__ float row_max(float x) {
  x = fmaxf(x, dpp<XOR1>(x)); x = fmaxf(x, dpp<XOR2>(x)); x = fmaxf(x, dpp<HMIR>(x)); x = fmaxf(x, dpp<MIR>(x)); return x;
}
__device__ __forceinline__ float wave_sum(float x) { return xr16_sum(row_sum(x)); }
__device__ __forceinline__ f32x4 mfma32(v4u a, v4u b, f32x4 c) {
  return __builtin_amdgcn_mfma_f32_16x16x32_bf16(__builtin_bit_cast(bf16x8, a), __builtin_bit_cast(bf16x8, b), c, 0, 0, 0);
}
__device__ __forceinline__ f32x4 mfma16(v2u a, unsigned b0, unsigned b1, f32x4 c) {
  v2u b = {b0, b1};
  return __builtin_amdgcn_mfma_f32_16x16x16bf16_1k(__builtin_bit_cast(s16x4, a), __builtin_bit_cast(s16x4, b), c, 0, 0, 0);
}
template <int AUX> __device__ __forceinline__ v4u ld16(rsrc_t r, int voff, int soff) {
  return __builtin_amdgcn_raw_buffer_load_b128(r, voff, soff, AUX);
}
__device__ __forceinline__ void st16(rsrc_t r, int voff, v4u v) { __builtin_amdgcn_raw_buffer_store_b128(v, r, voff, 0, AUX_SC1); }
__device__ __forceinline__ void st8(rsrc_t r, int voff, v2u v) { __builtin_amdgcn_raw_buffer_store_b64(v, r, voff, 0, AUX_SC1); }
#define WG_BARRIER() asm volatile("s_waitcnt lgkmcnt(0)\n\ts_barrier" ::: "memory")
#define LDS_WAIT() asm volatile("s_waitcnt lgkmcnt(0)" ::: "memory")
#define SB() __builtin_amdgcn_sched_barrier(0)

__device__ __forceinline__ v4u layernorm8(v4u p) {
  float x[8];
#pragma unroll
  for (int i = 0; i < 4; ++i) { x[2 * i] = lo16(p[i]); x[2 * i + 1] = hi16(p[i]); }
  float s = ((x[0] + x[1]) + (x[2] + x[3])) + ((x[4] + x[5]) + (x[6] + x[7]));
  const float mean = wave_sum(s) * (1.f / DM);
  float q = 0.f;
#pragma unroll
  for (int i = 0; i < 8; ++i) { const float d = x[i] - mean; q = fmaf(d, d, q); }
  const float var = wave_sum(q) * (1.f / DM);
  const float mb = rbf(mean);
  const float den = rbf(sqrtf(rbf(rbf(var) + 1e-5f)));
  v4u r;
#pragma unroll
  for (int i = 0; i < 4; ++i) r[i] = cvtpk(rbf(x[2 * i] - mb) / den, rbf(x[2 * i + 1] - mb) / den);
  return r;
}
struct LnStat { float mb, rden; float den; };
__device__ __forceinline__ LnStat ln_stats(v4u p) {
  float x[8];
#pragma unroll
  for (int i = 0; i < 4; ++i) { x[2 * i] = lo16(p[i]); x[2 * i + 1] = hi16(p[i]); }
  float s = ((x[0] + x[1]) + (x[2] + x[3])) + ((x[4] + x[5]) + (x[6] + x[7]));
  const float mean = wave_sum(s) * (1.f / DM);
  float q = 0.f;
#pragma unroll
  for (int i = 0; i < 8; ++i) { const float d = x[i] - mean; q = fmaf(d, d, q); }
  const float var = wave_sum(q) * (1.f / DM);
  LnStat r; r.mb = rbf(mean); r.den = rbf(sqrtf(rbf(rbf(var) + 1e-5f))); r.rden = 0.f;
  return r;
}
__device__ __forceinline__ unsigned ln_pair(unsigned p, LnStat st) { return cvtpk(rbf(lo16(p) - st.mb) / st.den, rbf(hi16(p) - st.mb) / st.den); }
__device__ __forceinline__ unsigned pick4(v4u v, int w) {
  const unsigned a = (w & 1) ? v[1] : v[0], b = (w & 1) ? v[3] : v[2];
  return (w & 2) ? b : a;
}
__device__ __forceinline__ float gemv16(unsigned xp, v4u a0, v4u a1, v4u b0, v4u b1, int lane) {
  float v[16];
  const bf16x2 xb = __builtin_bit_cast(bf16x2, xp);
#pragma unroll
  for (int i = 0; i < 4; ++i) {
    const unsigned alo = __builtin_amdgcn_perm(a1[i], a0[i], 0x05040100u), ahi = __builtin_amdgcn_perm(a1[i], a0[i], 0x07060302u);
    const unsigned blo = __builtin_amdgcn_perm(b1[i], b0[i], 0x05040100u), bhi = __builtin_amdgcn_perm(b1[i], b0[i], 0x07060302u);
    v[2 * i] = __builtin_amdgcn_fdot2_f32_bf16(__builtin_bit_cast(bf16x2, alo), xb, 0.f, false);
    v[2 * i + 1] = __builtin_amdgcn_fdot2_f32_bf16(__builtin_bit_cast(bf16x2, ahi), xb, 0.f, false);
    v[8 + 2 * i] = __builtin_amdgcn_fdot2_f32_bf16(__builtin_bit_cast(bf16x2, blo), xb, 0.f, false);
    v[8 + 2 * i + 1] = __builtin_amdgcn_fdot2_f32_bf16(__builtin_bit_cast(bf16x2, bhi), xb, 0.f, false);
  }
  float u[8];
#pragma unroll
  for (int k = 0; k < 8; ++k) { auto s = __builtin_amdgcn_permlane32_swap(asu(v[k]), asu(v[k + 8]), false, false); u[k] = asf(s[0]) + asf(s[1]); }
  float t[4];
#pragma unroll
  for (int k = 0; k < 4; ++k) { auto s = __builtin_amdgcn_permlane16_swap(asu(u[k]), asu(u[k + 4]), false, false); t[k] = asf(s[0]) + asf(s[1]); }
  float y[2];
#pragma unroll
  for (int k = 0; k < 2; ++k) { const float p = t[k] + dpp<ROR8>(t[k]), q = t[k + 2] + dpp<ROR8>(t[k + 2]); y[k] = (lane & 8) ? q : p; }
#pragma unroll
  for (int k = 0; k < 2; ++k) { y[k] += dpp<XOR1>(y[k]); y[k] += dpp<XOR2>(y[k]); y[k] += dpp<HMIR>(y[k]); }
  return (lane & 4) ? y[1] : y[0];
}

struct Tile { v4u k[8], v[8]; };

template <bool REFILL>
__device__ __forceinline__ void tile_step(Tile &t, v4u q, rsrc_t rk, rsrc_t rv, int koff, int voff, int snext, unsigned mk0, unsigned mk1,
                                          float &m2, float &lsum, f32x4 &acc0, f32x4 &acc1) {
  float s[8];
  const f32x4 z = {0.f, 0.f, 0.f, 0.f};
  f32x4 d[8];
#pragma unroll
  for (int g = 0; g < 8; ++g) {
    d[g] = mfma32(t.k[g], q, z);
    if (REFILL) t.k[g] = ld16<KAUX>(rk, koff, snext + g * 1024);
    SB();
  }
#pragma unroll
  for (int g = 0; g < 8; ++g) {
    s[g] = ((dpp<0x00>(d[g][0]) + dpp<0x55>(d[g][1])) + dpp<0xAA>(d[g][2])) + dpp<0xFF>(d[g][3]);
    asm("" : "+v"(s[g]));
  }
#pragma unroll
  for (int g = 0; g < 8; g += 2) {
    const unsigned a = cvtpk(s[g], s[g + 1]);
    const unsigned b = cvtpk(lo16(a) * SCALE, hi16(a) * SCALE);
    s[g] = lo16(b); s[g + 1] = hi16(b);
  }
  float mx = fmaxf(fmaxf(fmaxf(s[0], s[1]), fmaxf(s[2], s[3])), fmaxf(fmaxf(s[4], s[5]), fmaxf(s[6], s[7])));
  mx = xr16_max(mx);
  const float mn = fmaxf(m2, mx * LOG2E);
  const float alpha = __builtin_amdgcn_exp2f(m2 - mn);
  m2 = mn;
  lsum *= alpha;
  int ai = __builtin_bit_cast(int, alpha), ar;
  ar = __builtin_amdgcn_update_dpp(ai, ai, 0x150, 0x1, 0xf, false);
  ar = __builtin_amdgcn_update_dpp(ar, ai, 0x154, 0x2, 0xf, false);
  ar = __builtin_amdgcn_update_dpp(ar, ai, 0x158, 0x4, 0xf, false);
  ar = __builtin_amdgcn_update_dpp(ar, ai, 0x15c, 0x8, 0xf, false);
  const float al = __builtin_bit_cast(float, ar);
  acc0 *= al; acc1 *= al;
#pragma unroll
  for (int g = 0; g < 8; ++g) {
    const float p = __builtin_amdgcn_exp2f(fmaf(s[g], LOG2E, -mn));
    lsum += p;
    const unsigned pp = cvtpk(p, p);
    const v2u a = {pp & mk0, pp & mk1};
    acc0 = mfma16(a, t.v[g][0], t.v[g][1], acc0);
    acc1 = mfma16(a, t.v[g][2], t.v[g][3], acc1);
    if (REFILL) t.v[g] = ld16<VAUX>(rv, voff, snext + g * 1024);
    SB();
  }
}

template <int N>
__device__ __forceinline__ void gather(rsrc_t rs, const int (&off)[N], unsigned tag, v4u (&x)[N]) {
  for (unsigned spins = 0;; ++spins) {
    bool ok = true;
#pragma unroll
    for (int i = 0; i < N; ++i) x[i] = ld16<AUX_SC1>(rs, off[i], 0);
#pragma unroll
    for (int i = 0; i < N; ++i) ok = ok && x[i][1] == tag && x[i][3] == tag;
    if (__all(ok) || spins > SPIN_CAP) break;
    __builtin_amdgcn_s_sleep(1);
  }
}
template <int S> __device__ __forceinline__ float strided_sum(float x) {
  if (S <= 1) x += dpp<XOR1>(x);
  if (S <= 2) x += dpp<XOR2>(x);
  if (S <= 8) x += dpp<ROR8>(x);
  if (S <= 4) x += dpp<ROR4>(x);
  return xr16_sum(x);
}
template <int C>
__device__ __forceinline__ void publish512(rsrc_t rs, int base, int nprod, int p, f32x4 a0, f32x4 a1, LAS float *tr, int w, int lane, unsigned tag) {
  if (lane < 16) {
    *(LAS f32x4 *)(tr + w * 128 + 8 * lane) = a0;
    *(LAS f32x4 *)(tr + w * 128 + 8 * lane + 4) = a1;
  }
  LDS_WAIT();
  const f32x2 y = *(LAS f32x2 *)(tr + w * 128 + 2 * lane);
  const int col = 128 * w + 2 * lane, j = col / C, within = col % C;
  const v4u g = {asu(y[0]), tag, asu(y[1]), tag};
  st16(rs, base + ((j * nprod + p) * C + within) * 8, g);
}
template <int C, int NP>
__device__ __forceinline__ f32x2 reduce_cols(rsrc_t rs, int base, int j, unsigned tag, LAS float *red, int tid, int w, int lane) {
  constexpr int S = C / 2, NPIECE = NP * S, N = (NPIECE + NTHR - 1) / NTHR;
  int off[N]; v4u x[N];
#pragma unroll
  for (int i = 0; i < N; ++i) { const int pc = i * NTHR + tid; off[i] = base + j * (NP * C * 8) + (pc < NPIECE ? pc : tid) * 16; }
  gather<N>(rs, off, tag, x);
  float s0 = 0.f, s1 = 0.f;
#pragma unroll
  for (int i = 0; i < N; ++i) { const bool live = i * NTHR + tid < NPIECE; s0 += live ? asf(x[i][0]) : 0.f; s1 += live ? asf(x[i][2]) : 0.f; }
  s0 = strided_sum<S>(s0); s1 = strided_sum<S>(s1);
  if (lane < S) { f32x2 y = {s0, s1}; *(LAS f32x2 *)(red + w * 64 + 2 * lane) = y; }
  WG_BARRIER();
  f32x2 r = {0.f, 0.f};
#pragma unroll
  for (int ww = 0; ww < 4; ++ww) r += *(LAS f32x2 *)(red + ww * 64 + 2 * (lane % S));
  return r;
}

#define STAMP_AT(i) do { } while (0)
#define STAMP_FLUSH() do { } while (0)

constexpr int L_MRG = 0;
constexpr int L_ML = L_MRG + 2048;
constexpr int L_RED = L_ML + 32;
constexpr int L_RED2 = L_RED + 256;
constexpr int L_TR = L_RED2 + 256;
constexpr int L_Q = L_TR + 512;
constexpr int L_XN = L_Q + 256;
constexpr int L_END = L_XN + 256;
constexpr int LDS_BYTES = 84 * 1024;

__global__ void __launch_bounds__(NTHR, 2) fused_decode_kernel(Params p) {
  extern __shared__ __attribute__((aligned(16))) float smem[];
  LAS float *L = (LAS float *)smem;
  const int tid = threadIdx.x, lane = tid & 63, w = __builtin_amdgcn_readfirstlane(tid >> 6), b = blockIdx.x;
  const unsigned long long tstart = __builtin_amdgcn_s_memrealtime();
  STAMP_AT(0);
  const int kh = b >> 5, sp = b & 31;
  const rsrc_t rws = __builtin_amdgcn_make_buffer_rsrc(p.ws, 0, WS_USED, RS3);
  const rsrc_t rk = __builtin_amdgcn_make_buffer_rsrc((char *)p.k + ((size_t)kh * SK + sp * 2048) * 256, 0, 2048 * 256, RS3);
  const rsrc_t rv = __builtin_amdgcn_make_buffer_rsrc((char *)p.v + ((size_t)kh * SK + sp * 2048) * 256, 0, 2048 * 256, RS3);
  const rsrc_t rx = __builtin_amdgcn_make_buffer_rsrc((void *)p.x, 0, DM * 2, RS3);
  const rsrc_t rwq = __builtin_amdgcn_make_buffer_rsrc((void *)p.wq, 0, DM * 4096 * 2, RS3);
  const unsigned epoch = *(const __attribute__((address_space(4))) unsigned *)((const char *)p.ws + WS_EPOCH);
  const v4u xr = ld16<0>(rx, lane * 16, 0);
  const int qf = (b & 7) * 32 + (b >> 3);
  const unsigned xraw = __builtin_amdgcn_raw_buffer_load_b32(rx, (64 * w + lane) * 4, 0, 0);
  const int wqoff = (128 * w + 2 * lane) * 8192 + qf * 32;
  const v4u wa0 = ld16<WAUX>(rwq, wqoff, 0), wa1 = ld16<WAUX>(rwq, wqoff, 8192);
  const v4u wb0 = ld16<WAUX>(rwq, wqoff, 16), wb1 = ld16<WAUX>(rwq, wqoff, 8192 + 16);
  SB();
  asm volatile("s_barrier" ::: "memory");
  SB();
  const int g4 = lane >> 4, a4 = (lane >> 2) & 3, u4 = lane & 3;
  const int koff = a4 * 256 + (4 * g4 + u4) * 16, voff = lane * 16;
  Tile ta, tb;
#pragma unroll
  for (int g = 0; g < 8; ++g) ta.k[g] = ld16<KAUX>(rk, koff, w * 8192 + g * 1024);
#pragma unroll
  for (int g = 0; g < 8; ++g) ta.v[g] = ld16<VAUX>(rv, voff, w * 8192 + g * 1024);
  SB();
  const unsigned tag = epoch + 1u;
  {
    unsigned xsel = ln_pair(xraw, ln_stats(xr));
    const float r = gemv16(xsel, wa0, wa1, wb0, wb1, lane);
    if ((lane & 3) == 0) L[L_RED + w * 64 + (lane >> 2)] = r;
    WG_BARRIER();
    if (w == 0) {
      const int i = lane & 7;
      float s0 = 0.f, s1 = 0.f;
#pragma unroll
      for (int ww = 0; ww < 4; ++ww) { const f32x2 y = *(LAS f32x2 *)(L + L_RED + ww * 64 + 2 * i); s0 += y[0]; s1 += y[1]; }
      const v2u g = {cvtpk(s0, s1), tag};
      if (lane < 8 * QREP) st8(rws, WS_QG + (lane >> 3) * 16384 + ((b & 7) * 256 + (b >> 3) * 8 + i) * 8, g);
    }
  }
  STAMP_AT(1);
  asm volatile("s_waitcnt vmcnt(9)" ::: "memory");
  v4u q;
  {
    const int qoff = WS_QG + (b & (QREP - 1)) * 16384 + (kh * 256 + w * 64) * 8 + (lane & 31) * 16;
    v4u x0 = ld16<AUX_SC1>(rws, qoff, 0);
    SB();
#pragma unroll
    for (int g = 0; g < 8; ++g) tb.k[g] = ld16<KAUX>(rk, koff, (4 + w) * 8192 + g * 1024);
#pragma unroll
    for (int g = 0; g < 8; ++g) tb.v[g] = ld16<VAUX>(rv, voff, (4 + w) * 8192 + g * 1024);
    SB();
    bool ok = x0[1] == tag && x0[3] == tag;
    for (unsigned spins = 0; !__all(ok) && spins < SPIN_CAP; ++spins) {
      x0 = ld16<AUX_SC1>(rws, qoff, 0);
      ok = x0[1] == tag && x0[3] == tag;
    }
    if (lane < 32) *(LAS v2u *)(L + L_Q + w * 64 + 2 * lane) = v2u{x0[0], x0[2]};
    WG_BARRIER();
    q = *(LAS v4u *)(L + L_Q + a4 * 64 + (4 * g4 + u4) * 4);
  }
  STAMP_AT(2);
  const int pf = (b & 7) * 28 + (b >> 3);
  const bool hasf = (b >> 3) < 28;
  const rsrc_t rwg = __builtin_amdgcn_make_buffer_rsrc((void *)p.wg, 0, hasf ? DM * FF * 2 : 0, RS3);
  const rsrc_t rwu = __builtin_amdgcn_make_buffer_rsrc((void *)p.wu, 0, hasf ? DM * FF * 2 : 0, RS3);
  const int go = (128 * w + 2 * lane) * (FF * 2) + pf * 16;
  const v4u wg0 = ld16<WAUX>(rwg, go, 0), wg1 = ld16<WAUX>(rwg, go, FF * 2);
  const v4u wu0 = ld16<WAUX>(rwu, go, 0), wu1 = ld16<WAUX>(rwu, go, FF * 2);
  SB();
  const unsigned mk0 = u4 == 0 ? 0xFFFFu : u4 == 1 ? 0xFFFF0000u : 0u;
  const unsigned mk1 = u4 == 2 ? 0xFFFFu : u4 == 3 ? 0xFFFF0000u : 0u;
  float m2 = -3.0e38f, lsum = 0.f;
  f32x4 acc0 = {0.f, 0.f, 0.f, 0.f}, acc1 = acc0;
  for (int it = 0; it < 7; ++it) {
    const int s0 = ((2 * it + 2) * 4 + w) * 8192;
    tile_step<true>(ta, q, rk, rv, koff, voff, s0, mk0, mk1, m2, lsum, acc0, acc1);
    tile_step<true>(tb, q, rk, rv, koff, voff, s0 + 4 * 8192, mk0, mk1, m2, lsum, acc0, acc1);
  }
  const rsrc_t rwo = __builtin_amdgcn_make_buffer_rsrc((char *)p.wo + (size_t)b * 16384, 0, 16384, RS3);
  const rsrc_t rwd = __builtin_amdgcn_make_buffer_rsrc((char *)p.wd + (size_t)(hasf ? pf : 0) * 8192, 0, hasf ? 8192 : 0, RS3);
  v4u wo[4], wd[2];
  {
    const int o = g4 * 1024 + w * 256 + (lane & 15) * 16;
#pragma unroll
    for (int i = 0; i < 4; ++i) wo[i] = ld16<WAUX>(rwo, o, i * 4096);
#pragma unroll
    for (int i = 0; i < 2; ++i) wd[i] = ld16<WAUX>(rwd, o, i * 4096);
    SB();
  }
  tile_step<false>(ta, q, rk, rv, koff, voff, 0, mk0, mk1, m2, lsum, acc0, acc1);
  tile_step<false>(tb, q, rk, rv, koff, voff, 0, mk0, mk1, m2, lsum, acc0, acc1);
  STAMP_AT(3);
  {
    const float lw = xr16_sum(lsum);
    *(LAS f32x4 *)(L + L_MRG + w * 512 + g4 * 128 + 8 * (lane & 15)) = acc0;
    *(LAS f32x4 *)(L + L_MRG + w * 512 + g4 * 128 + 8 * (lane & 15) + 4) = acc1;
    if (lane < 16 && u4 == 0) { f32x2 y = {m2, lw}; *(LAS f32x2 *)(L + L_ML + (w * 4 + a4) * 2) = y; }
    WG_BARRIER();
    f32x2 ml[4];
#pragma unroll
    for (int ww = 0; ww < 4; ++ww) ml[ww] = *(LAS f32x2 *)(L + L_ML + (ww * 4 + w) * 2);
    const float M = fmaxf(fmaxf(ml[0][0], ml[1][0]), fmaxf(ml[2][0], ml[3][0]));
    float Ls = 0.f; f32x2 o = {0.f, 0.f};
#pragma unroll
    for (int ww = 0; ww < 4; ++ww) {
      const float e = __builtin_amdgcn_exp2f(ml[ww][0] - M);
      Ls = fmaf(e, ml[ww][1], Ls);
      o += e * *(LAS f32x2 *)(L + L_MRG + ww * 512 + w * 128 + 2 * lane);
    }
    const int head = 4 * kh + w;
    st16(rws, WS_OP + (((head * 8 + (lane >> 3)) * 32 + sp) * 16 + 2 * (lane & 7)) * 8, v4u{asu(o[0]), tag, asu(o[1]), tag});
    if (lane < 8) st16(rws, WS_ML + ((head * 8 + lane) * 32 + sp) * 16, v4u{asu(M), tag, asu(Ls), tag});
  }
  STAMP_AT(4);
  {
    const int off[2] = {WS_OP + b * 4096 + tid * 16, WS_ML + (b * 32 + 8 * w + (lane >> 3)) * 16};
    v4u x[2];
    gather<2>(rws, off, tag, x);
    STAMP_AT(5);
    const float ms = asf(x[1][0]), ls = asf(x[1][2]);
    const float Mw = xr16_max(fmaxf(ms, dpp<ROR8>(ms)));
    const float e = __builtin_amdgcn_exp2f(ms - Mw);
    const float v0 = strided_sum<8>(e * asf(x[0][0])), v1 = strided_sum<8>(e * asf(x[0][2])), lw = strided_sum<8>(e * ls);
    if (lane < 8) {
      const int d = 2 * lane;
      L[L_RED2 + w * 64 + (d & 3) * 4 + (d >> 2)] = v0;
      L[L_RED2 + w * 64 + ((d + 1) & 3) * 4 + (d >> 2)] = v1;
      if (lane == 0) *(LAS f32x2 *)(L + L_RED2 + w * 64 + 32) = f32x2{Mw, lw};
    }
    WG_BARRIER();
    f32x2 ml[4];
#pragma unroll
    for (int ww = 0; ww < 4; ++ww) ml[ww] = *(LAS f32x2 *)(L + L_RED2 + ww * 64 + 32);
    const float M = fmaxf(fmaxf(ml[0][0], ml[1][0]), fmaxf(ml[2][0], ml[3][0]));
    f32x4 o = {0.f, 0.f, 0.f, 0.f};
    float Lt = 0.f;
#pragma unroll
    for (int ww = 0; ww < 4; ++ww) {
      const float ew = __builtin_amdgcn_exp2f(ml[ww][0] - M);
      Lt = fmaf(ew, ml[ww][1], Lt);
      o += ew * *(LAS f32x4 *)(L + L_RED2 + ww * 64 + g4 * 4);
    }
    o *= __builtin_amdgcn_rcpf(Lt);
    f32x4 c0 = {0.f, 0.f, 0.f, 0.f}, c1 = c0;
#pragma unroll
    for (int i = 0; i < 4; ++i) {
      const unsigned pp = cvtpk(o[i], o[i]);
      const v2u a = {pp & mk0, pp & mk1};
      c0 = mfma16(a, wo[i][0], wo[i][1], c0);
      c1 = mfma16(a, wo[i][2], wo[i][3], c1);
    }
    publish512<512 / NB1>(rws, WS_PB, 256, b, c0, c1, L + L_TR, w, lane, tag);
  }
  STAMP_AT(6);
  if (b < NB1) {
    constexpr int C = 512 / NB1, S = C / 2;
    const unsigned xp = __builtin_amdgcn_raw_buffer_load_b32(rx, (b * C + 2 * (lane % S)) * 2, 0, 0);
    const f32x2 r = reduce_cols<C, 256>(rws, WS_PB, b, tag, L + L_RED, tid, w, lane);
    STAMP_AT(7);
    const unsigned hp = cvtpk(lo16(xp) + rbf(r[0]), hi16(xp) + rbf(r[1]));
    if (tid < S * REP) st8(rws, WS_HG + (tid / S) * 2048 + (b * S + tid % S) * 8, v2u{hp, tag});
  }
  STAMP_AT(8);
  if (w == 0) {
    const int base = WS_HG + (b % REP) * 2048 + lane * 32;
    const int off[2] = {base, base + 16};
    v4u x[2];
    gather<2>(rws, off, tag, x);
    const v4u hb = v4u{x[0][0], x[0][2], x[1][0], x[1][2]};
    *(LAS v4u *)(L + L_Q + 4 * lane) = hb;
    *(LAS v4u *)(L + L_XN + 4 * lane) = layernorm8(hb);
  }
  WG_BARRIER();
  STAMP_AT(9);
  if (hasf) {
    const unsigned xsel = ((LAS unsigned *)L)[L_XN + 64 * w + lane];
    const float r = gemv16(xsel, wg0, wg1, wu0, wu1, lane);
    if ((lane & 3) == 0) { const int vi = lane >> 2, col = vi & 7; L[L_RED2 + w * 64 + 16 + (col & 3) * 4 + (vi >> 3) * 2 + (col >> 2)] = r; }
    WG_BARRIER();
    f32x4 gu = {0.f, 0.f, 0.f, 0.f};
#pragma unroll
    for (int ww = 0; ww < 4; ++ww) gu += *(LAS f32x4 *)(L + L_RED2 + ww * 64 + 16 + g4 * 4);
    f32x4 c0 = {0.f, 0.f, 0.f, 0.f}, c1 = c0;
#pragma unroll
    for (int i = 0; i < 2; ++i) {
      const float hv = rbf(gu[i]) * rbf(gu[2 + i]);
      const unsigned pp = cvtpk(hv, hv);
      const v2u a = {pp & mk0, pp & mk1};
      c0 = mfma16(a, wd[i][0], wd[i][1], c0);
      c1 = mfma16(a, wd[i][2], wd[i][3], c1);
    }
    publish512<512 / ND>(rws, WS_PD, NPF, pf, c0, c1, L + L_TR, w, lane, tag);
  }
  STAMP_AT(10);
  if (b >= NWG - ND) {
    constexpr int C = 512 / ND, S = C / 2;
    const int j = b - (NWG - ND);
    const unsigned hp = ((LAS unsigned *)L)[L_Q + j * S + lane % S];
    WG_BARRIER();
    const f32x2 r = reduce_cols<C, NPF>(rws, WS_PD, j, tag, L + L_RED, tid, w, lane);
    const unsigned op = cvtpk(lo16(hp) + rbf(r[0]), hi16(hp) + rbf(r[1]));
    if (w == 0 && lane < S) ((unsigned *)p.out)[j * S + lane] = op;
    if (j == 0 && tid == 0) __builtin_amdgcn_raw_buffer_store_b32(tag, rws, WS_EPOCH, 0, AUX_SC1);
  }
  STAMP_AT(11);
  STAMP_FLUSH();
  { while (__builtin_amdgcn_s_memrealtime() - tstart < 7000) __builtin_amdgcn_s_sleep(2);
    if (w == 0) { __builtin_amdgcn_fence(__ATOMIC_RELEASE, "agent"); asm volatile("s_waitcnt vmcnt(0)" ::: "memory"); __builtin_amdgcn_fence(__ATOMIC_ACQUIRE, "agent"); asm volatile("s_waitcnt vmcnt(0)" ::: "memory"); }
    while (__builtin_amdgcn_s_memrealtime() - tstart < SPINTO) __builtin_amdgcn_s_sleep(2); }
}
}

extern "C" void kernel_launch(void *const *d_in, const int *in_sizes, int n_in, void *d_out, int out_size, void *d_ws, size_t ws_size,
                              hipStream_t stream) {
  Params p;
  p.x = d_in[0]; p.k = d_in[1]; p.v = d_in[2]; p.wq = d_in[3]; p.wo = d_in[4];
  p.wg = d_in[6]; p.wu = d_in[7]; p.wd = d_in[8];
  p.out = d_out; p.ws = d_ws;
  (void)hipFuncSetAttribute((const void *)fused_decode_kernel, hipFuncAttributeMaxDynamicSharedMemorySize, LDS_BYTES);
  fused_decode_kernel<<<NWG, NTHR, LDS_BYTES, stream>>>(p);
}
```

```cpp
#include <hip/hip_runtime.h>
#include <stdint.h>

#define STAMP 0
#define NB1 64
#define ND 32
#define REP 32
#define KAUX 2
#define VAUX 2
#define WAUX 2
#define PINARGS 1
#define QREP 4

namespace {
typedef unsigned v4u __attribute__((ext_vector_type(4)));
typedef unsigned v2u __attribute__((ext_vector_type(2)));
typedef float f32x4 __attribute__((ext_vector_type(4)));
typedef float f32x2 __attribute__((ext_vector_type(2)));
typedef short s16x4 __attribute__((ext_vector_type(4)));
typedef __bf16 bf16x8 __attribute__((ext_vector_type(8)));
typedef __bf16 bf16x2 __attribute__((ext_vector_type(2)));
typedef __amdgpu_buffer_rsrc_t rsrc_t;
#define LAS __attribute__((address_space(3)))

constexpr int DM = 512, SK = 65536, DH = 128, FF = 1792;
constexpr int NWG = 256, NTHR = 256;
constexpr int NPF = 224;
constexpr int AUX_NT = 2, AUX_SC1 = 16;
constexpr int RS3 = 0x00020000;
constexpr int WS_EPOCH = 0;
constexpr int WS_QG = 4096;
constexpr int WS_ML = WS_QG + 8 * 16384;
constexpr int WS_HG = WS_ML + 131072;
constexpr int WS_OP = 1 << 20;
constexpr int WS_PB = 2 << 20;
constexpr int WS_PD = 3 << 20;
constexpr int WS_USED = 4 << 20;
constexpr int WS_STAMP = 8 << 20;
constexpr unsigned SPIN_CAP = 1u << 14;
constexpr float LOG2E = 1.4426950408889634f;
constexpr float SCALE = 0.08838834764831845f;

struct Params {
  const void *x, *k, *v, *wq, *wo, *wg, *wu, *wd;
  void *out, *ws;
};

__device__ __forceinline__ float asf(unsigned u) { return __builtin_bit_cast(float, u); }
__device__ __forceinline__ unsigned asu(float f) { return __builtin_bit_cast(unsigned, f); }
template <int C> __device__ __forceinline__ float dpp(float x) {
  return __builtin_bit_cast(float, __builtin_amdgcn_mov_dpp(__builtin_bit_cast(int, x), C, 0xf, 0xf, true));
}
constexpr int XOR1 = 0xB1, XOR2 = 0x4E, HMIR = 0x141, MIR = 0x140, ROR4 = 0x124, ROR8 = 0x128;
__device__ __forceinline__ unsigned cvtpk(float lo, float hi) {
  f32x2 v = {lo, hi};
  bf16x2 r = __builtin_convertvector(v, bf16x2);
  return __builtin_bit_cast(unsigned, r);
}
__device__ __forceinline__ float lo16(unsigned u) { return asf(u << 16); }
__device__ __forceinline__ float hi16(unsigned u) { return asf(u & 0xffff0000u); }
__device__ __forceinline__ float rbf(float x) { return lo16(cvtpk(x, x)); }
__device__ __forceinline__ float xr16_max(float x) {
  auto s = __builtin_amdgcn_permlane16_swap(asu(x), asu(x), false, false);
  x = fmaxf(asf(s[0]), asf(s[1]));
  auto t = __builtin_amdgcn_permlane32_swap(asu(x), asu(x), false, false);
  return fmaxf(asf(t[0]), asf(t[1]));
}
__device__ __forceinline__ float xr16_sum(float x) {
  auto s = __builtin_amdgcn_permlane16_swap(asu(x), asu(x), false, false);
  x = asf(s[0]) + asf(s[1]);
  auto t = __builtin_amdgcn_permlane32_swap(asu(x), asu(x), false, false);
  return asf(t[0]) + asf(t[1]);
}
__device__ __forceinline__ float row_sum(float x) {
  x += dpp<XOR1>(x); x += dpp<XOR2>(x); x += dpp<HMIR>(x); x += dpp<MIR>(x); return x;
}
__device__ __forceinline__ float row_max(float x) {
  x = fmaxf(x, dpp<XOR1>(x)); x = fmaxf(x, dpp<XOR2>(x)); x = fmaxf(x, dpp<HMIR>(x)); x = fmaxf(x, dpp<MIR>(x)); return x;
}
__device__ __forceinline__ float wave_sum(float x) { return xr16_sum(row_sum(x)); }
__device__ __forceinline__ f32x4 mfma32(v4u a, v4u b, f32x4 c) {
  return __builtin_amdgcn_mfma_f32_16x16x32_bf16(__builtin_bit_cast(bf16x8, a), __builtin_bit_cast(bf16x8, b), c, 0, 0, 0);
}
__device__ __forceinline__ f32x4 mfma16(v2u a, unsigned b0, unsigned b1, f32x4 c) {
  v2u b = {b0, b1};
  return __builtin_amdgcn_mfma_f32_16x16x16bf16_1k(__builtin_bit_cast(s16x4, a), __builtin_bit_cast(s16x4, b), c, 0, 0, 0);
}
template <int AUX> __device__ __forceinline__ v4u ld16(rsrc_t r, int voff, int soff) {
  return __builtin_amdgcn_raw_buffer_load_b128(r, voff, soff, AUX);
}
__device__ __forceinline__ void st16(rsrc_t r, int voff, v4u v) { __builtin_amdgcn_raw_buffer_store_b128(v, r, voff, 0, AUX_SC1); }
__device__ __forceinline__ void st8(rsrc_t r, int voff, v2u v) { __builtin_amdgcn_raw_buffer_store_b64(v, r, voff, 0, AUX_SC1); }
#define WG_BARRIER() asm volatile("s_waitcnt lgkmcnt(0)\n\ts_barrier" ::: "memory")
#define LDS_WAIT() asm volatile("s_waitcnt lgkmcnt(0)" ::: "memory")
#define SB() __builtin_amdgcn_sched_barrier(0)

__device__ __forceinline__ v4u layernorm8(v4u p) {
  float x[8];
#pragma unroll
  for (int i = 0; i < 4; ++i) { x[2 * i] = lo16(p[i]); x[2 * i + 1] = hi16(p[i]); }
  float s = ((x[0] + x[1]) + (x[2] + x[3])) + ((x[4] + x[5]) + (x[6] + x[7]));
  const float mean = wave_sum(s) * (1.f / DM);
  float q = 0.f;
#pragma unroll
  for (int i = 0; i < 8; ++i) { const float d = x[i] - mean; q = fmaf(d, d, q); }
  const float var = wave_sum(q) * (1.f / DM);
  const float mb = rbf(mean);
  const float den = rbf(sqrtf(rbf(rbf(var) + 1e-5f)));
  v4u r;
#pragma unroll
  for (int i = 0; i < 4; ++i) r[i] = cvtpk(rbf(x[2 * i] - mb) / den, rbf(x[2 * i + 1] - mb) / den);
  return r;
}
struct LnStat { float mb, rden; float den; };
__device__ __forceinline__ LnStat ln_stats(v4u p) {
  float x[8];
#pragma unroll
  for (int i = 0; i < 4; ++i) { x[2 * i] = lo16(p[i]); x[2 * i + 1] = hi16(p[i]); }
  float s = ((x[0] + x[1]) + (x[2] + x[3])) + ((x[4] + x[5]) + (x[6] + x[7]));
  const float mean = wave_sum(s) * (1.f / DM);
  float q = 0.f;
#pragma unroll
  for (int i = 0; i < 8; ++i) { const float d = x[i] - mean; q = fmaf(d, d, q); }
  const float var = wave_sum(q) * (1.f / DM);
  LnStat r; r.mb = rbf(mean); r.den = rbf(sqrtf(rbf(rbf(var) + 1e-5f))); r.rden = 0.f;
  return r;
}
__device__ __forceinline__ unsigned ln_pair(unsigned p, LnStat st) { return cvtpk(rbf(lo16(p) - st.mb) / st.den, rbf(hi16(p) - st.mb) / st.den); }
__device__ __forceinline__ unsigned pick4(v4u v, int w) {
  const unsigned a = (w & 1) ? v[1] : v[0], b = (w & 1) ? v[3] : v[2];
  return (w & 2) ? b : a;
}
__device__ __forceinline__ float gemv16(unsigned xp, v4u a0, v4u a1, v4u b0, v4u b1, int lane) {
  float v[16];
  const bf16x2 xb = __builtin_bit_cast(bf16x2, xp);
#pragma unroll
  for (int i = 0; i < 4; ++i) {
    const unsigned alo = __builtin_amdgcn_perm(a1[i], a0[i], 0x05040100u), ahi = __builtin_amdgcn_perm(a1[i], a0[i], 0x07060302u);
    const unsigned blo = __builtin_amdgcn_perm(b1[i], b0[i], 0x05040100u), bhi = __builtin_amdgcn_perm(b1[i], b0[i], 0x07060302u);
    v[2 * i] = __builtin_amdgcn_fdot2_f32_bf16(__builtin_bit_cast(bf16x2, alo), xb, 0.f, false);
    v[2 * i + 1] = __builtin_amdgcn_fdot2_f32_bf16(__builtin_bit_cast(bf16x2, ahi), xb, 0.f, false);
    v[8 + 2 * i] = __builtin_amdgcn_fdot2_f32_bf16(__builtin_bit_cast(bf16x2, blo), xb, 0.f, false);
    v[8 + 2 * i + 1] = __builtin_amdgcn_fdot2_f32_bf16(__builtin_bit_cast(bf16x2, bhi), xb, 0.f, false);
  }
  float u[8];
#pragma unroll
  for (int k = 0; k < 8; ++k) { auto s = __builtin_amdgcn_permlane32_swap(asu(v[k]), asu(v[k + 8]), false, false); u[k] = asf(s[0]) + asf(s[1]); }
  float t[4];
#pragma unroll
  for (int k = 0; k < 4; ++k) { auto s = __builtin_amdgcn_permlane16_swap(asu(u[k]), asu(u[k + 4]), false, false); t[k] = asf(s[0]) + asf(s[1]); }
  float y[2];
#pragma unroll
  for (int k = 0; k < 2; ++k) { const float p = t[k] + dpp<ROR8>(t[k]), q = t[k + 2] + dpp<ROR8>(t[k + 2]); y[k] = (lane & 8) ? q : p; }
#pragma unroll
  for (int k = 0; k < 2; ++k) { y[k] += dpp<XOR1>(y[k]); y[k] += dpp<XOR2>(y[k]); y[k] += dpp<HMIR>(y[k]); }
  return (lane & 4) ? y[1] : y[0];
}

struct Tile { v4u k[8], v[8]; };

template <bool REFILL>
__device__ __forceinline__ void tile_step(Tile &t, v4u q, rsrc_t rk, rsrc_t rv, int koff, int voff, int snext, unsigned mk0, unsigned mk1,
                                          float &m2, float &lsum, f32x4 &acc0, f32x4 &acc1) {
  float s[8];
  const f32x4 z = {0.f, 0.f, 0.f, 0.f};
  f32x4 d[8];
#pragma unroll
  for (int g = 0; g < 8; ++g) {
    d[g] = mfma32(t.k[g], q, z);
    if (REFILL) t.k[g] = ld16<KAUX>(rk, koff, snext + g * 1024);
    SB();
  }
#pragma unroll
  for (int g = 0; g < 8; ++g) {
    s[g] = ((dpp<0x00>(d[g][0]) + dpp<0x55>(d[g][1])) + dpp<0xAA>(d[g][2])) + dpp<0xFF>(d[g][3]);
    asm("" : "+v"(s[g]));
  }
#pragma unroll
  for (int g = 0; g < 8; g += 2) {
    const unsigned a = cvtpk(s[g], s[g + 1]);
    const unsigned b = cvtpk(lo16(a) * SCALE, hi16(a) * SCALE);
    s[g] = lo16(b); s[g + 1] = hi16(b);
  }
  float mx = fmaxf(fmaxf(fmaxf(s[0], s[1]), fmaxf(s[2], s[3])), fmaxf(fmaxf(s[4], s[5]), fmaxf(s[6], s[7])));
  mx = xr16_max(mx);
  const float mn = fmaxf(m2, mx * LOG2E);
  const float alpha = __builtin_amdgcn_exp2f(m2 - mn);
  m2 = mn;
  lsum *= alpha;
  int ai = __builtin_bit_cast(int, alpha), ar;
  ar = __builtin_amdgcn_update_dpp(ai, ai, 0x150, 0x1, 0xf, false);
  ar = __builtin_amdgcn_update_dpp(ar, ai, 0x154, 0x2, 0xf, false);
  ar = __builtin_amdgcn_update_dpp(ar, ai, 0x158, 0x4, 0xf, false);
  ar = __builtin_amdgcn_update_dpp(ar, ai, 0x15c, 0x8, 0xf, false);
  const float al = __builtin_bit_cast(float, ar);
  acc0 *= al; acc1 *= al;
#pragma unroll
  for (int g = 0; g < 8; ++g) {
    const float p = __builtin_amdgcn_exp2f(fmaf(s[g], LOG2E, -mn));
    lsum += p;
    const unsigned pp = cvtpk(p, p);
    const v2u a = {pp & mk0, pp & mk1};
    acc0 = mfma16(a, t.v[g][0], t.v[g][1], acc0);
    acc1 = mfma16(a, t.v[g][2], t.v[g][3], acc1);
    if (REFILL) t.v[g] = ld16<VAUX>(rv, voff, snext + g * 1024);
    SB();
  }
}

template <int N>
__device__ __forceinline__ void gather(rsrc_t rs, const int (&off)[N], unsigned tag, v4u (&x)[N]) {
  for (unsigned spins = 0;; ++spins) {
    bool ok = true;
#pragma unroll
    for (int i = 0; i < N; ++i) x[i] = ld16<AUX_SC1>(rs, off[i], 0);
#pragma unroll
    for (int i = 0; i < N; ++i) ok = ok && x[i][1] == tag && x[i][3] == tag;
    if (__all(ok) || spins > SPIN_CAP) break;
    __builtin_amdgcn_s_sleep(1);
  }
}
template <int S> __device__ __forceinline__ float strided_sum(float x) {
  if (S <= 1) x += dpp<XOR1>(x);
  if (S <= 2) x += dpp<XOR2>(x);
  if (S <= 8) x += dpp<ROR8>(x);
  if (S <= 4) x += dpp<ROR4>(x);
  return xr16_sum(x);
}
template <int C>
__device__ __forceinline__ void publish512(rsrc_t rs, int base, int nprod, int p, f32x4 a0, f32x4 a1, LAS float *tr, int w, int lane, unsigned tag) {
  if (lane < 16) {
    *(LAS f32x4 *)(tr + w * 128 + 8 * lane) = a0;
    *(LAS f32x4 *)(tr + w * 128 + 8 * lane + 4) = a1;
  }
  LDS_WAIT();
  const f32x2 y = *(LAS f32x2 *)(tr + w * 128 + 2 * lane);
  const int col = 128 * w + 2 * lane, j = col / C, within = col % C;
  const v4u g = {asu(y[0]), tag, asu(y[1]), tag};
  st16(rs, base + ((j * nprod + p) * C + within) * 8, g);
}
template <int C, int NP>
__device__ __forceinline__ f32x2 reduce_cols(rsrc_t rs, int base, int j, unsigned tag, LAS float *red, int tid, int w, int lane) {
  constexpr int S = C / 2, NPIECE = NP * S, N = (NPIECE + NTHR - 1) / NTHR;
  int off[N]; v4u x[N];
#pragma unroll
  for (int i = 0; i < N; ++i) { const int pc = i * NTHR + tid; off[i] = base + j * (NP * C * 8) + (pc < NPIECE ? pc : tid) * 16; }
  gather<N>(rs, off, tag, x);
  float s0 = 0.f, s1 = 0.f;
#pragma unroll
  for (int i = 0; i < N; ++i) { const bool live = i * NTHR + tid < NPIECE; s0 += live ? asf(x[i][0]) : 0.f; s1 += live ? asf(x[i][2]) : 0.f; }
  s0 = strided_sum<S>(s0); s1 = strided_sum<S>(s1);
  if (lane < S) { f32x2 y = {s0, s1}; *(LAS f32x2 *)(red + w * 64 + 2 * lane) = y; }
  WG_BARRIER();
  f32x2 r = {0.f, 0.f};
#pragma unroll
  for (int ww = 0; ww < 4; ++ww) r += *(LAS f32x2 *)(red + ww * 64 + 2 * (lane % S));
  return r;
}

#define STAMP_AT(i) do { } while (0)
#define STAMP_FLUSH() do { } while (0)

constexpr int L_MRG = 0;
constexpr int L_ML = L_MRG + 2048;
constexpr int L_RED = L_ML + 32;
constexpr int L_RED2 = L_RED + 256;
constexpr int L_TR = L_RED2 + 256;
constexpr int L_Q = L_TR + 512;
constexpr int L_XN = L_Q + 256;
constexpr int L_END = L_XN + 256;
constexpr int LDS_BYTES = 84 * 1024;

__global__ void __launch_bounds__(NTHR, 2) fused_decode_kernel(Params p_) {
  extern __shared__ __attribute__((aligned(16))) float smem[];
  LAS float *L = (LAS float *)smem;
  const int tid = threadIdx.x, lane = tid & 63, w = __builtin_amdgcn_readfirstlane(tid >> 6), b = blockIdx.x;
  unsigned long long tstart;
  const __attribute__((address_space(4))) unsigned long long *pp = (const __attribute__((address_space(4))) unsigned long long *)__builtin_amdgcn_kernarg_segment_ptr();
  asm volatile("s_memrealtime %0" : "=s"(tstart), "+s"(pp));
  Params p;
  p.x = (const void *)pp[0]; p.k = (const void *)pp[1]; p.v = (const void *)pp[2]; p.wq = (const void *)pp[3]; p.wo = (const void *)pp[4];
  p.wg = (const void *)pp[5]; p.wu = (const void *)pp[6]; p.wd = (const void *)pp[7]; p.out = (void *)pp[8]; p.ws = (void *)pp[9];
  asm volatile("" :: "s"(p.x), "s"(p.k), "s"(p.v), "s"(p.wq), "s"(p.wo), "s"(p.wg), "s"(p.wu), "s"(p.wd), "s"(p.out), "s"(p.ws)); SB();
  STAMP_AT(0);
  const int kh = b >> 5, sp = b & 31;
  const rsrc_t rws = __builtin_amdgcn_make_buffer_rsrc(p.ws, 0, WS_USED, RS3);
  const rsrc_t rk = __builtin_amdgcn_make_buffer_rsrc((char *)p.k + ((size_t)kh * SK + sp * 2048) * 256, 0, 2048 * 256, RS3);
  const rsrc_t rv = __builtin_amdgcn_make_buffer_rsrc((char *)p.v + ((size_t)kh * SK + sp * 2048) * 256, 0, 2048 * 256, RS3);
  const rsrc_t rx = __builtin_amdgcn_make_buffer_rsrc((void *)p.x, 0, DM * 2, RS3);
  const rsrc_t rwq = __builtin_amdgcn_make_buffer_rsrc((void *)p.wq, 0, DM * 4096 * 2, RS3);
  const unsigned epoch = *(const __attribute__((address_space(4))) unsigned *)((const char *)p.ws + WS_EPOCH);
  const v4u xr = ld16<0>(rx, lane * 16, 0);
  const int qf = (b & 7) * 32 + (b >> 3);
  const unsigned xraw = __builtin_amdgcn_raw_buffer_load_b32(rx, (64 * w + lane) * 4, 0, 0);
  const int wqoff = (128 * w + 2 * lane) * 8192 + qf * 32;
  const v4u wa0 = ld16<WAUX>(rwq, wqoff, 0), wa1 = ld16<WAUX>(rwq, wqoff, 8192);
  const v4u wb0 = ld16<WAUX>(rwq, wqoff, 16), wb1 = ld16<WAUX>(rwq, wqoff, 8192 + 16);
  SB();
  asm volatile("s_barrier" ::: "memory");
  SB();
  const int g4 = lane >> 4, a4 = (lane >> 2) & 3, u4 = lane & 3;
  const int koff = a4 * 256 + (4 * g4 + u4) * 16, voff = lane * 16;
  Tile ta, tb;
#pragma unroll
  for (int g = 0; g < 8; ++g) ta.k[g] = ld16<KAUX>(rk, koff, w * 8192 + g * 1024);
#pragma unroll
  for (int g = 0; g < 8; ++g) ta.v[g] = ld16<VAUX>(rv, voff, w * 8192 + g * 1024);
  SB();
  const unsigned tag = epoch + 1u;
  {
    unsigned xsel = ln_pair(xraw, ln_stats(xr));
    const float r = gemv16(xsel, wa0, wa1, wb0, wb1, lane);
    if ((lane & 3) == 0) L[L_RED + w * 64 + (lane >> 2)] = r;
    WG_BARRIER();
    if (w == 0) {
      const int i = lane & 7;
      float s0 = 0.f, s1 = 0.f;
#pragma unroll
      for (int ww = 0; ww < 4; ++ww) { const f32x2 y = *(LAS f32x2 *)(L + L_RED + ww * 64 + 2 * i); s0 += y[0]; s1 += y[1]; }
      const v2u g = {cvtpk(s0, s1), tag};
      if (lane < 8 * QREP) st8(rws, WS_QG + (lane >> 3) * 16384 + ((b & 7) * 256 + (b >> 3) * 8 + i) * 8, g);
    }
  }
  STAMP_AT(1);
  asm volatile("s_waitcnt vmcnt(9)" ::: "memory");
  v4u q;
  {
    const int qoff = WS_QG + (b & (QREP - 1)) * 16384 + (kh * 256 + w * 64) * 8 + (lane & 31) * 16;
    v4u x0 = ld16<AUX_SC1>(rws, qoff, 0);
    SB();
#pragma unroll
    for (int g = 0; g < 8; ++g) tb.k[g] = ld16<KAUX>(rk, koff, (4 + w) * 8192 + g * 1024);
#pragma unroll
    for (int g = 0; g < 8; ++g) tb.v[g] = ld16<VAUX>(rv, voff, (4 + w) * 8192 + g * 1024);
    SB();
    bool ok = x0[1] == tag && x0[3] == tag;
    for (unsigned spins = 0; !__all(ok) && spins < SPIN_CAP; ++spins) {
      x0 = ld16<AUX_SC1>(rws, qoff, 0);
      ok = x0[1] == tag && x0[3] == tag;
    }
    if (lane < 32) *(LAS v2u *)(L + L_Q + w * 64 + 2 * lane) = v2u{x0[0], x0[2]};
    WG_BARRIER();
    q = *(LAS v4u *)(L + L_Q + a4 * 64 + (4 * g4 + u4) * 4);
  }
  STAMP_AT(2);
  const int pf = (b & 7) * 28 + (b >> 3);
  const bool hasf = (b >> 3) < 28;
  const rsrc_t rwg = __builtin_amdgcn_make_buffer_rsrc((void *)p.wg, 0, hasf ? DM * FF * 2 : 0, RS3);
  const rsrc_t rwu = __builtin_amdgcn_make_buffer_rsrc((void *)p.wu, 0, hasf ? DM * FF * 2 : 0, RS3);
  const int go = (128 * w + 2 * lane) * (FF * 2) + pf * 16;
  const v4u wg0 = ld16<WAUX>(rwg, go, 0), wg1 = ld16<WAUX>(rwg, go, FF * 2);
  const v4u wu0 = ld16<WAUX>(rwu, go, 0), wu1 = ld16<WAUX>(rwu, go, FF * 2);
  SB();
  const unsigned mk0 = u4 == 0 ? 0xFFFFu : u4 == 1 ? 0xFFFF0000u : 0u;
  const unsigned mk1 = u4 == 2 ? 0xFFFFu : u4 == 3 ? 0xFFFF0000u : 0u;
  float m2 = -3.0e38f, lsum = 0.f;
  f32x4 acc0 = {0.f, 0.f, 0.f, 0.f}, acc1 = acc0;
  for (int it = 0; it < 7; ++it) {
    const int s0 = ((2 * it + 2) * 4 + w) * 8192;
    tile_step<true>(ta, q, rk, rv, koff, voff, s0, mk0, mk1, m2, lsum, acc0, acc1);
    tile_step<true>(tb, q, rk, rv, koff, voff, s0 + 4 * 8192, mk0, mk1, m2, lsum, acc0, acc1);
  }
  const rsrc_t rwo = __builtin_amdgcn_make_buffer_rsrc((char *)p.wo + (size_t)b * 16384, 0, 16384, RS3);
  const rsrc_t rwd = __builtin_amdgcn_make_buffer_rsrc((char *)p.wd + (size_t)(hasf ? pf : 0) * 8192, 0, hasf ? 8192 : 0, RS3);
  v4u wo[4], wd[2];
  {
    const int o = g4 * 1024 + w * 256 + (lane & 15) * 16;
#pragma unroll
    for (int i = 0; i < 4; ++i) wo[i] = ld16<WAUX>(rwo, o, i * 4096);
#pragma unroll
    for (int i = 0; i < 2; ++i) wd[i] = ld16<WAUX>(rwd, o, i * 4096);
    SB();
  }
  tile_step<false>(ta, q, rk, rv, koff, voff, 0, mk0, mk1, m2, lsum, acc0, acc1);
  tile_step<false>(tb, q, rk, rv, koff, voff, 0, mk0, mk1, m2, lsum, acc0, acc1);
  STAMP_AT(3);
  {
    const float lw = xr16_sum(lsum);
    *(LAS f32x4 *)(L + L_MRG + w * 512 + g4 * 128 + 8 * (lane & 15)) = acc0;
    *(LAS f32x4 *)(L + L_MRG + w * 512 + g4 * 128 + 8 * (lane & 15) + 4) = acc1;
    if (lane < 16 && u4 == 0) { f32x2 y = {m2, lw}; *(LAS f32x2 *)(L + L_ML + (w * 4 + a4) * 2) = y; }
    WG_BARRIER();
    f32x2 ml[4];
#pragma unroll
    for (int ww = 0; ww < 4; ++ww) ml[ww] = *(LAS f32x2 *)(L + L_ML + (ww * 4 + w) * 2);
    const float M = fmaxf(fmaxf(ml[0][0], ml[1][0]), fmaxf(ml[2][0], ml[3][0]));
    float Ls = 0.f; f32x2 o = {0.f, 0.f};
#pragma unroll
    for (int ww = 0; ww < 4; ++ww) {
      const float e = __builtin_amdgcn_exp2f(ml[ww][0] - M);
      Ls = fmaf(e, ml[ww][1], Ls);
      o += e * *(LAS f32x2 *)(L + L_MRG + ww * 512 + w * 128 + 2 * lane);
    }
    const int head = 4 * kh + w;
    st16(rws, WS_OP + (((head * 8 + (lane >> 3)) * 32 + sp) * 16 + 2 * (lane & 7)) * 8, v4u{asu(o[0]), tag, asu(o[1]), tag});
    if (lane < 8) st16(rws, WS_ML + ((head * 8 + lane) * 32 + sp) * 16, v4u{asu(M), tag, asu(Ls), tag});
  }
  STAMP_AT(4);
  {
    const int off[2] = {WS_OP + b * 4096 + tid * 16, WS_ML + (b * 32 + 8 * w + (lane >> 3)) * 16};
    v4u x[2];
    gather<2>(rws, off, tag, x);
    STAMP_AT(5);
    const float ms = asf(x[1][0]), ls = asf(x[1][2]);
    const float Mw = xr16_max(fmaxf(ms, dpp<ROR8>(ms)));
    const float e = __builtin_amdgcn_exp2f(ms - Mw);
    const float v0 = strided_sum<8>(e * asf(x[0][0])), v1 = strided_sum<8>(e * asf(x[0][2])), lw = strided_sum<8>(e * ls);
    if (lane < 8) {
      const int d = 2 * lane;
      L[L_RED2 + w * 64 + (d & 3) * 4 + (d >> 2)] = v0;
      L[L_RED2 + w * 64 + ((d + 1) & 3) * 4 + (d >> 2)] = v1;
      if (lane == 0) *(LAS f32x2 *)(L + L_RED2 + w * 64 + 32) = f32x2{Mw, lw};
    }
    WG_BARRIER();
    f32x2 ml[4];
#pragma unroll
    for (int ww = 0; ww < 4; ++ww) ml[ww] = *(LAS f32x2 *)(L + L_RED2 + ww * 64 + 32);
    const float M = fmaxf(fmaxf(ml[0][0], ml[1][0]), fmaxf(ml[2][0], ml[3][0]));
    f32x4 o = {0.f, 0.f, 0.f, 0.f};
    float Lt = 0.f;
#pragma unroll
    for (int ww = 0; ww < 4; ++ww) {
      const float ew = __builtin_amdgcn_exp2f(ml[ww][0] - M);
      Lt = fmaf(ew, ml[ww][1], Lt);
      o += ew * *(LAS f32x4 *)(L + L_RED2 + ww * 64 + g4 * 4);
    }
    o *= __builtin_amdgcn_rcpf(Lt);
    f32x4 c0 = {0.f, 0.f, 0.f, 0.f}, c1 = c0;
#pragma unroll
    for (int i = 0; i < 4; ++i) {
      const unsigned pp = cvtpk(o[i], o[i]);
      const v2u a = {pp & mk0, pp & mk1};
      c0 = mfma16(a, wo[i][0], wo[i][1], c0);
      c1 = mfma16(a, wo[i][2], wo[i][3], c1);
    }
    publish512<512 / NB1>(rws, WS_PB, 256, b, c0, c1, L + L_TR, w, lane, tag);
  }
  STAMP_AT(6);
  if (b < NB1) {
    constexpr int C = 512 / NB1, S = C / 2;
    const unsigned xp = __builtin_amdgcn_raw_buffer_load_b32(rx, (b * C + 2 * (lane % S)) * 2, 0, 0);
    const f32x2 r = reduce_cols<C, 256>(rws, WS_PB, b, tag, L + L_RED, tid, w, lane);
    STAMP_AT(7);
    const unsigned hp = cvtpk(lo16(xp) + rbf(r[0]), hi16(xp) + rbf(r[1]));
    if (tid < S * REP) st8(rws, WS_HG + (tid / S) * 2048 + (b * S + tid % S) * 8, v2u{hp, tag});
  }
  STAMP_AT(8);
  if (w == 0) {
    const int base = WS_HG + (b % REP) * 2048 + lane * 32;
    const int off[2] = {base, base + 16};
    v4u x[2];
    gather<2>(rws, off, tag, x);
    const v4u hb = v4u{x[0][0], x[0][2], x[1][0], x[1][2]};
    *(LAS v4u *)(L + L_Q + 4 * lane) = hb;
    *(LAS v4u *)(L + L_XN + 4 * lane) = layernorm8(hb);
  }
  WG_BARRIER();
  STAMP_AT(9);
  if (hasf) {
    const unsigned xsel = ((LAS unsigned *)L)[L_XN + 64 * w + lane];
    const float r = gemv16(xsel, wg0, wg1, wu0, wu1, lane);
    if ((lane & 3) == 0) { const int vi = lane >> 2, col = vi & 7; L[L_RED2 + w * 64 + 16 + (col & 3) * 4 + (vi >> 3) * 2 + (col >> 2)] = r; }
    WG_BARRIER();
    f32x4 gu = {0.f, 0.f, 0.f, 0.f};
#pragma unroll
    for (int ww = 0; ww < 4; ++ww) gu += *(LAS f32x4 *)(L + L_RED2 + ww * 64 + 16 + g4 * 4);
    f32x4 c0 = {0.f, 0.f, 0.f, 0.f}, c1 = c0;
#pragma unroll
    for (int i = 0; i < 2; ++i) {
      const float hv = rbf(gu[i]) * rbf(gu[2 + i]);
      const unsigned pp = cvtpk(hv, hv);
      const v2u a = {pp & mk0, pp & mk1};
      c0 = mfma16(a, wd[i][0], wd[i][1], c0);
      c1 = mfma16(a, wd[i][2], wd[i][3], c1);
    }
    publish512<512 / ND>(rws, WS_PD, NPF, pf, c0, c1, L + L_TR, w, lane, tag);
  }
  STAMP_AT(10);
  if (b >= NWG - ND) {
    constexpr int C = 512 / ND, S = C / 2;
    const int j = b - (NWG - ND);
    const unsigned hp = ((LAS unsigned *)L)[L_Q + j * S + lane % S];
    WG_BARRIER();
    const f32x2 r = reduce_cols<C, NPF>(rws, WS_PD, j, tag, L + L_RED, tid, w, lane);
    const unsigned op = cvtpk(lo16(hp) + rbf(r[0]), hi16(hp) + rbf(r[1]));
    if (w == 0 && lane < S) ((unsigned *)p.out)[j * S + lane] = op;
    if (j == 0 && tid == 0) __builtin_amdgcn_raw_buffer_store_b32(tag, rws, WS_EPOCH, 0, AUX_SC1);
  }
  STAMP_AT(11);
  STAMP_FLUSH();
  asm volatile("s_waitcnt lgkmcnt(0)" ::: "memory");
  { while (__builtin_amdgcn_s_memrealtime() - tstart < 10000) __builtin_amdgcn_s_sleep(2); }
}
}

extern "C" void kernel_launch(void *const *d_in, const int *in_sizes, int n_in, void *d_out, int out_size, void *d_ws, size_t ws_size,
                              hipStream_t stream) {
  Params p;
  p.x = d_in[0]; p.k = d_in[1]; p.v = d_in[2]; p.wq = d_in[3]; p.wo = d_in[4];
  p.wg = d_in[6]; p.wu = d_in[7]; p.wd = d_in[8];
  p.out = d_out; p.ws = d_ws;
  (void)hipFuncSetAttribute((const void *)fused_decode_kernel, hipFuncAttributeMaxDynamicSharedMemorySize, LDS_BYTES);
  fused_decode_kernel<<<NWG, NTHR, LDS_BYTES, stream>>>(p);
}
```

```cpp
#include <hip/hip_runtime.h>
#include <stdint.h>

#define STAMP 0
#define NB1 64
#define ND 32
#define REP 32
#define KAUX 2
#define VAUX 0
#define WAUX 0
#define PINARGS 1
#define ORD 1
#define HM 1
#define TAU 0
#define TOFF 500
#define TK 60
#define TPOLL 280
#define GUSPREAD 1
#define QREP 4

namespace {
typedef unsigned v4u __attribute__((ext_vector_type(4)));
typedef unsigned v2u __attribute__((ext_vector_type(2)));
typedef float f32x4 __attribute__((ext_vector_type(4)));
typedef float f32x2 __attribute__((ext_vector_type(2)));
typedef short s16x4 __attribute__((ext_vector_type(4)));
typedef __bf16 bf16x8 __attribute__((ext_vector_type(8)));
typedef __bf16 bf16x2 __attribute__((ext_vector_type(2)));
typedef __amdgpu_buffer_rsrc_t rsrc_t;
#define LAS __attribute__((address_space(3)))

constexpr int DM = 512, SK = 65536, DH = 128, FF = 1792;
constexpr int NWG = 256, NTHR = 256;
constexpr int NPF = 224;
constexpr int AUX_NT = 2, AUX_SC1 = 16;
constexpr int RS3 = 0x00020000;
constexpr int WS_EPOCH = 0;
constexpr int WS_QG = 4 << 20;
constexpr int WS_ML = 4096;
constexpr int WS_HG = WS_ML + 131072;
constexpr int WS_OP = 1 << 20;
constexpr int WS_PB = 2 << 20;
constexpr int WS_PD = 3 << 20;
constexpr int WS_USED = 5 << 20;
constexpr int WS_STAMP = 8 << 20;
constexpr unsigned SPIN_CAP = 1u << 14;
constexpr float LOG2E = 1.4426950408889634f;
constexpr float SCALE = 0.08838834764831845f;

struct Params {
  const void *x, *k, *v, *wq, *wo, *wg, *wu, *wd;
  void *out, *ws;
};

__device__ __forceinline__ float asf(unsigned u) { return __builtin_bit_cast(float, u); }
__device__ __forceinline__ unsigned asu(float f) { return __builtin_bit_cast(unsigned, f); }
template <int C> __device__ __forceinline__ float dpp(float x) {
  return __builtin_bit_cast(float, __builtin_amdgcn_mov_dpp(__builtin_bit_cast(int, x), C, 0xf, 0xf, true));
}
constexpr int XOR1 = 0xB1, XOR2 = 0x4E, HMIR = 0x141, MIR = 0x140, ROR4 = 0x124, ROR8 = 0x128;
__device__ __forceinline__ unsigned cvtpk(float lo, float hi) {
  f32x2 v = {lo, hi};
  bf16x2 r = __builtin_convertvector(v, bf16x2);
  return __builtin_bit_cast(unsigned, r);
}
__device__ __forceinline__ float lo16(unsigned u) { return asf(u << 16); }
__device__ __forceinline__ float hi16(unsigned u) { return asf(u & 0xffff0000u); }
__device__ __forceinline__ float rbf(float x) { return lo16(cvtpk(x, x)); }
__device__ __forceinline__ float xr16_max(float x) {
  auto s = __builtin_amdgcn_permlane16_swap(asu(x), asu(x), false, false);
  x = fmaxf(asf(s[0]), asf(s[1]));
  auto t = __builtin_amdgcn_permlane32_swap(asu(x), asu(x), false, false);
  return fmaxf(asf(t[0]), asf(t[1]));
}
__device__ __forceinline__ float xr16_sum(float x) {
  auto s = __builtin_amdgcn_permlane16_swap(asu(x), asu(x), false, false);
  x = asf(s[0]) + asf(s[1]);
  auto t = __builtin_amdgcn_permlane32_swap(asu(x), asu(x), false, false);
  return asf(t[0]) + asf(t[1]);
}
__device__ __forceinline__ float row_sum(float x) {
  x += dpp<XOR1>(x); x += dpp<XOR2>(x); x += dpp<HMIR>(x); x += dpp<MIR>(x); return x;
}
__device__ __forceinline__ float row_max(float x) {
  x = fmaxf(x, dpp<XOR1>(x)); x = fmaxf(x, dpp<XOR2>(x)); x = fmaxf(x, dpp<HMIR>(x)); x = fmaxf(x, dpp<MIR>(x)); return x;
}
__device__ __forceinline__ float wave_sum(float x) { return xr16_sum(row_sum(x)); }
__device__ __forceinline__ f32x4 mfma32(v4u a, v4u b, f32x4 c) {
  return __builtin_amdgcn_mfma_f32_16x16x32_bf16(__builtin_bit_cast(bf16x8, a), __builtin_bit_cast(bf16x8, b), c, 0, 0, 0);
}
__device__ __forceinline__ f32x4 mfma16(v2u a, unsigned b0, unsigned b1, f32x4 c) {
  v2u b = {b0, b1};
  return __builtin_amdgcn_mfma_f32_16x16x16bf16_1k(__builtin_bit_cast(s16x4, a), __builtin_bit_cast(s16x4, b), c, 0, 0, 0);
}
template <int AUX> __device__ __forceinline__ v4u ld16(rsrc_t r, int voff, int soff) {
  return __builtin_amdgcn_raw_buffer_load_b128(r, voff, soff, AUX);
}
__device__ __forceinline__ void st16(rsrc_t r, int voff, v4u v) { __builtin_amdgcn_raw_buffer_store_b128(v, r, voff, 0, AUX_SC1); }
__device__ __forceinline__ void st8(rsrc_t r, int voff, v2u v) { __builtin_amdgcn_raw_buffer_store_b64(v, r, voff, 0, AUX_SC1); }
__device__ __forceinline__ v4u make_desc(const void *base, unsigned bytes) {
  const unsigned long long a = (unsigned long long)base;
  return v4u{(unsigned)a, (unsigned)(a >> 32) & 0xffffu, bytes, (unsigned)RS3};
}
template <int K> __device__ __forceinline__ void ld16_async_at(v4u &dst, v4u desc, int voff, int soff, int sel) {
  asm volatile("s_cmp_lg_u32 %4, %5\n\ts_cbranch_scc1 .Lskip%=\n\tbuffer_load_dwordx4 %0, %1, %2, %3 offen\n.Lskip%=:"
               : "+v"(dst) : "v"(voff), "s"(desc), "s"(soff), "s"(sel), "i"(K) : "scc");
}
#define WG_BARRIER() asm volatile("s_waitcnt lgkmcnt(0)\n\ts_barrier" ::: "memory")
#define LDS_WAIT() asm volatile("s_waitcnt lgkmcnt(0)" ::: "memory")
#define SB() __builtin_amdgcn_sched_barrier(0)

__device__ __forceinline__ v4u layernorm8(v4u p) {
  float x[8];
#pragma unroll
  for (int i = 0; i < 4; ++i) { x[2 * i] = lo16(p[i]); x[2 * i + 1] = hi16(p[i]); }
  float s = ((x[0] + x[1]) + (x[2] + x[3])) + ((x[4] + x[5]) + (x[6] + x[7]));
  const float mean = wave_sum(s) * (1.f / DM);
  float q = 0.f;
#pragma unroll
  for (int i = 0; i < 8; ++i) { const float d = x[i] - mean; q = fmaf(d, d, q); }
  const float var = wave_sum(q) * (1.f / DM);
  const float mb = rbf(mean);
  const float den = rbf(sqrtf(rbf(rbf(var) + 1e-5f)));
  v4u r;
#pragma unroll
  for (int i = 0; i < 4; ++i) r[i] = cvtpk(rbf(x[2 * i] - mb) / den, rbf(x[2 * i + 1] - mb) / den);
  return r;
}
struct LnStat { float mb, rden; float den; };
__device__ __forceinline__ LnStat ln_stats(v4u p) {
  float x[8];
#pragma unroll
  for (int i = 0; i < 4; ++i) { x[2 * i] = lo16(p[i]); x[2 * i + 1] = hi16(p[i]); }
  float s = ((x[0] + x[1]) + (x[2] + x[3])) + ((x[4] + x[5]) + (x[6] + x[7]));
  const float mean = wave_sum(s) * (1.f / DM);
  float q = 0.f;
#pragma unroll
  for (int i = 0; i < 8; ++i) { const float d = x[i] - mean; q = fmaf(d, d, q); }
  const float var = wave_sum(q) * (1.f / DM);
  LnStat r; r.mb = rbf(mean); r.den = rbf(__builtin_amdgcn_sqrtf(rbf(rbf(var) + 1e-5f))); r.rden = __builtin_amdgcn_rcpf(r.den);
  return r;
}
__device__ __forceinline__ unsigned ln_pair(unsigned p, LnStat st) { return cvtpk(rbf(lo16(p) - st.mb) * st.rden, rbf(hi16(p) - st.mb) * st.rden); }
__device__ __forceinline__ unsigned pick4(v4u v, int w) {
  const unsigned a = (w & 1) ? v[1] : v[0], b = (w & 1) ? v[3] : v[2];
  return (w & 2) ? b : a;
}
__device__ __forceinline__ float gemv16(unsigned xp, v4u a0, v4u a1, v4u b0, v4u b1, int lane) {
  float v[16];
  const bf16x2 xb = __builtin_bit_cast(bf16x2, xp);
#pragma unroll
  for (int i = 0; i < 4; ++i) {
    const unsigned alo = __builtin_amdgcn_perm(a1[i], a0[i], 0x05040100u), ahi = __builtin_amdgcn_perm(a1[i], a0[i], 0x07060302u);
    const unsigned blo = __builtin_amdgcn_perm(b1[i], b0[i], 0x05040100u), bhi = __builtin_amdgcn_perm(b1[i], b0[i], 0x07060302u);
    v[2 * i] = __builtin_amdgcn_fdot2_f32_bf16(__builtin_bit_cast(bf16x2, alo), xb, 0.f, false);
    v[2 * i + 1] = __builtin_amdgcn_fdot2_f32_bf16(__builtin_bit_cast(bf16x2, ahi), xb, 0.f, false);
    v[8 + 2 * i] = __builtin_amdgcn_fdot2_f32_bf16(__builtin_bit_cast(bf16x2, blo), xb, 0.f, false);
    v[8 + 2 * i + 1] = __builtin_amdgcn_fdot2_f32_bf16(__builtin_bit_cast(bf16x2, bhi), xb, 0.f, false);
  }
  float u[8];
#pragma unroll
  for (int k = 0; k < 8; ++k) { auto s = __builtin_amdgcn_permlane32_swap(asu(v[k]), asu(v[k + 8]), false, false); u[k] = asf(s[0]) + asf(s[1]); }
  float t[4];
#pragma unroll
  for (int k = 0; k < 4; ++k) { auto s = __builtin_amdgcn_permlane16_swap(asu(u[k]), asu(u[k + 4]), false, false); t[k] = asf(s[0]) + asf(s[1]); }
  float y[2];
#pragma unroll
  for (int k = 0; k < 2; ++k) { const float p = t[k] + dpp<ROR8>(t[k]), q = t[k + 2] + dpp<ROR8>(t[k + 2]); y[k] = (lane & 8) ? q : p; }
#pragma unroll
  for (int k = 0; k < 2; ++k) { y[k] += dpp<XOR1>(y[k]); y[k] += dpp<XOR2>(y[k]); y[k] += dpp<HMIR>(y[k]); }
  return (lane & 4) ? y[1] : y[0];
}

__device__ __forceinline__ f32x2 gemv32(const unsigned (&xp)[4], const v4u (&e)[4], const v4u (&o)[4]) {
  float v[8];
#pragma unroll
  for (int c = 0; c < 8; ++c) v[c] = 0.f;
#pragma unroll
  for (int i = 0; i < 4; ++i) {
    const bf16x2 xb = __builtin_bit_cast(bf16x2, xp[i]);
#pragma unroll
    for (int d = 0; d < 4; ++d) {
      const unsigned lo = __builtin_amdgcn_perm(o[i][d], e[i][d], 0x05040100u), hi = __builtin_amdgcn_perm(o[i][d], e[i][d], 0x07060302u);
      v[2 * d] = __builtin_amdgcn_fdot2_f32_bf16(__builtin_bit_cast(bf16x2, lo), xb, v[2 * d], false);
      v[2 * d + 1] = __builtin_amdgcn_fdot2_f32_bf16(__builtin_bit_cast(bf16x2, hi), xb, v[2 * d + 1], false);
    }
  }
  float u[4];
#pragma unroll
  for (int k = 0; k < 4; ++k) { auto s = __builtin_amdgcn_permlane32_swap(asu(v[k]), asu(v[k + 4]), false, false); u[k] = asf(s[0]) + asf(s[1]); }
  f32x2 t;
#pragma unroll
  for (int k = 0; k < 2; ++k) {
    auto s = __builtin_amdgcn_permlane16_swap(asu(u[k]), asu(u[k + 2]), false, false);
    float y = asf(s[0]) + asf(s[1]);
    y += dpp<ROR8>(y); y += dpp<ROR4>(y);
    t[k] = y;
  }
  return t;
}

struct Tile { v4u k[8], v[8]; };

template <bool REFILL>
__device__ __forceinline__ void tile_step(Tile &t, v4u q, rsrc_t rk, rsrc_t rv, int koff, int voff, int snext, unsigned mk0, unsigned mk1,
                                          float &m2, float &lsum, f32x4 &acc0, f32x4 &acc1) {
  float s[8];
  const f32x4 z = {0.f, 0.f, 0.f, 0.f};
  f32x4 d[8];
#pragma unroll
  for (int g = 0; g < 8; ++g) {
    d[g] = mfma32(t.k[g], q, z);
    if (REFILL) t.k[g] = ld16<KAUX>(rk, koff, snext + g * 1024);
    SB();
  }
#pragma unroll
  for (int g = 0; g < 8; ++g) {
    s[g] = ((dpp<0x00>(d[g][0]) + dpp<0x55>(d[g][1])) + dpp<0xAA>(d[g][2])) + dpp<0xFF>(d[g][3]);
    asm("" : "+v"(s[g]));
  }
#pragma unroll
  for (int g = 0; g < 8; g += 2) {
    const unsigned a = cvtpk(s[g], s[g + 1]);
    const unsigned b = cvtpk(lo16(a) * SCALE, hi16(a) * SCALE);
    s[g] = lo16(b); s[g + 1] = hi16(b);
  }
  float mx = fmaxf(fmaxf(fmaxf(s[0], s[1]), fmaxf(s[2], s[3])), fmaxf(fmaxf(s[4], s[5]), fmaxf(s[6], s[7])));
  mx = xr16_max(mx);
  const float mn = fmaxf(m2, mx * LOG2E);
  const float alpha = __builtin_amdgcn_exp2f(m2 - mn);
  m2 = mn;
  lsum *= alpha;
  int ai = __builtin_bit_cast(int, alpha), ar;
  ar = __builtin_amdgcn_update_dpp(ai, ai, 0x150, 0x1, 0xf, false);
  ar = __builtin_amdgcn_update_dpp(ar, ai, 0x154, 0x2, 0xf, false);
  ar = __builtin_amdgcn_update_dpp(ar, ai, 0x158, 0x4, 0xf, false);
  ar = __builtin_amdgcn_update_dpp(ar, ai, 0x15c, 0x8, 0xf, false);
  const float al = __builtin_bit_cast(float, ar);
  acc0 *= al; acc1 *= al;
#pragma unroll
  for (int g = 0; g < 8; ++g) {
    const float p = __builtin_amdgcn_exp2f(fmaf(s[g], LOG2E, -mn));
    lsum += p;
    const unsigned pp = cvtpk(p, p);
    const v2u a = {pp & mk0, pp & mk1};
    acc0 = mfma16(a, t.v[g][0], t.v[g][1], acc0);
    acc1 = mfma16(a, t.v[g][2], t.v[g][3], acc1);
    if (REFILL) t.v[g] = ld16<VAUX>(rv, voff, snext + g * 1024);
    SB();
  }
}

template <int N>
__device__ __forceinline__ void gather(rsrc_t rs, const int (&off)[N], unsigned tag, v4u (&x)[N]) {
  for (unsigned spins = 0;; ++spins) {
    bool ok = true;
    asm volatile("" ::: "memory");
#pragma unroll
    for (int i = 0; i < N; ++i) x[i] = ld16<AUX_SC1>(rs, off[i], 0);
#pragma unroll
    for (int i = 0; i < N; ++i) ok = ok & (x[i][1] == tag) & (x[i][3] == tag);
    if (__all(ok) || spins > SPIN_CAP) break;
    __builtin_amdgcn_s_sleep(1);
  }
}
template <int S> __device__ __forceinline__ float strided_sum(float x) {
  if (S <= 1) x += dpp<XOR1>(x);
  if (S <= 2) x += dpp<XOR2>(x);
  if (S <= 8) x += dpp<ROR8>(x);
  if (S <= 4) x += dpp<ROR4>(x);
  return xr16_sum(x);
}
template <int C>
__device__ __forceinline__ void publish512(rsrc_t rs, int base, int nprod, int p, f32x4 a0, f32x4 a1, LAS float *tr, int w, int lane, unsigned tag) {
  if (lane < 16) {
    *(LAS f32x4 *)(tr + w * 128 + 8 * lane) = a0;
    *(LAS f32x4 *)(tr + w * 128 + 8 * lane + 4) = a1;
  }
  LDS_WAIT();
  const f32x2 y = *(LAS f32x2 *)(tr + w * 128 + 2 * lane);
  const int col = 128 * w + 2 * lane, j = col / C, within = col % C;
  const v4u g = {asu(y[0]), tag, asu(y[1]), tag};
  st16(rs, base + ((j * nprod + p) * C + within) * 8, g);
}
template <int C, int NP>
__device__ __forceinline__ f32x2 reduce_cols(rsrc_t rs, int base, int j, unsigned tag, LAS float *red, int tid, int w, int lane) {
  constexpr int S = C / 2, NPIECE = NP * S, N = (NPIECE + NTHR - 1) / NTHR;
  int off[N]; v4u x[N];
#pragma unroll
  for (int i = 0; i < N; ++i) { const int pc = i * NTHR + tid; off[i] = base + j * (NP * C * 8) + (pc < NPIECE ? pc : tid) * 16; }
  gather<N>(rs, off, tag, x);
  float s0 = 0.f, s1 = 0.f;
#pragma unroll
  for (int i = 0; i < N; ++i) { const bool live = i * NTHR + tid < NPIECE; s0 += live ? asf(x[i][0]) : 0.f; s1 += live ? asf(x[i][2]) : 0.f; }
  s0 = strided_sum<S>(s0); s1 = strided_sum<S>(s1);
  if (lane < S) { f32x2 y = {s0, s1}; *(LAS f32x2 *)(red + w * 64 + 2 * lane) = y; }
  WG_BARRIER();
  f32x2 r = {0.f, 0.f};
#pragma unroll
  for (int ww = 0; ww < 4; ++ww) r += *(LAS f32x2 *)(red + ww * 64 + 2 * (lane % S));
  return r;
}

#define STAMP_AT(i) do { } while (0)
#define STAMP_FLUSH() do { } while (0)

constexpr int L_MRG = 0;
constexpr int L_ML = L_MRG + 2048;
constexpr int L_RED = L_ML + 32;
constexpr int L_RED2 = L_RED + 256;
constexpr int L_TR = L_RED2 + 256;
constexpr int L_Q = L_TR + 512;
constexpr int L_XN = L_Q + 256;
constexpr int L_END = L_XN + 1024;
constexpr int LDS_BYTES = 84 * 1024;

__global__ void __launch_bounds__(NTHR, 2) fused_decode_kernel(Params p) {
  extern __shared__ __attribute__((aligned(16))) float smem[];
  LAS float *L = (LAS float *)smem;
  const int tid = threadIdx.x, lane = tid & 63, w = __builtin_amdgcn_readfirstlane(tid >> 6), b = blockIdx.x;
  asm volatile("" :: "s"(p.x), "s"(p.k), "s"(p.v), "s"(p.wq), "s"(p.wo), "s"(p.wg), "s"(p.wu), "s"(p.wd), "s"(p.out), "s"(p.ws));
  STAMP_AT(0);
  const unsigned tstart = (unsigned)__builtin_amdgcn_s_memrealtime();
  const int kh = HM ? b >> 5 : b & 7, sp = HM ? b & 31 : b >> 3;
  const rsrc_t rws = __builtin_amdgcn_make_buffer_rsrc(p.ws, 0, WS_USED, RS3);
  const rsrc_t rk = __builtin_amdgcn_make_buffer_rsrc((char *)p.k + (size_t)kh * SK * 256, 0, SK * 256, RS3);
  const rsrc_t rv = __builtin_amdgcn_make_buffer_rsrc((char *)p.v + (size_t)kh * SK * 256, 0, SK * 256, RS3);
  const int tbase = ORD == 1 ? sp * 524288 + w * 8192 : ORD == 2 ? (sp * 4 + w) * 8192 : (w * 32 + sp) * 8192;
  constexpr int TSTR = ORD == 1 ? 4 * 8192 : 128 * 8192;
  const rsrc_t rx = __builtin_amdgcn_make_buffer_rsrc((void *)p.x, 0, DM * 2, RS3);
  const rsrc_t rwq = __builtin_amdgcn_make_buffer_rsrc((void *)p.wq, 0, DM * 4096 * 2, RS3);
  const unsigned epoch = *(const __attribute__((address_space(4))) unsigned *)((const char *)p.ws + WS_EPOCH);
  const bool hasq = (b & 7) < 4;
  const int pq = (b >> 3) * 4 + (b & 3), hq = pq & 31, rb = pq >> 5;
  const int g4 = lane >> 4, a4 = (lane >> 2) & 3, u4 = lane & 3;
  const int koff = a4 * 256 + (4 * g4 + u4) * 16, voff = lane * 16;
  const unsigned mk0 = u4 == 0 ? 0xFFFFu : u4 == 1 ? 0xFFFF0000u : 0u;
  const unsigned mk1 = u4 == 2 ? 0xFFFFu : u4 == 3 ? 0xFFFF0000u : 0u;
  v4u wqf[8];
  unsigned xq[8];
  Tile ta, tb;
  if (hasq) {
    const v4u xr = ld16<0>(rx, lane * 16, 0);
    const int wqoff = (128 * rb + 32 * w + g4) * 8192 + hq * 256 + (lane & 15) * 16;
#pragma unroll
    for (int i = 0; i < 8; ++i) wqf[i] = ld16<WAUX>(rwq, wqoff, i * 4 * 8192);
    SB();
    const LnStat st = ln_stats(xr);
    *(LAS v4u *)(L + L_XN + w * 256 + 4 * lane) = v4u{ln_pair(xr[0], st), ln_pair(xr[1], st), ln_pair(xr[2], st), ln_pair(xr[3], st)};
    LDS_WAIT();
#pragma unroll
    for (int i = 0; i < 8; ++i) xq[i] = ((LAS unsigned short *)(L + L_XN + w * 256))[128 * rb + 32 * w + 4 * i + g4];
    STAMP_AT(12);
  }
  if (hasq) {
    f32x4 c0 = {0.f, 0.f, 0.f, 0.f}, c1 = c0;
#pragma unroll
    for (int i = 0; i < 8; ++i) {
      const unsigned pp = xq[i] | (xq[i] << 16);
      const v2u a = {pp & mk0, pp & mk1};
      c0 = mfma16(a, wqf[i][0], wqf[i][1], c0);
      c1 = mfma16(a, wqf[i][2], wqf[i][3], c1);
    }
    if (lane < 16) {
      *(LAS f32x4 *)(L + L_TR + w * 128 + 8 * lane) = c0;
      *(LAS f32x4 *)(L + L_TR + w * 128 + 8 * lane + 4) = c1;
    }
    STAMP_AT(13);
    WG_BARRIER();
    if (w < QREP) {
      f32x2 y = {0.f, 0.f};
#pragma unroll
      for (int ww = 0; ww < 4; ++ww) y += *(LAS f32x2 *)(L + L_TR + ww * 128 + 2 * lane);
      st16(rws, WS_QG + w * 131072 + ((hq * 4 + rb) * 128 + 2 * lane) * 8, v4u{asu(y[0]), epoch + 1u, asu(y[1]), epoch + 1u});
    }
  }
  STAMP_AT(1);
#pragma unroll
  for (int g = 0; g < 8; ++g) ta.k[g] = ld16<KAUX>(rk, koff, tbase + g * 1024);
#pragma unroll
  for (int g = 0; g < 8; ++g) ta.v[g] = ld16<VAUX>(rv, voff, tbase + g * 1024);
  SB();
  const unsigned tag = epoch + 1u;
  while ((unsigned)__builtin_amdgcn_s_memrealtime() - tstart < (unsigned)TPOLL) __builtin_amdgcn_s_sleep(1);
  v4u q;
  {
    const int qoff = WS_QG + (sp & (QREP - 1)) * 131072 + (4 * kh + w) * 4096 + lane * 16;
    v4u x[4];
#pragma unroll
    for (int i = 0; i < 4; ++i) x[i] = ld16<AUX_SC1>(rws, qoff, i * 1024);
    SB();
    asm volatile("s_barrier" ::: "memory");
    SB();
#pragma unroll
    for (int g = 0; g < 8; ++g) tb.k[g] = ld16<KAUX>(rk, koff, tbase + TSTR + g * 1024);
#pragma unroll
    for (int g = 0; g < 8; ++g) tb.v[g] = ld16<VAUX>(rv, voff, tbase + TSTR + g * 1024);
    SB();
    bool ok = true;
#pragma unroll
    for (int i = 0; i < 4; ++i) ok = ok & (x[i][1] == tag) & (x[i][3] == tag);
    if (__builtin_expect(!__all(ok), 0)) {
      for (unsigned spins = 0; spins < SPIN_CAP; ++spins) {
        bool ok2 = true;
        asm volatile("" ::: "memory");
#pragma unroll
        for (int i = 0; i < 4; ++i) x[i] = ld16<AUX_SC1>(rws, qoff, i * 1024);
#pragma unroll
        for (int i = 0; i < 4; ++i) ok2 = ok2 & (x[i][1] == tag) & (x[i][3] == tag);
        if (__all(ok2)) break;
      }
      __builtin_amdgcn_s_waitcnt(0x0F70);
    }
    ((LAS unsigned *)L)[L_Q + w * 64 + lane] = cvtpk((asf(x[0][0]) + asf(x[1][0])) + (asf(x[2][0]) + asf(x[3][0])),
                                                     (asf(x[0][2]) + asf(x[1][2])) + (asf(x[2][2]) + asf(x[3][2])));
    WG_BARRIER();
    q = *(LAS v4u *)(L + L_Q + a4 * 64 + (4 * g4 + u4) * 4);
  }
  STAMP_AT(2);
  const int pf = (b & 7) * 28 + (b >> 3);
  const bool hasf = (b >> 3) < 28;
  const rsrc_t rwg = __builtin_amdgcn_make_buffer_rsrc((void *)p.wg, 0, hasf ? DM * FF * 2 : 0, RS3);
  const rsrc_t rwu = __builtin_amdgcn_make_buffer_rsrc((void *)p.wu, 0, hasf ? DM * FF * 2 : 0, RS3);
  const int go = (128 * w + 2 * lane) * (FF * 2) + pf * 16;
  v4u wg0 = {0u, 0u, 0u, 0u}, wg1 = wg0, wu0 = wg0, wu1 = wg0;
  const v4u dwg = make_desc(p.wg, hasf ? DM * FF * 2 : 0), dwu = make_desc(p.wu, hasf ? DM * FF * 2 : 0);
  if (!GUSPREAD) {
    wg0 = ld16<WAUX>(rwg, go, 0); wg1 = ld16<WAUX>(rwg, go, FF * 2);
    wu0 = ld16<WAUX>(rwu, go, 0); wu1 = ld16<WAUX>(rwu, go, FF * 2);
  }
  SB();
  float m2 = -3.0e38f, lsum = 0.f;
  f32x4 acc0 = {0.f, 0.f, 0.f, 0.f}, acc1 = acc0;
  for (int it = 0; it < 7; ++it) {
    const int s0 = tbase + (2 * it + 2) * TSTR;
    if (TAU) { while ((unsigned)__builtin_amdgcn_s_memrealtime() - tstart < (unsigned)(TOFF + 2 * it * TAU)) __builtin_amdgcn_s_sleep(1); }
    if (GUSPREAD) {
      ld16_async_at<1>(wg0, dwg, go, 0, it);
      ld16_async_at<2>(wg1, dwg, go, FF * 2, it);
      ld16_async_at<3>(wu0, dwu, go, 0, it);
      ld16_async_at<4>(wu1, dwu, go, FF * 2, it);
      SB();
    }
    tile_step<true>(ta, q, rk, rv, koff, voff, s0, mk0, mk1, m2, lsum, acc0, acc1);
    if (TAU) { while ((unsigned)__builtin_amdgcn_s_memrealtime() - tstart < (unsigned)(TOFF + (2 * it + 1) * TAU)) __builtin_amdgcn_s_sleep(1); }
    tile_step<true>(tb, q, rk, rv, koff, voff, s0 + TSTR, mk0, mk1, m2, lsum, acc0, acc1);
  }
  const rsrc_t rwo = __builtin_amdgcn_make_buffer_rsrc((char *)p.wo + (size_t)b * 16384, 0, 16384, RS3);
  const rsrc_t rwd = __builtin_amdgcn_make_buffer_rsrc((char *)p.wd + (size_t)(hasf ? pf : 0) * 8192, 0, hasf ? 8192 : 0, RS3);
  v4u wo[4], wd[2];
  {
    const int o = g4 * 1024 + w * 256 + (lane & 15) * 16;
#pragma unroll
    for (int i = 0; i < 4; ++i) wo[i] = ld16<WAUX>(rwo, o, i * 4096);
#pragma unroll
    for (int i = 0; i < 2; ++i) wd[i] = ld16<WAUX>(rwd, o, i * 4096);
    SB();
  }
  tile_step<false>(ta, q, rk, rv, koff, voff, 0, mk0, mk1, m2, lsum, acc0, acc1);
  tile_step<false>(tb, q, rk, rv, koff, voff, 0, mk0, mk1, m2, lsum, acc0, acc1);
  STAMP_AT(3);
  {
    const float lw = xr16_sum(lsum);
    *(LAS f32x4 *)(L + L_MRG + w * 512 + g4 * 128 + 8 * (lane & 15)) = acc0;
    *(LAS f32x4 *)(L + L_MRG + w * 512 + g4 * 128 + 8 * (lane & 15) + 4) = acc1;
    if (lane < 16 && u4 == 0) { f32x2 y = {m2, lw}; *(LAS f32x2 *)(L + L_ML + (w * 4 + a4) * 2) = y; }
    WG_BARRIER();
    f32x2 ml[4];
#pragma unroll
    for (int ww = 0; ww < 4; ++ww) ml[ww] = *(LAS f32x2 *)(L + L_ML + (ww * 4 + w) * 2);
    const float M = fmaxf(fmaxf(ml[0][0], ml[1][0]), fmaxf(ml[2][0], ml[3][0]));
    float Ls = 0.f; f32x2 o = {0.f, 0.f};
#pragma unroll
    for (int ww = 0; ww < 4; ++ww) {
      const float e = __builtin_amdgcn_exp2f(ml[ww][0] - M);
      Ls = fmaf(e, ml[ww][1], Ls);
      o += e * *(LAS f32x2 *)(L + L_MRG + ww * 512 + w * 128 + 2 * lane);
    }
    const int head = 4 * kh + w;
    st16(rws, WS_OP + (((head * 8 + (lane >> 3)) * 32 + sp) * 16 + 2 * (lane & 7)) * 8, v4u{asu(o[0]), tag, asu(o[1]), tag});
    if (lane < 8) st16(rws, WS_ML + ((head * 8 + lane) * 32 + sp) * 16, v4u{asu(M), tag, asu(Ls), tag});
  }
  STAMP_AT(4);
  {
    const int off[2] = {WS_OP + b * 4096 + tid * 16, WS_ML + (b * 32 + 8 * w + (lane >> 3)) * 16};
    v4u x[2];
    gather<2>(rws, off, tag, x);
    STAMP_AT(5);
    const float ms = asf(x[1][0]), ls = asf(x[1][2]);
    const float Mw = xr16_max(fmaxf(ms, dpp<ROR8>(ms)));
    const float e = __builtin_amdgcn_exp2f(ms - Mw);
    const float v0 = strided_sum<8>(e * asf(x[0][0])), v1 = strided_sum<8>(e * asf(x[0][2])), lw = strided_sum<8>(e * ls);
    if (lane < 8) {
      const int d = 2 * lane;
      L[L_RED2 + w * 64 + (d & 3) * 4 + (d >> 2)] = v0;
      L[L_RED2 + w * 64 + ((d + 1) & 3) * 4 + (d >> 2)] = v1;
      if (lane == 0) *(LAS f32x2 *)(L + L_RED2 + w * 64 + 32) = f32x2{Mw, lw};
    }
    WG_BARRIER();
    f32x2 ml[4];
#pragma unroll
    for (int ww = 0; ww < 4; ++ww) ml[ww] = *(LAS f32x2 *)(L + L_RED2 + ww * 64 + 32);
    const float M = fmaxf(fmaxf(ml[0][0], ml[1][0]), fmaxf(ml[2][0], ml[3][0]));
    f32x4 o = {0.f, 0.f, 0.f, 0.f};
    float Lt = 0.f;
#pragma unroll
    for (int ww = 0; ww < 4; ++ww) {
      const float ew = __builtin_amdgcn_exp2f(ml[ww][0] - M);
      Lt = fmaf(ew, ml[ww][1], Lt);
      o += ew * *(LAS f32x4 *)(L + L_RED2 + ww * 64 + g4 * 4);
    }
    o *= __builtin_amdgcn_rcpf(Lt);
    f32x4 c0 = {0.f, 0.f, 0.f, 0.f}, c1 = c0;
#pragma unroll
    for (int i = 0; i < 4; ++i) {
      const unsigned pp = cvtpk(o[i], o[i]);
      const v2u a = {pp & mk0, pp & mk1};
      c0 = mfma16(a, wo[i][0], wo[i][1], c0);
      c1 = mfma16(a, wo[i][2], wo[i][3], c1);
    }
    publish512<512 / NB1>(rws, WS_PB, 256, b, c0, c1, L + L_TR, w, lane, tag);
  }
  STAMP_AT(6);
  if (b < NB1) {
    constexpr int C = 512 / NB1, S = C / 2;
    const unsigned xp = __builtin_amdgcn_raw_buffer_load_b32(rx, (b * C + 2 * (lane % S)) * 2, 0, 0);
    const f32x2 r = reduce_cols<C, 256>(rws, WS_PB, b, tag, L + L_RED, tid, w, lane);
    const unsigned hp = cvtpk(lo16(xp) + rbf(r[0]), hi16(xp) + rbf(r[1]));
    if (tid < S * REP) st8(rws, WS_HG + (tid / S) * 2048 + (b * S + tid % S) * 8, v2u{hp, tag});
  }
  STAMP_AT(8);
  if (w == 0) {
    const int base = WS_HG + (b % REP) * 2048 + lane * 32;
    const int off[2] = {base, base + 16};
    v4u x[2];
    gather<2>(rws, off, tag, x);
    const v4u hb = v4u{x[0][0], x[0][2], x[1][0], x[1][2]};
    *(LAS v4u *)(L + L_Q + 4 * lane) = hb;
    const LnStat st = ln_stats(hb);
    *(LAS v4u *)(L + L_XN + 4 * lane) = v4u{ln_pair(hb[0], st), ln_pair(hb[1], st), ln_pair(hb[2], st), ln_pair(hb[3], st)};
  }
  WG_BARRIER();
  STAMP_AT(9);
  if (hasf) {
    const unsigned xsel = ((LAS unsigned *)L)[L_XN + 64 * w + lane];
    const float r = gemv16(xsel, wg0, wg1, wu0, wu1, lane);
    if ((lane & 3) == 0) { const int vi = lane >> 2, col = vi & 7; L[L_RED2 + w * 64 + 16 + (col & 3) * 4 + (vi >> 3) * 2 + (col >> 2)] = r; }
    WG_BARRIER();
    f32x4 gu = {0.f, 0.f, 0.f, 0.f};
#pragma unroll
    for (int ww = 0; ww < 4; ++ww) gu += *(LAS f32x4 *)(L + L_RED2 + ww * 64 + 16 + g4 * 4);
    f32x4 c0 = {0.f, 0.f, 0.f, 0.f}, c1 = c0;
#pragma unroll
    for (int i = 0; i < 2; ++i) {
      const float hv = rbf(gu[i]) * rbf(gu[2 + i]);
      const unsigned pp = cvtpk(hv, hv);
      const v2u a = {pp & mk0, pp & mk1};
      c0 = mfma16(a, wd[i][0], wd[i][1], c0);
      c1 = mfma16(a, wd[i][2], wd[i][3], c1);
    }
    publish512<512 / ND>(rws, WS_PD, NPF, pf, c0, c1, L + L_TR, w, lane, tag);
  }
  STAMP_AT(10);
  if (b >= NWG - ND) {
    constexpr int C = 512 / ND, S = C / 2;
    const int j = b - (NWG - ND);
    const unsigned hp = ((LAS unsigned *)L)[L_Q + j * S + lane % S];
    WG_BARRIER();
    const f32x2 r = reduce_cols<C, NPF>(rws, WS_PD, j, tag, L + L_RED, tid, w, lane);
    const unsigned op = cvtpk(lo16(hp) + rbf(r[0]), hi16(hp) + rbf(r[1]));
    if (w == 0 && lane < S) ((unsigned *)p.out)[j * S + lane] = op;
    if (j == 0 && tid == 0) __builtin_amdgcn_raw_buffer_store_b32(tag, rws, WS_EPOCH, 0, AUX_SC1);
  }
  STAMP_AT(11);
  STAMP_FLUSH();
}
}

extern "C" void kernel_launch(void *const *d_in, const int *in_sizes, int n_in, void *d_out, int out_size, void *d_ws, size_t ws_size,
                              hipStream_t stream) {
  Params p;
  p.x = d_in[0]; p.k = d_in[1]; p.v = d_in[2]; p.wq = d_in[3]; p.wo = d_in[4];
  p.wg = d_in[6]; p.wu = d_in[7]; p.wd = d_in[8];
  p.out = d_out; p.ws = d_ws;
  (void)hipFuncSetAttribute((const void *)fused_decode_kernel, hipFuncAttributeMaxDynamicSharedMemorySize, LDS_BYTES);
  fused_decode_kernel<<<NWG, NTHR, LDS_BYTES, stream>>>(p);
}
```

```cpp
#include <hip/hip_runtime.h>
#include <stdint.h>

#define STAMP 0
#define NB1 64
#define ND 32
#define REP 32
#define KAUX 2
#define VAUX 0
#define WAUX 0
#define PINARGS 1
#define ORD 1
#define HM 1
#define TAU 0
#define TOFF 500
#define TK 60
#define TPOLL 280
#define GUSPREAD 1
#define PROBE 12
#define AMP 10
#define QREP 4

namespace {
typedef unsigned v4u __attribute__((ext_vector_type(4)));
typedef unsigned v2u __attribute__((ext_vector_type(2)));
typedef float f32x4 __attribute__((ext_vector_type(4)));
typedef float f32x2 __attribute__((ext_vector_type(2)));
typedef short s16x4 __attribute__((ext_vector_type(4)));
typedef __bf16 bf16x8 __attribute__((ext_vector_type(8)));
typedef __bf16 bf16x2 __attribute__((ext_vector_type(2)));
typedef __amdgpu_buffer_rsrc_t rsrc_t;
#define LAS __attribute__((address_space(3)))

constexpr int DM = 512, SK = 65536, DH = 128, FF = 1792;
constexpr int NWG = 256, NTHR = 256;
constexpr int NPF = 224;
constexpr int AUX_NT = 2, AUX_SC1 = 16;
constexpr int RS3 = 0x00020000;
constexpr int WS_EPOCH = 0;
constexpr int WS_QG = 4 << 20;
constexpr int WS_ML = 4096;
constexpr int WS_HG = WS_ML + 131072;
constexpr int WS_OP = 1 << 20;
constexpr int WS_PB = 2 << 20;
constexpr int WS_PD = 3 << 20;
constexpr int WS_USED = 5 << 20;
constexpr int WS_STAMP = 8 << 20;
constexpr unsigned SPIN_CAP = 1u << 14;
constexpr float LOG2E = 1.4426950408889634f;
constexpr float SCALE = 0.08838834764831845f;

struct Params {
  const void *x, *k, *v, *wq, *wo, *wg, *wu, *wd;
  void *out, *ws;
};

__device__ __forceinline__ float asf(unsigned u) { return __builtin_bit_cast(float, u); }
__device__ __forceinline__ unsigned asu(float f) { return __builtin_bit_cast(unsigned, f); }
template <int C> __device__ __forceinline__ float dpp(float x) {
  return __builtin_bit_cast(float, __builtin_amdgcn_mov_dpp(__builtin_bit_cast(int, x), C, 0xf, 0xf, true));
}
constexpr int XOR1 = 0xB1, XOR2 = 0x4E, HMIR = 0x141, MIR = 0x140, ROR4 = 0x124, ROR8 = 0x128;
__device__ __forceinline__ unsigned cvtpk(float lo, float hi) {
  f32x2 v = {lo, hi};
  bf16x2 r = __builtin_convertvector(v, bf16x2);
  return __builtin_bit_cast(unsigned, r);
}
__device__ __forceinline__ float lo16(unsigned u) { return asf(u << 16); }
__device__ __forceinline__ float hi16(unsigned u) { return asf(u & 0xffff0000u); }
__device__ __forceinline__ float rbf(float x) { return lo16(cvtpk(x, x)); }
__device__ __forceinline__ float xr16_max(float x) {
  auto s = __builtin_amdgcn_permlane16_swap(asu(x), asu(x), false, false);
  x = fmaxf(asf(s[0]), asf(s[1]));
  auto t = __builtin_amdgcn_permlane32_swap(asu(x), asu(x), false, false);
  return fmaxf(asf(t[0]), asf(t[1]));
}
__device__ __forceinline__ float xr16_sum(float x) {
  auto s = __builtin_amdgcn_permlane16_swap(asu(x), asu(x), false, false);
  x = asf(s[0]) + asf(s[1]);
  auto t = __builtin_amdgcn_permlane32_swap(asu(x), asu(x), false, false);
  return asf(t[0]) + asf(t[1]);
}
__device__ __forceinline__ float row_sum(float x) {
  x += dpp<XOR1>(x); x += dpp<XOR2>(x); x += dpp<HMIR>(x); x += dpp<MIR>(x); return x;
}
__device__ __forceinline__ float row_max(float x) {
  x = fmaxf(x, dpp<XOR1>(x)); x = fmaxf(x, dpp<XOR2>(x)); x = fmaxf(x, dpp<HMIR>(x)); x = fmaxf(x, dpp<MIR>(x)); return x;
}
__device__ __forceinline__ float wave_sum(float x) { return xr16_sum(row_sum(x)); }
__device__ __forceinline__ f32x4 mfma32(v4u a, v4u b, f32x4 c) {
  return __builtin_amdgcn_mfma_f32_16x16x32_bf16(__builtin_bit_cast(bf16x8, a), __builtin_bit_cast(bf16x8, b), c, 0, 0, 0);
}
__device__ __forceinline__ f32x4 mfma16(v2u a, unsigned b0, unsigned b1, f32x4 c) {
  v2u b = {b0, b1};
  return __builtin_amdgcn_mfma_f32_16x16x16bf16_1k(__builtin_bit_cast(s16x4, a), __builtin_bit_cast(s16x4, b), c, 0, 0, 0);
}
template <int AUX> __device__ __forceinline__ v4u ld16(rsrc_t r, int voff, int soff) {
  return __builtin_amdgcn_raw_buffer_load_b128(r, voff, soff, AUX);
}
__device__ __forceinline__ void st16(rsrc_t r, int voff, v4u v) { __builtin_amdgcn_raw_buffer_store_b128(v, r, voff, 0, AUX_SC1); }
__device__ __forceinline__ void st8(rsrc_t r, int voff, v2u v) { __builtin_amdgcn_raw_buffer_store_b64(v, r, voff, 0, AUX_SC1); }
__device__ __forceinline__ v4u make_desc(const void *base, unsigned bytes) {
  const unsigned long long a = (unsigned long long)base;
  return v4u{(unsigned)a, (unsigned)(a >> 32) & 0xffffu, bytes, (unsigned)RS3};
}
template <int K> __device__ __forceinline__ void ld16_async_at(v4u &dst, v4u desc, int voff, int soff, int sel) {
  asm volatile("s_cmp_lg_u32 %4, %5\n\ts_cbranch_scc1 .Lskip%=\n\tbuffer_load_dwordx4 %0, %1, %2, %3 offen\n.Lskip%=:"
               : "+v"(dst) : "v"(voff), "s"(desc), "s"(soff), "s"(sel), "i"(K) : "scc");
}
#define WG_BARRIER() asm volatile("s_waitcnt lgkmcnt(0)\n\ts_barrier" ::: "memory")
#define LDS_WAIT() asm volatile("s_waitcnt lgkmcnt(0)" ::: "memory")
#define SB() __builtin_amdgcn_sched_barrier(0)

__device__ __forceinline__ v4u layernorm8(v4u p) {
  float x[8];
#pragma unroll
  for (int i = 0; i < 4; ++i) { x[2 * i] = lo16(p[i]); x[2 * i + 1] = hi16(p[i]); }
  float s = ((x[0] + x[1]) + (x[2] + x[3])) + ((x[4] + x[5]) + (x[6] + x[7]));
  const float mean = wave_sum(s) * (1.f / DM);
  float q = 0.f;
#pragma unroll
  for (int i = 0; i < 8; ++i) { const float d = x[i] - mean; q = fmaf(d, d, q); }
  const float var = wave_sum(q) * (1.f / DM);
  const float mb = rbf(mean);
  const float den = rbf(sqrtf(rbf(rbf(var) + 1e-5f)));
  v4u r;
#pragma unroll
  for (int i = 0; i < 4; ++i) r[i] = cvtpk(rbf(x[2 * i] - mb) / den, rbf(x[2 * i + 1] - mb) / den);
  return r;
}
struct LnStat { float mb, rden; float den; };
__device__ __forceinline__ LnStat ln_stats(v4u p) {
  float x[8];
#pragma unroll
  for (int i = 0; i < 4; ++i) { x[2 * i] = lo16(p[i]); x[2 * i + 1] = hi16(p[i]); }
  float s = ((x[0] + x[1]) + (x[2] + x[3])) + ((x[4] + x[5]) + (x[6] + x[7]));
  const float mean = wave_sum(s) * (1.f / DM);
  float q = 0.f;
#pragma unroll
  for (int i = 0; i < 8; ++i) { const float d = x[i] - mean; q = fmaf(d, d, q); }
  const float var = wave_sum(q) * (1.f / DM);
  LnStat r; r.mb = rbf(mean); r.den = rbf(__builtin_amdgcn_sqrtf(rbf(rbf(var) + 1e-5f))); r.rden = __builtin_amdgcn_rcpf(r.den);
  return r;
}
__device__ __forceinline__ unsigned ln_pair(unsigned p, LnStat st) { return cvtpk(rbf(lo16(p) - st.mb) * st.rden, rbf(hi16(p) - st.mb) * st.rden); }
__device__ __forceinline__ unsigned pick4(v4u v, int w) {
  const unsigned a = (w & 1) ? v[1] : v[0], b = (w & 1) ? v[3] : v[2];
  return (w & 2) ? b : a;
}
__device__ __forceinline__ float gemv16(unsigned xp, v4u a0, v4u a1, v4u b0, v4u b1, int lane) {
  float v[16];
  const bf16x2 xb = __builtin_bit_cast(bf16x2, xp);
#pragma unroll
  for (int i = 0; i < 4; ++i) {
    const unsigned alo = __builtin_amdgcn_perm(a1[i], a0[i], 0x05040100u), ahi = __builtin_amdgcn_perm(a1[i], a0[i], 0x07060302u);
    const unsigned blo = __builtin_amdgcn_perm(b1[i], b0[i], 0x05040100u), bhi = __builtin_amdgcn_perm(b1[i], b0[i], 0x07060302u);
    v[2 * i] = __builtin_amdgcn_fdot2_f32_bf16(__builtin_bit_cast(bf16x2, alo), xb, 0.f, false);
    v[2 * i + 1] = __builtin_amdgcn_fdot2_f32_bf16(__builtin_bit_cast(bf16x2, ahi), xb, 0.f, false);
    v[8 + 2 * i] = __builtin_amdgcn_fdot2_f32_bf16(__builtin_bit_cast(bf16x2, blo), xb, 0.f, false);
    v[8 + 2 * i + 1] = __builtin_amdgcn_fdot2_f32_bf16(__builtin_bit_cast(bf16x2, bhi), xb, 0.f, false);
  }
  float u[8];
#pragma unroll
  for (int k = 0; k < 8; ++k) { auto s = __builtin_amdgcn_permlane32_swap(asu(v[k]), asu(v[k + 8]), false, false); u[k] = asf(s[0]) + asf(s[1]); }
  float t[4];
#pragma unroll
  for (int k = 0; k < 4; ++k) { auto s = __builtin_amdgcn_permlane16_swap(asu(u[k]), asu(u[k + 4]), false, false); t[k] = asf(s[0]) + asf(s[1]); }
  float y[2];
#pragma unroll
  for (int k = 0; k < 2; ++k) { const float p = t[k] + dpp<ROR8>(t[k]), q = t[k + 2] + dpp<ROR8>(t[k + 2]); y[k] = (lane & 8) ? q : p; }
#pragma unroll
  for (int k = 0; k < 2; ++k) { y[k] += dpp<XOR1>(y[k]); y[k] += dpp<XOR2>(y[k]); y[k] += dpp<HMIR>(y[k]); }
  return (lane & 4) ? y[1] : y[0];
}

__device__ __forceinline__ f32x2 gemv32(const unsigned (&xp)[4], const v4u (&e)[4], const v4u (&o)[4]) {
  float v[8];
#pragma unroll
  for (int c = 0; c < 8; ++c) v[c] = 0.f;
#pragma unroll
  for (int i = 0; i < 4; ++i) {
    const bf16x2 xb = __builtin_bit_cast(bf16x2, xp[i]);
#pragma unroll
    for (int d = 0; d < 4; ++d) {
      const unsigned lo = __builtin_amdgcn_perm(o[i][d], e[i][d], 0x05040100u), hi = __builtin_amdgcn_perm(o[i][d], e[i][d], 0x07060302u);
      v[2 * d] = __builtin_amdgcn_fdot2_f32_bf16(__builtin_bit_cast(bf16x2, lo), xb, v[2 * d], false);
      v[2 * d + 1] = __builtin_amdgcn_fdot2_f32_bf16(__builtin_bit_cast(bf16x2, hi), xb, v[2 * d + 1], false);
    }
  }
  float u[4];
#pragma unroll
  for (int k = 0; k < 4; ++k) { auto s = __builtin_amdgcn_permlane32_swap(asu(v[k]), asu(v[k + 4]), false, false); u[k] = asf(s[0]) + asf(s[1]); }
  f32x2 t;
#pragma unroll
  for (int k = 0; k < 2; ++k) {
    auto s = __builtin_amdgcn_permlane16_swap(asu(u[k]), asu(u[k + 2]), false, false);
    float y = asf(s[0]) + asf(s[1]);
    y += dpp<ROR8>(y); y += dpp<ROR4>(y);
    t[k] = y;
  }
  return t;
}

struct Tile { v4u k[8], v[8]; };

template <bool REFILL>
__device__ __forceinline__ void tile_step(Tile &t, v4u q, rsrc_t rk, rsrc_t rv, int koff, int voff, int snext, unsigned mk0, unsigned mk1,
                                          float &m2, float &lsum, f32x4 &acc0, f32x4 &acc1) {
  float s[8];
  const f32x4 z = {0.f, 0.f, 0.f, 0.f};
  f32x4 d[8];
#pragma unroll
  for (int g = 0; g < 8; ++g) {
    d[g] = mfma32(t.k[g], q, z);
    if (REFILL) t.k[g] = ld16<KAUX>(rk, koff, snext + g * 1024);
    SB();
  }
#pragma unroll
  for (int g = 0; g < 8; ++g) {
    s[g] = ((dpp<0x00>(d[g][0]) + dpp<0x55>(d[g][1])) + dpp<0xAA>(d[g][2])) + dpp<0xFF>(d[g][3]);
    asm("" : "+v"(s[g]));
  }
#pragma unroll
  for (int g = 0; g < 8; g += 2) {
    const unsigned a = cvtpk(s[g], s[g + 1]);
    const unsigned b = cvtpk(lo16(a) * SCALE, hi16(a) * SCALE);
    s[g] = lo16(b); s[g + 1] = hi16(b);
  }
  float mx = fmaxf(fmaxf(fmaxf(s[0], s[1]), fmaxf(s[2], s[3])), fmaxf(fmaxf(s[4], s[5]), fmaxf(s[6], s[7])));
  mx = xr16_max(mx);
  const float mn = fmaxf(m2, mx * LOG2E);
  const float alpha = __builtin_amdgcn_exp2f(m2 - mn);
  m2 = mn;
  lsum *= alpha;
  int ai = __builtin_bit_cast(int, alpha), ar;
  ar = __builtin_amdgcn_update_dpp(ai, ai, 0x150, 0x1, 0xf, false);
  ar = __builtin_amdgcn_update_dpp(ar, ai, 0x154, 0x2, 0xf, false);
  ar = __builtin_amdgcn_update_dpp(ar, ai, 0x158, 0x4, 0xf, false);
  ar = __builtin_amdgcn_update_dpp(ar, ai, 0x15c, 0x8, 0xf, false);
  const float al = __builtin_bit_cast(float, ar);
  acc0 *= al; acc1 *= al;
#pragma unroll
  for (int g = 0; g < 8; ++g) {
    const float p = __builtin_amdgcn_exp2f(fmaf(s[g], LOG2E, -mn));
    lsum += p;
    const unsigned pp = cvtpk(p, p);
    const v2u a = {pp & mk0, pp & mk1};
    acc0 = mfma16(a, t.v[g][0], t.v[g][1], acc0);
    acc1 = mfma16(a, t.v[g][2], t.v[g][3], acc1);
    if (REFILL) t.v[g] = ld16<VAUX>(rv, voff, snext + g * 1024);
    SB();
  }
}

template <int N>
__device__ __forceinline__ void gather(rsrc_t rs, const int (&off)[N], unsigned tag, v4u (&x)[N]) {
  for (unsigned spins = 0;; ++spins) {
    bool ok = true;
    asm volatile("" ::: "memory");
#pragma unroll
    for (int i = 0; i < N; ++i) x[i] = ld16<AUX_SC1>(rs, off[i], 0);
#pragma unroll
    for (int i = 0; i < N; ++i) ok = ok & (x[i][1] == tag) & (x[i][3] == tag);
    if (__all(ok) || spins > SPIN_CAP) break;
    __builtin_amdgcn_s_sleep(1);
  }
}
template <int S> __device__ __forceinline__ float strided_sum(float x) {
  if (S <= 1) x += dpp<XOR1>(x);
  if (S <= 2) x += dpp<XOR2>(x);
  if (S <= 8) x += dpp<ROR8>(x);
  if (S <= 4) x += dpp<ROR4>(x);
  return xr16_sum(x);
}
template <int C>
__device__ __forceinline__ void publish512(rsrc_t rs, int base, int nprod, int p, f32x4 a0, f32x4 a1, LAS float *tr, int w, int lane, unsigned tag) {
  if (lane < 16) {
    *(LAS f32x4 *)(tr + w * 128 + 8 * lane) = a0;
    *(LAS f32x4 *)(tr + w * 128 + 8 * lane + 4) = a1;
  }
  LDS_WAIT();
  const f32x2 y = *(LAS f32x2 *)(tr + w * 128 + 2 * lane);
  const int col = 128 * w + 2 * lane, j = col / C, within = col % C;
  const v4u g = {asu(y[0]), tag, asu(y[1]), tag};
  st16(rs, base + ((j * nprod + p) * C + within) * 8, g);
}
template <int C, int NP>
__device__ __forceinline__ f32x2 reduce_cols(rsrc_t rs, int base, int j, unsigned tag, LAS float *red, int tid, int w, int lane) {
  constexpr int S = C / 2, NPIECE = NP * S, N = (NPIECE + NTHR - 1) / NTHR;
  int off[N]; v4u x[N];
#pragma unroll
  for (int i = 0; i < N; ++i) { const int pc = i * NTHR + tid; off[i] = base + j * (NP * C * 8) + (pc < NPIECE ? pc : tid) * 16; }
  gather<N>(rs, off, tag, x);
  float s0 = 0.f, s1 = 0.f;
#pragma unroll
  for (int i = 0; i < N; ++i) { const bool live = i * NTHR + tid < NPIECE; s0 += live ? asf(x[i][0]) : 0.f; s1 += live ? asf(x[i][2]) : 0.f; }
  s0 = strided_sum<S>(s0); s1 = strided_sum<S>(s1);
  if (lane < S) { f32x2 y = {s0, s1}; *(LAS f32x2 *)(red + w * 64 + 2 * lane) = y; }
  WG_BARRIER();
  f32x2 r = {0.f, 0.f};
#pragma unroll
  for (int ww = 0; ww < 4; ++ww) r += *(LAS f32x2 *)(red + ww * 64 + 2 * (lane % S));
  return r;
}

#define STAMP_AT(i) do { if ((i) == PROBE) { SB(); probe_t = (unsigned)__builtin_amdgcn_s_memrealtime(); SB(); } } while (0)
#define STAMP_FLUSH() do { const unsigned x_ = probe_t ? probe_t - tstart : 0u; while ((unsigned)__builtin_amdgcn_s_memrealtime() - tstart < 7000u + AMP * x_) __builtin_amdgcn_s_sleep(2); } while (0)

constexpr int L_MRG = 0;
constexpr int L_ML = L_MRG + 2048;
constexpr int L_RED = L_ML + 32;
constexpr int L_RED2 = L_RED + 256;
constexpr int L_TR = L_RED2 + 256;
constexpr int L_Q = L_TR + 512;
constexpr int L_XN = L_Q + 256;
constexpr int L_END = L_XN + 1024;
constexpr int LDS_BYTES = 84 * 1024;

__global__ void __launch_bounds__(NTHR, 2) fused_decode_kernel(Params p) {
  extern __shared__ __attribute__((aligned(16))) float smem[];
  LAS float *L = (LAS float *)smem;
  const int tid = threadIdx.x, lane = tid & 63, w = __builtin_amdgcn_readfirstlane(tid >> 6), b = blockIdx.x;
  asm volatile("" :: "s"(p.x), "s"(p.k), "s"(p.v), "s"(p.wq), "s"(p.wo), "s"(p.wg), "s"(p.wu), "s"(p.wd), "s"(p.out), "s"(p.ws));
  unsigned probe_t = 0;
  const unsigned tstart = (unsigned)__builtin_amdgcn_s_memrealtime();
  const int kh = HM ? b >> 5 : b & 7, sp = HM ? b & 31 : b >> 3;
  const rsrc_t rws = __builtin_amdgcn_make_buffer_rsrc(p.ws, 0, WS_USED, RS3);
  const rsrc_t rk = __builtin_amdgcn_make_buffer_rsrc((char *)p.k + (size_t)kh * SK * 256, 0, SK * 256, RS3);
  const rsrc_t rv = __builtin_amdgcn_make_buffer_rsrc((char *)p.v + (size_t)kh * SK * 256, 0, SK * 256, RS3);
  const int tbase = ORD == 1 ? sp * 524288 + w * 8192 : ORD == 2 ? (sp * 4 + w) * 8192 : (w * 32 + sp) * 8192;
  constexpr int TSTR = ORD == 1 ? 4 * 8192 : 128 * 8192;
  const rsrc_t rx = __builtin_amdgcn_make_buffer_rsrc((void *)p.x, 0, DM * 2, RS3);
  const rsrc_t rwq = __builtin_amdgcn_make_buffer_rsrc((void *)p.wq, 0, DM * 4096 * 2, RS3);
  const unsigned epoch = *(const __attribute__((address_space(4))) unsigned *)((const char *)p.ws + WS_EPOCH);
  const bool hasq = (b & 7) < 4;
  const int pq = (b >> 3) * 4 + (b & 3), hq = pq & 31, rb = pq >> 5;
  const int g4 = lane >> 4, a4 = (lane >> 2) & 3, u4 = lane & 3;
  const int koff = a4 * 256 + (4 * g4 + u4) * 16, voff = lane * 16;
  const unsigned mk0 = u4 == 0 ? 0xFFFFu : u4 == 1 ? 0xFFFF0000u : 0u;
  const unsigned mk1 = u4 == 2 ? 0xFFFFu : u4 == 3 ? 0xFFFF0000u : 0u;
  v4u wqf[8];
  unsigned xq[8];
  Tile ta, tb;
  if (hasq) {
    const v4u xr = ld16<0>(rx, lane * 16, 0);
    const int wqoff = (128 * rb + 32 * w + g4) * 8192 + hq * 256 + (lane & 15) * 16;
#pragma unroll
    for (int i = 0; i < 8; ++i) wqf[i] = ld16<WAUX>(rwq, wqoff, i * 4 * 8192);
    SB();
    const LnStat st = ln_stats(xr);
    *(LAS v4u *)(L + L_XN + w * 256 + 4 * lane) = v4u{ln_pair(xr[0], st), ln_pair(xr[1], st), ln_pair(xr[2], st), ln_pair(xr[3], st)};
    LDS_WAIT();
#pragma unroll
    for (int i = 0; i < 8; ++i) xq[i] = ((LAS unsigned short *)(L + L_XN + w * 256))[128 * rb + 32 * w + 4 * i + g4];
    STAMP_AT(12);
  }
  if (hasq) {
    f32x4 c0 = {0.f, 0.f, 0.f, 0.f}, c1 = c0;
#pragma unroll
    for (int i = 0; i < 8; ++i) {
      const unsigned pp = xq[i] | (xq[i] << 16);
      const v2u a = {pp & mk0, pp & mk1};
      c0 = mfma16(a, wqf[i][0], wqf[i][1], c0);
      c1 = mfma16(a, wqf[i][2], wqf[i][3], c1);
    }
    if (lane < 16) {
      *(LAS f32x4 *)(L + L_TR + w * 128 + 8 * lane) = c0;
      *(LAS f32x4 *)(L + L_TR + w * 128 + 8 * lane + 4) = c1;
    }
    STAMP_AT(13);
    WG_BARRIER();
    if (w < QREP) {
      f32x2 y = {0.f, 0.f};
#pragma unroll
      for (int ww = 0; ww < 4; ++ww) y += *(LAS f32x2 *)(L + L_TR + ww * 128 + 2 * lane);
      st16(rws, WS_QG + w * 131072 + ((hq * 4 + rb) * 128 + 2 * lane) * 8, v4u{asu(y[0]), epoch + 1u, asu(y[1]), epoch + 1u});
    }
  }
  STAMP_AT(1);
#pragma unroll
  for (int g = 0; g < 8; ++g) ta.k[g] = ld16<KAUX>(rk, koff, tbase + g * 1024);
#pragma unroll
  for (int g = 0; g < 8; ++g) ta.v[g] = ld16<VAUX>(rv, voff, tbase + g * 1024);
  SB();
  const unsigned tag = epoch + 1u;
  while ((unsigned)__builtin_amdgcn_s_memrealtime() - tstart < (unsigned)TPOLL) __builtin_amdgcn_s_sleep(1);
  v4u q;
  {
    const int qoff = WS_QG + (sp & (QREP - 1)) * 131072 + (4 * kh + w) * 4096 + lane * 16;
    v4u x[4];
#pragma unroll
    for (int i = 0; i < 4; ++i) x[i] = ld16<AUX_SC1>(rws, qoff, i * 1024);
    SB();
    asm volatile("s_barrier" ::: "memory");
    SB();
#pragma unroll
    for (int g = 0; g < 8; ++g) tb.k[g] = ld16<KAUX>(rk, koff, tbase + TSTR + g * 1024);
#pragma unroll
    for (int g = 0; g < 8; ++g) tb.v[g] = ld16<VAUX>(rv, voff, tbase + TSTR + g * 1024);
    SB();
    bool ok = true;
#pragma unroll
    for (int i = 0; i < 4; ++i) ok = ok & (x[i][1] == tag) & (x[i][3] == tag);
    { bool o2 = __all(ok); asm volatile("" :: "s"((int)o2)); STAMP_AT(14); }
    if (__builtin_expect(!__all(ok), 0)) {
      for (unsigned spins = 0; spins < SPIN_CAP; ++spins) {
        bool ok2 = true;
        asm volatile("" ::: "memory");
#pragma unroll
        for (int i = 0; i < 4; ++i) x[i] = ld16<AUX_SC1>(rws, qoff, i * 1024);
#pragma unroll
        for (int i = 0; i < 4; ++i) ok2 = ok2 & (x[i][1] == tag) & (x[i][3] == tag);
        if (__all(ok2)) break;
      }
      __builtin_amdgcn_s_waitcnt(0x0F70);
    }
    ((LAS unsigned *)L)[L_Q + w * 64 + lane] = cvtpk((asf(x[0][0]) + asf(x[1][0])) + (asf(x[2][0]) + asf(x[3][0])),
                                                     (asf(x[0][2]) + asf(x[1][2])) + (asf(x[2][2]) + asf(x[3][2])));
    WG_BARRIER();
    q = *(LAS v4u *)(L + L_Q + a4 * 64 + (4 * g4 + u4) * 4);
  }
  STAMP_AT(2);
  const int pf = (b & 7) * 28 + (b >> 3);
  const bool hasf = (b >> 3) < 28;
  const rsrc_t rwg = __builtin_amdgcn_make_buffer_rsrc((void *)p.wg, 0, hasf ? DM * FF * 2 : 0, RS3);
  const rsrc_t rwu = __builtin_amdgcn_make_buffer_rsrc((void *)p.wu, 0, hasf ? DM * FF * 2 : 0, RS3);
  const int go = (128 * w + 2 * lane) * (FF * 2) + pf * 16;
  v4u wg0 = {0u, 0u, 0u, 0u}, wg1 = wg0, wu0 = wg0, wu1 = wg0;
  const v4u dwg = make_desc(p.wg, hasf ? DM * FF * 2 : 0), dwu = make_desc(p.wu, hasf ? DM * FF * 2 : 0);
  if (!GUSPREAD) {
    wg0 = ld16<WAUX>(rwg, go, 0); wg1 = ld16<WAUX>(rwg, go, FF * 2);
    wu0 = ld16<WAUX>(rwu, go, 0); wu1 = ld16<WAUX>(rwu, go, FF * 2);
  }
  SB();
  float m2 = -3.0e38f, lsum = 0.f;
  f32x4 acc0 = {0.f, 0.f, 0.f, 0.f}, acc1 = acc0;
  for (int it = 0; it < 7; ++it) {
    const int s0 = tbase + (2 * it + 2) * TSTR;
    if (TAU) { while ((unsigned)__builtin_amdgcn_s_memrealtime() - tstart < (unsigned)(TOFF + 2 * it * TAU)) __builtin_amdgcn_s_sleep(1); }
    if (GUSPREAD) {
      ld16_async_at<1>(wg0, dwg, go, 0, it);
      ld16_async_at<2>(wg1, dwg, go, FF * 2, it);
      ld16_async_at<3>(wu0, dwu, go, 0, it);
      ld16_async_at<4>(wu1, dwu, go, FF * 2, it);
      SB();
    }
    tile_step<true>(ta, q, rk, rv, koff, voff, s0, mk0, mk1, m2, lsum, acc0, acc1);
    if (TAU) { while ((unsigned)__builtin_amdgcn_s_memrealtime() - tstart < (unsigned)(TOFF + (2 * it + 1) * TAU)) __builtin_amdgcn_s_sleep(1); }
    tile_step<true>(tb, q, rk, rv, koff, voff, s0 + TSTR, mk0, mk1, m2, lsum, acc0, acc1);
  }
  const rsrc_t rwo = __builtin_amdgcn_make_buffer_rsrc((char *)p.wo + (size_t)b * 16384, 0, 16384, RS3);
  const rsrc_t rwd = __builtin_amdgcn_make_buffer_rsrc((char *)p.wd + (size_t)(hasf ? pf : 0) * 8192, 0, hasf ? 8192 : 0, RS3);
  v4u wo[4], wd[2];
  {
    const int o = g4 * 1024 + w * 256 + (lane & 15) * 16;
#pragma unroll
    for (int i = 0; i < 4; ++i) wo[i] = ld16<WAUX>(rwo, o, i * 4096);
#pragma unroll
    for (int i = 0; i < 2; ++i) wd[i] = ld16<WAUX>(rwd, o, i * 4096);
    SB();
  }
  tile_step<false>(ta, q, rk, rv, koff, voff, 0, mk0, mk1, m2, lsum, acc0, acc1);
  tile_step<false>(tb, q, rk, rv, koff, voff, 0, mk0, mk1, m2, lsum, acc0, acc1);
  STAMP_AT(3);
  {
    const float lw = xr16_sum(lsum);
    *(LAS f32x4 *)(L + L_MRG + w * 512 + g4 * 128 + 8 * (lane & 15)) = acc0;
    *(LAS f32x4 *)(L + L_MRG + w * 512 + g4 * 128 + 8 * (lane & 15) + 4) = acc1;
    if (lane < 16 && u4 == 0) { f32x2 y = {m2, lw}; *(LAS f32x2 *)(L + L_ML + (w * 4 + a4) * 2) = y; }
    WG_BARRIER();
    f32x2 ml[4];
#pragma unroll
    for (int ww = 0; ww < 4; ++ww) ml[ww] = *(LAS f32x2 *)(L + L_ML + (ww * 4 + w) * 2);
    const float M = fmaxf(fmaxf(ml[0][0], ml[1][0]), fmaxf(ml[2][0], ml[3][0]));
    float Ls = 0.f; f32x2 o = {0.f, 0.f};
#pragma unroll
    for (int ww = 0; ww < 4; ++ww) {
      const float e = __builtin_amdgcn_exp2f(ml[ww][0] - M);
      Ls = fmaf(e, ml[ww][1], Ls);
      o += e * *(LAS f32x2 *)(L + L_MRG + ww * 512 + w * 128 + 2 * lane);
    }
    const int head = 4 * kh + w;
    st16(rws, WS_OP + (((head * 8 + (lane >> 3)) * 32 + sp) * 16 + 2 * (lane & 7)) * 8, v4u{asu(o[0]), tag, asu(o[1]), tag});
    if (lane < 8) st16(rws, WS_ML + ((head * 8 + lane) * 32 + sp) * 16, v4u{asu(M), tag, asu(Ls), tag});
  }
  STAMP_AT(4);
  {
    const int off[2] = {WS_OP + b * 4096 + tid * 16, WS_ML + (b * 32 + 8 * w + (lane >> 3)) * 16};
    v4u x[2];
    gather<2>(rws, off, tag, x);
    STAMP_AT(5);
    const float ms = asf(x[1][0]), ls = asf(x[1][2]);
    const float Mw = xr16_max(fmaxf(ms, dpp<ROR8>(ms)));
    const float e = __builtin_amdgcn_exp2f(ms - Mw);
    const float v0 = strided_sum<8>(e * asf(x[0][0])), v1 = strided_sum<8>(e * asf(x[0][2])), lw = strided_sum<8>(e * ls);
    if (lane < 8) {
      const int d = 2 * lane;
      L[L_RED2 + w * 64 + (d & 3) * 4 + (d >> 2)] = v0;
      L[L_RED2 + w * 64 + ((d + 1) & 3) * 4 + (d >> 2)] = v1;
      if (lane == 0) *(LAS f32x2 *)(L + L_RED2 + w * 64 + 32) = f32x2{Mw, lw};
    }
    WG_BARRIER();
    f32x2 ml[4];
#pragma unroll
    for (int ww = 0; ww < 4; ++ww) ml[ww] = *(LAS f32x2 *)(L + L_RED2 + ww * 64 + 32);
    const float M = fmaxf(fmaxf(ml[0][0], ml[1][0]), fmaxf(ml[2][0], ml[3][0]));
    f32x4 o = {0.f, 0.f, 0.f, 0.f};
    float Lt = 0.f;
#pragma unroll
    for (int ww = 0; ww < 4; ++ww) {
      const float ew = __builtin_amdgcn_exp2f(ml[ww][0] - M);
      Lt = fmaf(ew, ml[ww][1], Lt);
      o += ew * *(LAS f32x4 *)(L + L_RED2 + ww * 64 + g4 * 4);
    }
    o *= __builtin_amdgcn_rcpf(Lt);
    f32x4 c0 = {0.f, 0.f, 0.f, 0.f}, c1 = c0;
#pragma unroll
    for (int i = 0; i < 4; ++i) {
      const unsigned pp = cvtpk(o[i], o[i]);
      const v2u a = {pp & mk0, pp & mk1};
      c0 = mfma16(a, wo[i][0], wo[i][1], c0);
      c1 = mfma16(a, wo[i][2], wo[i][3], c1);
    }
    publish512<512 / NB1>(rws, WS_PB, 256, b, c0, c1, L + L_TR, w, lane, tag);
  }
  STAMP_AT(6);
  if (b < NB1) {
    constexpr int C = 512 / NB1, S = C / 2;
    const unsigned xp = __builtin_amdgcn_raw_buffer_load_b32(rx, (b * C + 2 * (lane % S)) * 2, 0, 0);
    const f32x2 r = reduce_cols<C, 256>(rws, WS_PB, b, tag, L + L_RED, tid, w, lane);
    const unsigned hp = cvtpk(lo16(xp) + rbf(r[0]), hi16(xp) + rbf(r[1]));
    if (tid < S * REP) st8(rws, WS_HG + (tid / S) * 2048 + (b * S + tid % S) * 8, v2u{hp, tag});
  }
  STAMP_AT(8);
  if (w == 0) {
    const int base = WS_HG + (b % REP) * 2048 + lane * 32;
    const int off[2] = {base, base + 16};
    v4u x[2];
    gather<2>(rws, off, tag, x);
    const v4u hb = v4u{x[0][0], x[0][2], x[1][0], x[1][2]};
    *(LAS v4u *)(L + L_Q + 4 * lane) = hb;
    const LnStat st = ln_stats(hb);
    *(LAS v4u *)(L + L_XN + 4 * lane) = v4u{ln_pair(hb[0], st), ln_pair(hb[1], st), ln_pair(hb[2], st), ln_pair(hb[3], st)};
  }
  WG_BARRIER();
  STAMP_AT(9);
  if (hasf) {
    const unsigned xsel = ((LAS unsigned *)L)[L_XN + 64 * w + lane];
    const float r = gemv16(xsel, wg0, wg1, wu0, wu1, lane);
    if ((lane & 3) == 0) { const int vi = lane >> 2, col = vi & 7; L[L_RED2 + w * 64 + 16 + (col & 3) * 4 + (vi >> 3) * 2 + (col >> 2)] = r; }
    WG_BARRIER();
    f32x4 gu = {0.f, 0.f, 0.f, 0.f};
#pragma unroll
    for (int ww = 0; ww < 4; ++ww) gu += *(LAS f32x4 *)(L + L_RED2 + ww * 64 + 16 + g4 * 4);
    f32x4 c0 = {0.f, 0.f, 0.f, 0.f}, c1 = c0;
#pragma unroll
    for (int i = 0; i < 2; ++i) {
      const float hv = rbf(gu[i]) * rbf(gu[2 + i]);
      const unsigned pp = cvtpk(hv, hv);
      const v2u a = {pp & mk0, pp & mk1};
      c0 = mfma16(a, wd[i][0], wd[i][1], c0);
      c1 = mfma16(a, wd[i][2], wd[i][3], c1);
    }
    publish512<512 / ND>(rws, WS_PD, NPF, pf, c0, c1, L + L_TR, w, lane, tag);
  }
  STAMP_AT(10);
  if (b >= NWG - ND) {
    constexpr int C = 512 / ND, S = C / 2;
    const int j = b - (NWG - ND);
    const unsigned hp = ((LAS unsigned *)L)[L_Q + j * S + lane % S];
    WG_BARRIER();
    const f32x2 r = reduce_cols<C, NPF>(rws, WS_PD, j, tag, L + L_RED, tid, w, lane);
    const unsigned op = cvtpk(lo16(hp) + rbf(r[0]), hi16(hp) + rbf(r[1]));
    if (w == 0 && lane < S) ((unsigned *)p.out)[j * S + lane] = op;
    if (j == 0 && tid == 0) __builtin_amdgcn_raw_buffer_store_b32(tag, rws, WS_EPOCH, 0, AUX_SC1);
  }
  STAMP_AT(11);
  STAMP_FLUSH();
}
}

extern "C" void kernel_launch(void *const *d_in, const int *in_sizes, int n_in, void *d_out, int out_size, void *d_ws, size_t ws_size,
                              hipStream_t stream) {
  Params p;
  p.x = d_in[0]; p.k = d_in[1]; p.v = d_in[2]; p.wq = d_in[3]; p.wo = d_in[4];
  p.wg = d_in[6]; p.wu = d_in[7]; p.wd = d_in[8];
  p.out = d_out; p.ws = d_ws;
  (void)hipFuncSetAttribute((const void *)fused_decode_kernel, hipFuncAttributeMaxDynamicSharedMemorySize, LDS_BYTES);
  fused_decode_kernel<<<NWG, NTHR, LDS_BYTES, stream>>>(p);
}
```

```cpp
#include <hip/hip_runtime.h>
#include <stdint.h>

#define STAMP 0
#define NB1 64
#define ND 32
#define REP 32
#define KAUX 2
#define VAUX 2
#define WAUX 2
#define PINARGS 1
#define ORD 1
#define HM 1
#define TAU 0
#define TOFF 500
#define TK 60
#define TPOLL 280
#define GUSPREAD 1
#define QREP 4

namespace {
typedef unsigned v4u __attribute__((ext_vector_type(4)));
typedef unsigned v2u __attribute__((ext_vector_type(2)));
typedef float f32x4 __attribute__((ext_vector_type(4)));
typedef float f32x2 __attribute__((ext_vector_type(2)));
typedef short s16x4 __attribute__((ext_vector_type(4)));
typedef __bf16 bf16x8 __attribute__((ext_vector_type(8)));
typedef __bf16 bf16x2 __attribute__((ext_vector_type(2)));
typedef __amdgpu_buffer_rsrc_t rsrc_t;
#define LAS __attribute__((address_space(3)))

constexpr int DM = 512, SK = 65536, DH = 128, FF = 1792;
constexpr int NWG = 256, NTHR = 256;
constexpr int NPF = 224;
constexpr int AUX_NT = 2, AUX_SC1 = 16;
constexpr int RS3 = 0x00020000;
constexpr int WS_EPOCH = 0;
constexpr int WS_QG = 4 << 20;
constexpr int WS_ML = 4096;
constexpr int WS_HG = WS_ML + 131072;
constexpr int WS_OP = 1 << 20;
constexpr int WS_PB = 2 << 20;
constexpr int WS_PD = 3 << 20;
constexpr int WS_USED = 5 << 20;
constexpr int WS_STAMP = 8 << 20;
constexpr unsigned SPIN_CAP = 1u << 14;
constexpr float LOG2E = 1.4426950408889634f;
constexpr float SCALE = 0.08838834764831845f;

struct Params {
  const void *x, *k, *v, *wq, *wo, *wg, *wu, *wd;
  void *out, *ws;
};

__device__ __forceinline__ float asf(unsigned u) { return __builtin_bit_cast(float, u); }
__device__ __forceinline__ unsigned asu(float f) { return __builtin_bit_cast(unsigned, f); }
template <int C> __device__ __forceinline__ float dpp(float x) {
  return __builtin_bit_cast(float, __builtin_amdgcn_mov_dpp(__builtin_bit_cast(int, x), C, 0xf, 0xf, true));
}
constexpr int XOR1 = 0xB1, XOR2 = 0x4E, HMIR = 0x141, MIR = 0x140, ROR4 = 0x124, ROR8 = 0x128;
__device__ __forceinline__ unsigned cvtpk(float lo, float hi) {
  f32x2 v = {lo, hi};
  bf16x2 r = __builtin_convertvector(v, bf16x2);
  return __builtin_bit_cast(unsigned, r);
}
__device__ __forceinline__ float lo16(unsigned u) { return asf(u << 16); }
__device__ __forceinline__ float hi16(unsigned u) { return asf(u & 0xffff0000u); }
__device__ __forceinline__ float rbf(float x) { return lo16(cvtpk(x, x)); }
__device__ __forceinline__ float xr16_max(float x) {
  auto s = __builtin_amdgcn_permlane16_swap(asu(x), asu(x), false, false);
  x = fmaxf(asf(s[0]), asf(s[1]));
  auto t = __builtin_amdgcn_permlane32_swap(asu(x), asu(x), false, false);
  return fmaxf(asf(t[0]), asf(t[1]));
}
__device__ __forceinline__ float xr16_sum(float x) {
  auto s = __builtin_amdgcn_permlane16_swap(asu(x), asu(x), false, false);
  x = asf(s[0]) + asf(s[1]);
  auto t = __builtin_amdgcn_permlane32_swap(asu(x), asu(x), false, false);
  return asf(t[0]) + asf(t[1]);
}
__device__ __forceinline__ float row_sum(float x) {
  x += dpp<XOR1>(x); x += dpp<XOR2>(x); x += dpp<HMIR>(x); x += dpp<MIR>(x); return x;
}
__device__ __forceinline__ float row_max(float x) {
  x = fmaxf(x, dpp<XOR1>(x)); x = fmaxf(x, dpp<XOR2>(x)); x = fmaxf(x, dpp<HMIR>(x)); x = fmaxf(x, dpp<MIR>(x)); return x;
}
__device__ __forceinline__ float wave_sum(float x) { return xr16_sum(row_sum(x)); }
__device__ __forceinline__ f32x4 mfma32(v4u a, v4u b, f32x4 c) {
  return __builtin_amdgcn_mfma_f32_16x16x32_bf16(__builtin_bit_cast(bf16x8, a), __builtin_bit_cast(bf16x8, b), c, 0, 0, 0);
}
__device__ __forceinline__ f32x4 mfma16(v2u a, unsigned b0, unsigned b1, f32x4 c) {
  v2u b = {b0, b1};
  return __builtin_amdgcn_mfma_f32_16x16x16bf16_1k(__builtin_bit_cast(s16x4, a), __builtin_bit_cast(s16x4, b), c, 0, 0, 0);
}
template <int AUX> __device__ __forceinline__ v4u ld16(rsrc_t r, int voff, int soff) {
  return __builtin_amdgcn_raw_buffer_load_b128(r, voff, soff, AUX);
}
__device__ __forceinline__ void st16(rsrc_t r, int voff, v4u v) { __builtin_amdgcn_raw_buffer_store_b128(v, r, voff, 0, AUX_SC1); }
__device__ __forceinline__ void st8(rsrc_t r, int voff, v2u v) { __builtin_amdgcn_raw_buffer_store_b64(v, r, voff, 0, AUX_SC1); }
__device__ __forceinline__ v4u make_desc(const void *base, unsigned bytes) {
  const unsigned long long a = (unsigned long long)base;
  return v4u{(unsigned)a, (unsigned)(a >> 32) & 0xffffu, bytes, (unsigned)RS3};
}
template <int K> __device__ __forceinline__ void ld16_async_at(v4u &dst, v4u desc, int voff, int soff, int sel) {
  asm volatile("s_cmp_lg_u32 %4, %5\n\ts_cbranch_scc1 .Lskip%=\n\tbuffer_load_dwordx4 %0, %1, %2, %3 offen\n.Lskip%=:"
               : "+v"(dst) : "v"(voff), "s"(desc), "s"(soff), "s"(sel), "i"(K) : "scc");
}
#define WG_BARRIER() asm volatile("s_waitcnt lgkmcnt(0)\n\ts_barrier" ::: "memory")
#define LDS_WAIT() asm volatile("s_waitcnt lgkmcnt(0)" ::: "memory")
#define SB() __builtin_amdgcn_sched_barrier(0)

__device__ __forceinline__ v4u layernorm8(v4u p) {
  float x[8];
#pragma unroll
  for (int i = 0; i < 4; ++i) { x[2 * i] = lo16(p[i]); x[2 * i + 1] = hi16(p[i]); }
  float s = ((x[0] + x[1]) + (x[2] + x[3])) + ((x[4] + x[5]) + (x[6] + x[7]));
  const float mean = wave_sum(s) * (1.f / DM);
  float q = 0.f;
#pragma unroll
  for (int i = 0; i < 8; ++i) { const float d = x[i] - mean; q = fmaf(d, d, q); }
  const float var = wave_sum(q) * (1.f / DM);
  const float mb = rbf(mean);
  const float den = rbf(sqrtf(rbf(rbf(var) + 1e-5f)));
  v4u r;
#pragma unroll
  for (int i = 0; i < 4; ++i) r[i] = cvtpk(rbf(x[2 * i] - mb) / den, rbf(x[2 * i + 1] - mb) / den);
  return r;
}
struct LnStat { float mb, rden; float den; };
__device__ __forceinline__ LnStat ln_stats(v4u p) {
  float x[8];
#pragma unroll
  for (int i = 0; i < 4; ++i) { x[2 * i] = lo16(p[i]); x[2 * i + 1] = hi16(p[i]); }
  float s = ((x[0] + x[1]) + (x[2] + x[3])) + ((x[4] + x[5]) + (x[6] + x[7]));
  const float mean = wave_sum(s) * (1.f / DM);
  float q = 0.f;
#pragma unroll
  for (int i = 0; i < 8; ++i) { const float d = x[i] - mean; q = fmaf(d, d, q); }
  const float var = wave_sum(q) * (1.f / DM);
  LnStat r; r.mb = rbf(mean); r.den = rbf(__builtin_amdgcn_sqrtf(rbf(rbf(var) + 1e-5f))); r.rden = __builtin_amdgcn_rcpf(r.den);
  return r;
}
__device__ __forceinline__ unsigned ln_pair(unsigned p, LnStat st) { return cvtpk(rbf(lo16(p) - st.mb) * st.rden, rbf(hi16(p) - st.mb) * st.rden); }
__device__ __forceinline__ unsigned pick4(v4u v, int w) {
  const unsigned a = (w & 1) ? v[1] : v[0], b = (w & 1) ? v[3] : v[2];
  return (w & 2) ? b : a;
}
__device__ __forceinline__ float gemv16(unsigned xp, v4u a0, v4u a1, v4u b0, v4u b1, int lane) {
  float v[16];
  const bf16x2 xb = __builtin_bit_cast(bf16x2, xp);
#pragma unroll
  for (int i = 0; i < 4; ++i) {
    const unsigned alo = __builtin_amdgcn_perm(a1[i], a0[i], 0x05040100u), ahi = __builtin_amdgcn_perm(a1[i], a0[i], 0x07060302u);
    const unsigned blo = __builtin_amdgcn_perm(b1[i], b0[i], 0x05040100u), bhi = __builtin_amdgcn_perm(b1[i], b0[i], 0x07060302u);
    v[2 * i] = __builtin_amdgcn_fdot2_f32_bf16(__builtin_bit_cast(bf16x2, alo), xb, 0.f, false);
    v[2 * i + 1] = __builtin_amdgcn_fdot2_f32_bf16(__builtin_bit_cast(bf16x2, ahi), xb, 0.f, false);
    v[8 + 2 * i] = __builtin_amdgcn_fdot2_f32_bf16(__builtin_bit_cast(bf16x2, blo), xb, 0.f, false);
    v[8 + 2 * i + 1] = __builtin_amdgcn_fdot2_f32_bf16(__builtin_bit_cast(bf16x2, bhi), xb, 0.f, false);
  }
  float u[8];
#pragma unroll
  for (int k = 0; k < 8; ++k) { auto s = __builtin_amdgcn_permlane32_swap(asu(v[k]), asu(v[k + 8]), false, false); u[k] = asf(s[0]) + asf(s[1]); }
  float t[4];
#pragma unroll
  for (int k = 0; k < 4; ++k) { auto s = __builtin_amdgcn_permlane16_swap(asu(u[k]), asu(u[k + 4]), false, false); t[k] = asf(s[0]) + asf(s[1]); }
  float y[2];
#pragma unroll
  for (int k = 0; k < 2; ++k) { const float p = t[k] + dpp<ROR8>(t[k]), q = t[k + 2] + dpp<ROR8>(t[k + 2]); y[k] = (lane & 8) ? q : p; }
#pragma unroll
  for (int k = 0; k < 2; ++k) { y[k] += dpp<XOR1>(y[k]); y[k] += dpp<XOR2>(y[k]); y[k] += dpp<HMIR>(y[k]); }
  return (lane & 4) ? y[1] : y[0];
}

__device__ __forceinline__ f32x2 gemv32(const unsigned (&xp)[4], const v4u (&e)[4], const v4u (&o)[4]) {
  float v[8];
#pragma unroll
  for (int c = 0; c < 8; ++c) v[c] = 0.f;
#pragma unroll
  for (int i = 0; i < 4; ++i) {
    const bf16x2 xb = __builtin_bit_cast(bf16x2, xp[i]);
#pragma unroll
    for (int d = 0; d < 4; ++d) {
      const unsigned lo = __builtin_amdgcn_perm(o[i][d], e[i][d], 0x05040100u), hi = __builtin_amdgcn_perm(o[i][d], e[i][d], 0x07060302u);
      v[2 * d] = __builtin_amdgcn_fdot2_f32_bf16(__builtin_bit_cast(bf16x2, lo), xb, v[2 * d], false);
      v[2 * d + 1] = __builtin_amdgcn_fdot2_f32_bf16(__builtin_bit_cast(bf16x2, hi), xb, v[2 * d + 1], false);
    }
  }
  float u[4];
#pragma unroll
  for (int k = 0; k < 4; ++k) { auto s = __builtin_amdgcn_permlane32_swap(asu(v[k]), asu(v[k + 4]), false, false); u[k] = asf(s[0]) + asf(s[1]); }
  f32x2 t;
#pragma unroll
  for (int k = 0; k < 2; ++k) {
    auto s = __builtin_amdgcn_permlane16_swap(asu(u[k]), asu(u[k + 2]), false, false);
    float y = asf(s[0]) + asf(s[1]);
    y += dpp<ROR8>(y); y += dpp<ROR4>(y);
    t[k] = y;
  }
  return t;
}

struct Tile { v4u k[8], v[8]; };

template <bool REFILL>
__device__ __forceinline__ void tile_step(Tile &t, v4u q, rsrc_t rk, rsrc_t rv, int koff, int voff, int snext, unsigned mk0, unsigned mk1,
                                          float &m2, float &lsum, f32x4 &acc0, f32x4 &acc1) {
  float s[8];
  const f32x4 z = {0.f, 0.f, 0.f, 0.f};
  f32x4 d[8];
#pragma unroll
  for (int g = 0; g < 8; ++g) {
    d[g] = mfma32(t.k[g], q, z);
    if (REFILL) t.k[g] = ld16<KAUX>(rk, koff, snext + g * 1024);
    SB();
  }
#pragma unroll
  for (int g = 0; g < 8; ++g) {
    s[g] = ((dpp<0x00>(d[g][0]) + dpp<0x55>(d[g][1])) + dpp<0xAA>(d[g][2])) + dpp<0xFF>(d[g][3]);
    asm("" : "+v"(s[g]));
  }
#pragma unroll
  for (int g = 0; g < 8; g += 2) {
    const unsigned a = cvtpk(s[g], s[g + 1]);
    const unsigned b = cvtpk(lo16(a) * SCALE, hi16(a) * SCALE);
    s[g] = lo16(b); s[g + 1] = hi16(b);
  }
  float mx = fmaxf(fmaxf(fmaxf(s[0], s[1]), fmaxf(s[2], s[3])), fmaxf(fmaxf(s[4], s[5]), fmaxf(s[6], s[7])));
  mx = xr16_max(mx);
  const float mn = fmaxf(m2, mx * LOG2E);
  const float alpha = __builtin_amdgcn_exp2f(m2 - mn);
  m2 = mn;
  lsum *= alpha;
  int ai = __builtin_bit_cast(int, alpha), ar;
  ar = __builtin_amdgcn_update_dpp(ai, ai, 0x150, 0x1, 0xf, false);
  ar = __builtin_amdgcn_update_dpp(ar, ai, 0x154, 0x2, 0xf, false);
  ar = __builtin_amdgcn_update_dpp(ar, ai, 0x158, 0x4, 0xf, false);
  ar = __builtin_amdgcn_update_dpp(ar, ai, 0x15c, 0x8, 0xf, false);
  const float al = __builtin_bit_cast(float, ar);
  acc0 *= al; acc1 *= al;
#pragma unroll
  for (int g = 0; g < 8; ++g) {
    const float p = __builtin_amdgcn_exp2f(fmaf(s[g], LOG2E, -mn));
    lsum += p;
    const unsigned pp = cvtpk(p, p);
    const v2u a = {pp & mk0, pp & mk1};
    acc0 = mfma16(a, t.v[g][0], t.v[g][1], acc0);
    acc1 = mfma16(a, t.v[g][2], t.v[g][3], acc1);
    if (REFILL) t.v[g] = ld16<VAUX>(rv, voff, snext + g * 1024);
    SB();
  }
}

template <int N>
__device__ __forceinline__ void gather(rsrc_t rs, const int (&off)[N], unsigned tag, v4u (&x)[N]) {
  for (unsigned spins = 0;; ++spins) {
    bool ok = true;
    asm volatile("" ::: "memory");
#pragma unroll
    for (int i = 0; i < N; ++i) x[i] = ld16<AUX_SC1>(rs, off[i], 0);
#pragma unroll
    for (int i = 0; i < N; ++i) ok = ok & (x[i][1] == tag) & (x[i][3] == tag);
    if (__all(ok) || spins > SPIN_CAP) break;
    __builtin_amdgcn_s_sleep(1);
  }
}
template <int S> __device__ __forceinline__ float strided_sum(float x) {
  if (S <= 1) x += dpp<XOR1>(x);
  if (S <= 2) x += dpp<XOR2>(x);
  if (S <= 8) x += dpp<ROR8>(x);
  if (S <= 4) x += dpp<ROR4>(x);
  return xr16_sum(x);
}
template <int C>
__device__ __forceinline__ void publish512(rsrc_t rs, int base, int nprod, int p, f32x4 a0, f32x4 a1, LAS float *tr, int w, int lane, unsigned tag) {
  if (lane < 16) {
    *(LAS f32x4 *)(tr + w * 128 + 8 * lane) = a0;
    *(LAS f32x4 *)(tr + w * 128 + 8 * lane + 4) = a1;
  }
  LDS_WAIT();
  const f32x2 y = *(LAS f32x2 *)(tr + w * 128 + 2 * lane);
  const int col = 128 * w + 2 * lane, j = col / C, within = col % C;
  const v4u g = {asu(y[0]), tag, asu(y[1]), tag};
  st16(rs, base + ((j * nprod + p) * C + within) * 8, g);
}
template <int C, int NP>
__device__ __forceinline__ f32x2 reduce_cols(rsrc_t rs, int base, int j, unsigned tag, LAS float *red, int tid, int w, int lane) {
  constexpr int S = C / 2, NPIECE = NP * S, N = (NPIECE + NTHR - 1) / NTHR;
  int off[N]; v4u x[N];
#pragma unroll
  for (int i = 0; i < N; ++i) { const int pc = i * NTHR + tid; off[i] = base + j * (NP * C * 8) + (pc < NPIECE ? pc : tid) * 16; }
  gather<N>(rs, off, tag, x);
  float s0 = 0.f, s1 = 0.f;
#pragma unroll
  for (int i = 0; i < N; ++i) { const bool live = i * NTHR + tid < NPIECE; s0 += live ? asf(x[i][0]) : 0.f; s1 += live ? asf(x[i][2]) : 0.f; }
  s0 = strided_sum<S>(s0); s1 = strided_sum<S>(s1);
  if (lane < S) { f32x2 y = {s0, s1}; *(LAS f32x2 *)(red + w * 64 + 2 * lane) = y; }
  WG_BARRIER();
  f32x2 r = {0.f, 0.f};
#pragma unroll
  for (int ww = 0; ww < 4; ++ww) r += *(LAS f32x2 *)(red + ww * 64 + 2 * (lane % S));
  return r;
}

#define STAMP_AT(i) do { } while (0)
#define STAMP_FLUSH() do { } while (0)

constexpr int L_MRG = 0;
constexpr int L_ML = L_MRG + 2048;
constexpr int L_RED = L_ML + 32;
constexpr int L_RED2 = L_RED + 256;
constexpr int L_TR = L_RED2 + 256;
constexpr int L_Q = L_TR + 512;
constexpr int L_XN = L_Q + 256;
constexpr int L_END = L_XN + 1024;
constexpr int LDS_BYTES = 84 * 1024;

__global__ void __launch_bounds__(NTHR, 2) fused_decode_kernel(Params p) {
  extern __shared__ __attribute__((aligned(16))) float smem[];
  LAS float *L = (LAS float *)smem;
  const int tid = threadIdx.x, lane = tid & 63, w = __builtin_amdgcn_readfirstlane(tid >> 6), b = blockIdx.x;
  asm volatile("" :: "s"(p.x), "s"(p.k), "s"(p.v), "s"(p.wq), "s"(p.wo), "s"(p.wg), "s"(p.wu), "s"(p.wd), "s"(p.out), "s"(p.ws));
  STAMP_AT(0);
  const unsigned tstart = (unsigned)__builtin_amdgcn_s_memrealtime();
  const int kh = HM ? b >> 5 : b & 7, sp = HM ? b & 31 : b >> 3;
  const rsrc_t rws = __builtin_amdgcn_make_buffer_rsrc(p.ws, 0, WS_USED, RS3);
  const rsrc_t rk = __builtin_amdgcn_make_buffer_rsrc((char *)p.k + (size_t)kh * SK * 256, 0, SK * 256, RS3);
  const rsrc_t rv = __builtin_amdgcn_make_buffer_rsrc((char *)p.v + (size_t)kh * SK * 256, 0, SK * 256, RS3);
  const int tbase = ORD == 1 ? sp * 524288 + w * 8192 : ORD == 2 ? (sp * 4 + w) * 8192 : (w * 32 + sp) * 8192;
  constexpr int TSTR = ORD == 1 ? 4 * 8192 : 128 * 8192;
  const rsrc_t rx = __builtin_amdgcn_make_buffer_rsrc((void *)p.x, 0, DM * 2, RS3);
  const rsrc_t rwq = __builtin_amdgcn_make_buffer_rsrc((void *)p.wq, 0, DM * 4096 * 2, RS3);
  const unsigned epoch = *(const __attribute__((address_space(4))) unsigned *)((const char *)p.ws + WS_EPOCH);
  const bool hasq = (b & 7) < 4;
  const int pq = (b >> 3) * 4 + (b & 3), hq = pq & 31, rb = pq >> 5;
  const int g4 = lane >> 4, a4 = (lane >> 2) & 3, u4 = lane & 3;
  const int koff = a4 * 256 + (4 * g4 + u4) * 16, voff = lane * 16;
  const unsigned mk0 = u4 == 0 ? 0xFFFFu : u4 == 1 ? 0xFFFF0000u : 0u;
  const unsigned mk1 = u4 == 2 ? 0xFFFFu : u4 == 3 ? 0xFFFF0000u : 0u;
  v4u wqf[8];
  unsigned xq[8];
  Tile ta, tb;
  if (hasq) {
    const v4u xr = ld16<0>(rx, lane * 16, 0);
    const int wqoff = (128 * rb + 32 * w + g4) * 8192 + hq * 256 + (lane & 15) * 16;
#pragma unroll
    for (int i = 0; i < 8; ++i) wqf[i] = ld16<WAUX>(rwq, wqoff, i * 4 * 8192);
    SB();
    const LnStat st = ln_stats(xr);
    *(LAS v4u *)(L + L_XN + w * 256 + 4 * lane) = v4u{ln_pair(xr[0], st), ln_pair(xr[1], st), ln_pair(xr[2], st), ln_pair(xr[3], st)};
    LDS_WAIT();
#pragma unroll
    for (int i = 0; i < 8; ++i) xq[i] = ((LAS unsigned short *)(L + L_XN + w * 256))[128 * rb + 32 * w + 4 * i + g4];
    STAMP_AT(12);
  }
  if (hasq) {
    f32x4 c0 = {0.f, 0.f, 0.f, 0.f}, c1 = c0;
#pragma unroll
    for (int i = 0; i < 8; ++i) {
      const unsigned pp = xq[i] | (xq[i] << 16);
      const v2u a = {pp & mk0, pp & mk1};
      c0 = mfma16(a, wqf[i][0], wqf[i][1], c0);
      c1 = mfma16(a, wqf[i][2], wqf[i][3], c1);
    }
    if (lane < 16) {
      *(LAS f32x4 *)(L + L_TR + w * 128 + 8 * lane) = c0;
      *(LAS f32x4 *)(L + L_TR + w * 128 + 8 * lane + 4) = c1;
    }
    STAMP_AT(13);
    WG_BARRIER();
    if (w < QREP) {
      f32x2 y = {0.f, 0.f};
#pragma unroll
      for (int ww = 0; ww < 4; ++ww) y += *(LAS f32x2 *)(L + L_TR + ww * 128 + 2 * lane);
      st16(rws, WS_QG + w * 131072 + ((hq * 4 + rb) * 128 + 2 * lane) * 8, v4u{asu(y[0]), epoch + 1u, asu(y[1]), epoch + 1u});
    }
  }
  STAMP_AT(1);
#pragma unroll
  for (int g = 0; g < 8; ++g) ta.k[g] = ld16<KAUX>(rk, koff, tbase + g * 1024);
#pragma unroll
  for (int g = 0; g < 8; ++g) ta.v[g] = ld16<VAUX>(rv, voff, tbase + g * 1024);
  SB();
  const unsigned tag = epoch + 1u;
  while ((unsigned)__builtin_amdgcn_s_memrealtime() - tstart < (unsigned)TPOLL) __builtin_amdgcn_s_sleep(1);
  v4u q;
  {
    const int qoff = WS_QG + (sp & (QREP - 1)) * 131072 + (4 * kh + w) * 4096 + lane * 16;
    v4u x[4];
#pragma unroll
    for (int i = 0; i < 4; ++i) x[i] = ld16<AUX_SC1>(rws, qoff, i * 1024);
    SB();
    asm volatile("s_barrier" ::: "memory");
    SB();
#pragma unroll
    for (int g = 0; g < 8; ++g) tb.k[g] = ld16<KAUX>(rk, koff, tbase + TSTR + g * 1024);
#pragma unroll
    for (int g = 0; g < 8; ++g) tb.v[g] = ld16<VAUX>(rv, voff, tbase + TSTR + g * 1024);
    SB();
    bool ok = true;
#pragma unroll
    for (int i = 0; i < 4; ++i) ok = ok & (x[i][1] == tag) & (x[i][3] == tag);
    if (__builtin_expect(!__all(ok), 0)) {
      for (unsigned spins = 0; spins < SPIN_CAP; ++spins) {
        bool ok2 = true;
        asm volatile("" ::: "memory");
#pragma unroll
        for (int i = 0; i < 4; ++i) x[i] = ld16<AUX_SC1>(rws, qoff, i * 1024);
#pragma unroll
        for (int i = 0; i < 4; ++i) ok2 = ok2 & (x[i][1] == tag) & (x[i][3] == tag);
        if (__all(ok2)) break;
      }
      __builtin_amdgcn_s_waitcnt(0x0F70);
    }
    ((LAS unsigned *)L)[L_Q + w * 64 + lane] = cvtpk((asf(x[0][0]) + asf(x[1][0])) + (asf(x[2][0]) + asf(x[3][0])),
                                                     (asf(x[0][2]) + asf(x[1][2])) + (asf(x[2][2]) + asf(x[3][2])));
    WG_BARRIER();
    q = *(LAS v4u *)(L + L_Q + a4 * 64 + (4 * g4 + u4) * 4);
  }
  STAMP_AT(2);
  const int pf = (b & 7) * 28 + (b >> 3);
  const bool hasf = (b >> 3) < 28;
  const rsrc_t rwg = __builtin_amdgcn_make_buffer_rsrc((void *)p.wg, 0, hasf ? DM * FF * 2 : 0, RS3);
  const rsrc_t rwu = __builtin_amdgcn_make_buffer_rsrc((void *)p.wu, 0, hasf ? DM * FF * 2 : 0, RS3);
  const int go = (128 * w + 2 * lane) * (FF * 2) + pf * 16;
  v4u wg0 = {0u, 0u, 0u, 0u}, wg1 = wg0, wu0 = wg0, wu1 = wg0;
  const v4u dwg = make_desc(p.wg, hasf ? DM * FF * 2 : 0), dwu = make_desc(p.wu, hasf ? DM * FF * 2 : 0);
  if (!GUSPREAD) {
    wg0 = ld16<WAUX>(rwg, go, 0); wg1 = ld16<WAUX>(rwg, go, FF * 2);
    wu0 = ld16<WAUX>(rwu, go, 0); wu1 = ld16<WAUX>(rwu, go, FF * 2);
  }
  SB();
  float m2 = -3.0e38f, lsum = 0.f;
  f32x4 acc0 = {0.f, 0.f, 0.f, 0.f}, acc1 = acc0;
  for (int it = 0; it < 7; ++it) {
    const int s0 = tbase + (2 * it + 2) * TSTR;
    if (TAU) { while ((unsigned)__builtin_amdgcn_s_memrealtime() - tstart < (unsigned)(TOFF + 2 * it * TAU)) __builtin_amdgcn_s_sleep(1); }
    if (GUSPREAD) {
      ld16_async_at<1>(wg0, dwg, go, 0, it);
      ld16_async_at<2>(wg1, dwg, go, FF * 2, it);
      ld16_async_at<3>(wu0, dwu, go, 0, it);
      ld16_async_at<4>(wu1, dwu, go, FF * 2, it);
      SB();
    }
    tile_step<true>(ta, q, rk, rv, koff, voff, s0, mk0, mk1, m2, lsum, acc0, acc1);
    if (TAU) { while ((unsigned)__builtin_amdgcn_s_memrealtime() - tstart < (unsigned)(TOFF + (2 * it + 1) * TAU)) __builtin_amdgcn_s_sleep(1); }
    tile_step<true>(tb, q, rk, rv, koff, voff, s0 + TSTR, mk0, mk1, m2, lsum, acc0, acc1);
  }
  const rsrc_t rwo = __builtin_amdgcn_make_buffer_rsrc((char *)p.wo + (size_t)b * 16384, 0, 16384, RS3);
  const rsrc_t rwd = __builtin_amdgcn_make_buffer_rsrc((char *)p.wd + (size_t)(hasf ? pf : 0) * 8192, 0, hasf ? 8192 : 0, RS3);
  v4u wo[4], wd[2];
  {
    const int o = g4 * 1024 + w * 256 + (lane & 15) * 16;
#pragma unroll
    for (int i = 0; i < 4; ++i) wo[i] = ld16<WAUX>(rwo, o, i * 4096);
#pragma unroll
    for (int i = 0; i < 2; ++i) wd[i] = ld16<WAUX>(rwd, o, i * 4096);
    SB();
  }
  tile_step<false>(ta, q, rk, rv, koff, voff, 0, mk0, mk1, m2, lsum, acc0, acc1);
  tile_step<false>(tb, q, rk, rv, koff, voff, 0, mk0, mk1, m2, lsum, acc0, acc1);
  STAMP_AT(3);
  {
    const float lw = xr16_sum(lsum);
    *(LAS f32x4 *)(L + L_MRG + w * 512 + g4 * 128 + 8 * (lane & 15)) = acc0;
    *(LAS f32x4 *)(L + L_MRG + w * 512 + g4 * 128 + 8 * (lane & 15) + 4) = acc1;
    if (lane < 16 && u4 == 0) { f32x2 y = {m2, lw}; *(LAS f32x2 *)(L + L_ML + (w * 4 + a4) * 2) = y; }
    WG_BARRIER();
    f32x2 ml[4];
#pragma unroll
    for (int ww = 0; ww < 4; ++ww) ml[ww] = *(LAS f32x2 *)(L + L_ML + (ww * 4 + w) * 2);
    const float M = fmaxf(fmaxf(ml[0][0], ml[1][0]), fmaxf(ml[2][0], ml[3][0]));
    float Ls = 0.f; f32x2 o = {0.f, 0.f};
#pragma unroll
    for (int ww = 0; ww < 4; ++ww) {
      const float e = __builtin_amdgcn_exp2f(ml[ww][0] - M);
      Ls = fmaf(e, ml[ww][1], Ls);
      o += e * *(LAS f32x2 *)(L + L_MRG + ww * 512 + w * 128 + 2 * lane);
    }
    const int head = 4 * kh + w;
    st16(rws, WS_OP + (((head * 8 + (lane >> 3)) * 32 + sp) * 16 + 2 * (lane & 7)) * 8, v4u{asu(o[0]), tag, asu(o[1]), tag});
    if (lane < 8) st16(rws, WS_ML + ((head * 8 + lane) * 32 + sp) * 16, v4u{asu(M), tag, asu(Ls), tag});
  }
  STAMP_AT(4);
  {
    const int off[2] = {WS_OP + b * 4096 + tid * 16, WS_ML + (b * 32 + 8 * w + (lane >> 3)) * 16};
    v4u x[2];
    gather<2>(rws, off, tag, x);
    STAMP_AT(5);
    const float ms = asf(x[1][0]), ls = asf(x[1][2]);
    const float Mw = xr16_max(fmaxf(ms, dpp<ROR8>(ms)));
    const float e = __builtin_amdgcn_exp2f(ms - Mw);
    const float v0 = strided_sum<8>(e * asf(x[0][0])), v1 = strided_sum<8>(e * asf(x[0][2])), lw = strided_sum<8>(e * ls);
    if (lane < 8) {
      const int d = 2 * lane;
      L[L_RED2 + w * 64 + (d & 3) * 4 + (d >> 2)] = v0;
      L[L_RED2 + w * 64 + ((d + 1) & 3) * 4 + (d >> 2)] = v1;
      if (lane == 0) *(LAS f32x2 *)(L + L_RED2 + w * 64 + 32) = f32x2{Mw, lw};
    }
    WG_BARRIER();
    f32x2 ml[4];
#pragma unroll
    for (int ww = 0; ww < 4; ++ww) ml[ww] = *(LAS f32x2 *)(L + L_RED2 + ww * 64 + 32);
    const float M = fmaxf(fmaxf(ml[0][0], ml[1][0]), fmaxf(ml[2][0], ml[3][0]));
    f32x4 o = {0.f, 0.f, 0.f, 0.f};
    float Lt = 0.f;
#pragma unroll
    for (int ww = 0; ww < 4; ++ww) {
      const float ew = __builtin_amdgcn_exp2f(ml[ww][0] - M);
      Lt = fmaf(ew, ml[ww][1], Lt);
      o += ew * *(LAS f32x4 *)(L + L_RED2 + ww * 64 + g4 * 4);
    }
    o *= __builtin_amdgcn_rcpf(Lt);
    f32x4 c0 = {0.f, 0.f, 0.f, 0.f}, c1 = c0;
#pragma unroll
    for (int i = 0; i < 4; ++i) {
      const unsigned pp = cvtpk(o[i], o[i]);
      const v2u a = {pp & mk0, pp & mk1};
      c0 = mfma16(a, wo[i][0], wo[i][1], c0);
      c1 = mfma16(a, wo[i][2], wo[i][3], c1);
    }
    publish512<512 / NB1>(rws, WS_PB, 256, b, c0, c1, L + L_TR, w, lane, tag);
  }
  STAMP_AT(6);
  if (b < NB1) {
    constexpr int C = 512 / NB1, S = C / 2;
    const unsigned xp = __builtin_amdgcn_raw_buffer_load_b32(rx, (b * C + 2 * (lane % S)) * 2, 0, 0);
    const f32x2 r = reduce_cols<C, 256>(rws, WS_PB, b, tag, L + L_RED, tid, w, lane);
    const unsigned hp = cvtpk(lo16(xp) + rbf(r[0]), hi16(xp) + rbf(r[1]));
    if (tid < S * REP) st8(rws, WS_HG + (tid / S) * 2048 + (b * S + tid % S) * 8, v2u{hp, tag});
  }
  STAMP_AT(8);
  if (w == 0) {
    const int base = WS_HG + (b % REP) * 2048 + lane * 32;
    const int off[2] = {base, base + 16};
    v4u x[2];
    gather<2>(rws, off, tag, x);
    const v4u hb = v4u{x[0][0], x[0][2], x[1][0], x[1][2]};
    *(LAS v4u *)(L + L_Q + 4 * lane) = hb;
    const LnStat st = ln_stats(hb);
    *(LAS v4u *)(L + L_XN + 4 * lane) = v4u{ln_pair(hb[0], st), ln_pair(hb[1], st), ln_pair(hb[2], st), ln_pair(hb[3], st)};
  }
  WG_BARRIER();
  STAMP_AT(9);
  if (hasf) {
    const unsigned xsel = ((LAS unsigned *)L)[L_XN + 64 * w + lane];
    const float r = gemv16(xsel, wg0, wg1, wu0, wu1, lane);
    if ((lane & 3) == 0) { const int vi = lane >> 2, col = vi & 7; L[L_RED2 + w * 64 + 16 + (col & 3) * 4 + (vi >> 3) * 2 + (col >> 2)] = r; }
    WG_BARRIER();
    f32x4 gu = {0.f, 0.f, 0.f, 0.f};
#pragma unroll
    for (int ww = 0; ww < 4; ++ww) gu += *(LAS f32x4 *)(L + L_RED2 + ww * 64 + 16 + g4 * 4);
    f32x4 c0 = {0.f, 0.f, 0.f, 0.f}, c1 = c0;
#pragma unroll
    for (int i = 0; i < 2; ++i) {
      const float hv = rbf(gu[i]) * rbf(gu[2 + i]);
      const unsigned pp = cvtpk(hv, hv);
      const v2u a = {pp & mk0, pp & mk1};
      c0 = mfma16(a, wd[i][0], wd[i][1], c0);
      c1 = mfma16(a, wd[i][2], wd[i][3], c1);
    }
    publish512<512 / ND>(rws, WS_PD, NPF, pf, c0, c1, L + L_TR, w, lane, tag);
  }
  STAMP_AT(10);
  if (b >= NWG - ND) {
    constexpr int C = 512 / ND, S = C / 2;
    const int j = b - (NWG - ND);
    const unsigned hp = ((LAS unsigned *)L)[L_Q + j * S + lane % S];
    WG_BARRIER();
    const f32x2 r = reduce_cols<C, NPF>(rws, WS_PD, j, tag, L + L_RED, tid, w, lane);
    const unsigned op = cvtpk(lo16(hp) + rbf(r[0]), hi16(hp) + rbf(r[1]));
    if (w == 0 && lane < S) ((unsigned *)p.out)[j * S + lane] = op;
    if (j == 0 && tid == 0) __builtin_amdgcn_raw_buffer_store_b32(tag, rws, WS_EPOCH, 0, AUX_SC1);
  }
  STAMP_AT(11);
  STAMP_FLUSH();
}
}

extern "C" void kernel_launch(void *const *d_in, const int *in_sizes, int n_in, void *d_out, int out_size, void *d_ws, size_t ws_size,
                              hipStream_t stream) {
  Params p;
  p.x = d_in[0]; p.k = d_in[1]; p.v = d_in[2]; p.wq = d_in[3]; p.wo = d_in[4];
  p.wg = d_in[6]; p.wu = d_in[7]; p.wd = d_in[8];
  p.out = d_out; p.ws = d_ws;
  (void)hipFuncSetAttribute((const void *)fused_decode_kernel, hipFuncAttributeMaxDynamicSharedMemorySize, LDS_BYTES);
  fused_decode_kernel<<<NWG, NTHR, LDS_BYTES, stream>>>(p);
}
```

```cpp
#include <hip/hip_runtime.h>
#include <stdint.h>

#define STAMP 0
#define REP 16
#define KAUX 19
#define VAUX 19
#define WAUX 19
#define PINARGS 1
#define ORD 1
#define HM 1
#define TAU 0
#define TOFF 500
#define TK 60
#define TPOLL 280
#define DREP 8
#define DEFER 0
#define PIPEPOLL 0
#define QREP 4

namespace {
typedef unsigned v4u __attribute__((ext_vector_type(4)));
typedef unsigned v2u __attribute__((ext_vector_type(2)));
typedef float f32x4 __attribute__((ext_vector_type(4)));
typedef float f32x2 __attribute__((ext_vector_type(2)));
typedef short s16x4 __attribute__((ext_vector_type(4)));
typedef __bf16 bf16x8 __attribute__((ext_vector_type(8)));
typedef __bf16 bf16x2 __attribute__((ext_vector_type(2)));
typedef __amdgpu_buffer_rsrc_t rsrc_t;
#define LAS __attribute__((address_space(3)))

constexpr int DM = 512, SK = 65536, DH = 128, FF = 1792;
constexpr int NWG = 256, NTHR = 256;
constexpr int NPW = 128, NPF = 56;
constexpr int AUX_NT = 2, AUX_SC1 = 16;
constexpr int RS3 = 0x00020000;
constexpr int WS_EPOCH = 0;
constexpr int WS_ML = 4096;
constexpr int WS_HG = WS_ML + 65536;
constexpr int WS_DN = WS_HG + 65536;
constexpr int WS_OP = 1 << 20;
constexpr int WS_PB = 2 << 20;
constexpr int WS_PD = 3 << 20;
constexpr int WS_QG = 4 << 20;
constexpr int WS_USED = 5 << 20;
constexpr int WS_STAMP = 8 << 20;
constexpr unsigned SPIN_CAP = 1u << 14;
constexpr float LOG2E = 1.4426950408889634f;
constexpr float SCALE = 0.08838834764831845f;

struct Params {
  const void *x, *k, *v, *wq, *wo, *wg, *wu, *wd;
  void *out, *ws;
};

__device__ __forceinline__ float asf(unsigned u) { return __builtin_bit_cast(float, u); }
__device__ __forceinline__ unsigned asu(float f) { return __builtin_bit_cast(unsigned, f); }
template <int C> __device__ __forceinline__ float dpp(float x) {
  return __builtin_bit_cast(float, __builtin_amdgcn_mov_dpp(__builtin_bit_cast(int, x), C, 0xf, 0xf, true));
}
constexpr int XOR1 = 0xB1, XOR2 = 0x4E, HMIR = 0x141, MIR = 0x140, ROR4 = 0x124, ROR8 = 0x128;
__device__ __forceinline__ unsigned cvtpk(float lo, float hi) {
  f32x2 v = {lo, hi};
  bf16x2 r = __builtin_convertvector(v, bf16x2);
  return __builtin_bit_cast(unsigned, r);
}
__device__ __forceinline__ float lo16(unsigned u) { return asf(u << 16); }
__device__ __forceinline__ float hi16(unsigned u) { return asf(u & 0xffff0000u); }
__device__ __forceinline__ float rbf(float x) { return lo16(cvtpk(x, x)); }
__device__ __forceinline__ float xr16_max(float x) {
  auto s = __builtin_amdgcn_permlane16_swap(asu(x), asu(x), false, false);
  x = fmaxf(asf(s[0]), asf(s[1]));
  auto t = __builtin_amdgcn_permlane32_swap(asu(x), asu(x), false, false);
  return fmaxf(asf(t[0]), asf(t[1]));
}
__device__ __forceinline__ float xr16_sum(float x) {
  auto s = __builtin_amdgcn_permlane16_swap(asu(x), asu(x), false, false);
  x = asf(s[0]) + asf(s[1]);
  auto t = __builtin_amdgcn_permlane32_swap(asu(x), asu(x), false, false);
  return asf(t[0]) + asf(t[1]);
}
__device__ __forceinline__ float row_sum(float x) {
  x += dpp<XOR1>(x); x += dpp<XOR2>(x); x += dpp<HMIR>(x); x += dpp<MIR>(x); return x;
}
__device__ __forceinline__ float row_max(float x) {
  x = fmaxf(x, dpp<XOR1>(x)); x = fmaxf(x, dpp<XOR2>(x)); x = fmaxf(x, dpp<HMIR>(x)); x = fmaxf(x, dpp<MIR>(x)); return x;
}
__device__ __forceinline__ float wave_sum(float x) { return xr16_sum(row_sum(x)); }
__device__ __forceinline__ f32x4 mfma32(v4u a, v4u b, f32x4 c) {
  return __builtin_amdgcn_mfma_f32_16x16x32_bf16(__builtin_bit_cast(bf16x8, a), __builtin_bit_cast(bf16x8, b), c, 0, 0, 0);
}
__device__ __forceinline__ f32x4 mfma16(v2u a, unsigned b0, unsigned b1, f32x4 c) {
  v2u b = {b0, b1};
  return __builtin_amdgcn_mfma_f32_16x16x16bf16_1k(__builtin_bit_cast(s16x4, a), __builtin_bit_cast(s16x4, b), c, 0, 0, 0);
}
template <int AUX> __device__ __forceinline__ v4u ld16(rsrc_t r, int voff, int soff) {
  return __builtin_amdgcn_raw_buffer_load_b128(r, voff, soff, AUX);
}
__device__ __forceinline__ void st16(rsrc_t r, int voff, v4u v) { __builtin_amdgcn_raw_buffer_store_b128(v, r, voff, 0, AUX_SC1); }
__device__ __forceinline__ void st8(rsrc_t r, int voff, v2u v) { __builtin_amdgcn_raw_buffer_store_b64(v, r, voff, 0, AUX_SC1); }
#define WG_BARRIER() asm volatile("s_waitcnt lgkmcnt(0)\n\ts_barrier" ::: "memory")
#define LDS_WAIT() asm volatile("s_waitcnt lgkmcnt(0)" ::: "memory")
#define SB() __builtin_amdgcn_sched_barrier(0)

struct LnStat { float mb, rden; float den; };
__device__ __forceinline__ LnStat ln_stats(v4u p) {
  float s = 0.f, q = 0.f;
#pragma unroll
  for (int i = 0; i < 4; ++i) {
    const float a = lo16(p[i]), b = hi16(p[i]);
    s += a + b;
    q = fmaf(a, a, fmaf(b, b, q));
  }
  s += dpp<XOR1>(s); q += dpp<XOR1>(q); s += dpp<XOR2>(s); q += dpp<XOR2>(q);
  s += dpp<HMIR>(s); q += dpp<HMIR>(q); s += dpp<MIR>(s); q += dpp<MIR>(q);
  auto t = __builtin_amdgcn_permlane16_swap(asu(s), asu(q), false, false);
  float y = asf(t[0]) + asf(t[1]);
  auto u = __builtin_amdgcn_permlane32_swap(asu(y), asu(y), false, false);
  y = asf(u[0]) + asf(u[1]);
  auto v = __builtin_amdgcn_permlane16_swap(asu(y), asu(y), false, false);
  const float mean = asf(v[0]) * (1.f / DM);
  const float var = fmaf(-mean, mean, asf(v[1]) * (1.f / DM));
  LnStat r; r.mb = rbf(mean); r.den = rbf(__builtin_amdgcn_sqrtf(rbf(rbf(var) + 1e-5f))); r.rden = __builtin_amdgcn_rcpf(r.den);
  return r;
}
__device__ __forceinline__ unsigned ln_pair(unsigned p, LnStat st) { return cvtpk(rbf(lo16(p) - st.mb) * st.rden, rbf(hi16(p) - st.mb) * st.rden); }
struct Tile { v4u k[8], v[8]; };

template <bool REFILL>
__device__ __forceinline__ void tile_step(Tile &t, v4u q, rsrc_t rk, rsrc_t rv, int koff, int voff, int snext, unsigned mk0, unsigned mk1,
                                          float &m2, float &lsum, f32x4 &acc0, f32x4 &acc1) {
  float s[8];
  const f32x4 z = {0.f, 0.f, 0.f, 0.f};
  f32x4 d[8];
#pragma unroll
  for (int g = 0; g < 8; ++g) {
    d[g] = mfma32(t.k[g], q, z);
    if (REFILL) t.k[g] = ld16<KAUX>(rk, koff, snext + g * 1024);
    SB();
  }
#pragma unroll
  for (int g = 0; g < 8; ++g) {
    s[g] = ((dpp<0x00>(d[g][0]) + dpp<0x55>(d[g][1])) + dpp<0xAA>(d[g][2])) + dpp<0xFF>(d[g][3]);
    asm("" : "+v"(s[g]));
  }
#pragma unroll
  for (int g = 0; g < 8; g += 2) {
    const unsigned a = cvtpk(s[g], s[g + 1]);
    const unsigned b = cvtpk(lo16(a) * SCALE, hi16(a) * SCALE);
    s[g] = lo16(b); s[g + 1] = hi16(b);
  }
  float mx = fmaxf(fmaxf(fmaxf(s[0], s[1]), fmaxf(s[2], s[3])), fmaxf(fmaxf(s[4], s[5]), fmaxf(s[6], s[7])));
  mx = xr16_max(mx);
  const float mn = fmaxf(m2, mx * LOG2E);
  const float alpha = __builtin_amdgcn_exp2f(m2 - mn);
  m2 = mn;
  lsum *= alpha;
  int ai = __builtin_bit_cast(int, alpha), ar;
  ar = __builtin_amdgcn_update_dpp(ai, ai, 0x150, 0x1, 0xf, false);
  ar = __builtin_amdgcn_update_dpp(ar, ai, 0x154, 0x2, 0xf, false);
  ar = __builtin_amdgcn_update_dpp(ar, ai, 0x158, 0x4, 0xf, false);
  ar = __builtin_amdgcn_update_dpp(ar, ai, 0x15c, 0x8, 0xf, false);
  const float al = __builtin_bit_cast(float, ar);
  acc0 *= al; acc1 *= al;
#pragma unroll
  for (int g = 0; g < 8; ++g) {
    const float p = __builtin_amdgcn_exp2f(fmaf(s[g], LOG2E, -mn));
    lsum += p;
    const unsigned pp = cvtpk(p, p);
    const v2u a = {pp & mk0, pp & mk1};
    acc0 = mfma16(a, t.v[g][0], t.v[g][1], acc0);
    acc1 = mfma16(a, t.v[g][2], t.v[g][3], acc1);
    if (REFILL) t.v[g] = ld16<VAUX>(rv, voff, snext + g * 1024);
    SB();
  }
}

template <int N>
__device__ __forceinline__ bool tags_ok(const v4u (&x)[N], unsigned tag) {
  bool ok = true;
#pragma unroll
  for (int i = 0; i < N; ++i) ok = ok & (x[i][1] == tag) & (x[i][3] == tag);
  return __all(ok);
}
template <int N>
__device__ __forceinline__ void sweep(rsrc_t rs, const int (&off)[N], v4u (&x)[N]) {
  asm volatile("" ::: "memory");
#pragma unroll
  for (int i = 0; i < N; ++i) x[i] = ld16<AUX_SC1>(rs, off[i], 0);
}
template <int N>
__device__ __forceinline__ void gather(rsrc_t rs, const int (&off)[N], unsigned tag, v4u (&x)[N]) {
  for (unsigned spins = 0;; ++spins) {
    sweep<N>(rs, off, x);
    if (tags_ok<N>(x, tag) || spins > SPIN_CAP) break;
    __builtin_amdgcn_s_sleep(1);
  }
}
#define GATHER(N, off, x) gather<N>(rws, off, tag, x)
#define KEEP(N, x) do { } while (0)
template <int S> __device__ __forceinline__ float strided_sum(float x) {
  if (S <= 1) x += dpp<XOR1>(x);
  if (S <= 2) x += dpp<XOR2>(x);
  if (S <= 8) x += dpp<ROR8>(x);
  if (S <= 4) x += dpp<ROR4>(x);
  return xr16_sum(x);
}
__device__ __forceinline__ void publish512(rsrc_t rs, int base, int nprod, int p, f32x4 a0, f32x4 a1, LAS float *tr, int w, int lane, unsigned tag) {
  LAS unsigned *t = (LAS unsigned *)tr + w * 64;
  if (lane < 16) *(LAS v4u *)(t + 4 * lane) = v4u{cvtpk(a0[0], a0[1]), cvtpk(a0[2], a0[3]), cvtpk(a1[0], a1[1]), cvtpk(a1[2], a1[3])};
  LDS_WAIT();
  const v2u y = *(LAS v2u *)(t + 2 * (lane & 31));
  if (lane < 32) st16(rs, base + ((8 * w + (lane >> 2)) * nprod + p) * 64 + (lane & 3) * 16, v4u{y[0], tag, y[1], tag});
}
template <int N>
__device__ __forceinline__ f32x4 reduce_cols(const v4u (&x)[N], bool last_live, LAS float *red, int w, int lane) {
  f32x4 s = {0.f, 0.f, 0.f, 0.f};
#pragma unroll
  for (int i = 0; i < N; ++i) {
    const f32x4 v = {lo16(x[i][0]), hi16(x[i][0]), lo16(x[i][2]), hi16(x[i][2])};
    if (i + 1 < N) s += v; else s += last_live ? v : f32x4{0.f, 0.f, 0.f, 0.f};
  }
  auto sa = __builtin_amdgcn_permlane32_swap(asu(s[0]), asu(s[2]), false, false);
  auto sb = __builtin_amdgcn_permlane32_swap(asu(s[1]), asu(s[3]), false, false);
  auto sc = __builtin_amdgcn_permlane16_swap(asu(asf(sa[0]) + asf(sa[1])), asu(asf(sb[0]) + asf(sb[1])), false, false);
  float v = asf(sc[0]) + asf(sc[1]);
  v += dpp<ROR8>(v); v += dpp<ROR4>(v);
  if ((lane & 12) == 0) red[w * 64 + 4 * (lane & 3) + 2 * (lane >> 5) + ((lane >> 4) & 1)] = v;
  WG_BARRIER();
  f32x4 r = {0.f, 0.f, 0.f, 0.f};
#pragma unroll
  for (int ww = 0; ww < 4; ++ww) r += *(LAS f32x4 *)(red + ww * 64 + 4 * (lane & 3));
  return r;
}
__device__ __forceinline__ void gemv_step(unsigned pp, unsigned mk0, unsigned mk1, v4u f, f32x4 &c0, f32x4 &c1) {
  const v2u a = {pp & mk0, pp & mk1};
  c0 = mfma16(a, f[0], f[1], c0);
  c1 = mfma16(a, f[2], f[3], c1);
}

#define STAMP_AT(i) do { } while (0)
#define STAMP_FLUSH() do { } while (0)

constexpr int L_MRG = 0;
constexpr int L_ML = L_MRG + 2048;
constexpr int L_RED = L_ML + 32;
constexpr int L_RED2 = L_RED + 256;
constexpr int L_TR = L_RED2 + 256;
constexpr int L_Q = L_TR + 512;
constexpr int L_XN = L_Q + 256;
constexpr int L_HID = L_XN + 1024;
constexpr int L_END = L_HID + 32;
constexpr int LDS_BYTES = 84 * 1024;

__global__ void __launch_bounds__(NTHR, 2) fused_decode_kernel(Params p) {
  extern __shared__ __attribute__((aligned(16))) float smem[];
  LAS float *L = (LAS float *)smem;
  const int tid = threadIdx.x, lane = tid & 63, w = __builtin_amdgcn_readfirstlane(tid >> 6), b = blockIdx.x;
  asm volatile("" :: "s"(p.x), "s"(p.k), "s"(p.v), "s"(p.wq), "s"(p.wo), "s"(p.wg), "s"(p.wu), "s"(p.wd), "s"(p.out), "s"(p.ws));
  STAMP_AT(0);
  const unsigned tstart = (unsigned)__builtin_amdgcn_s_memrealtime();
  const int kh = HM ? b >> 5 : b & 7, sp = HM ? b & 31 : b >> 3;
  const rsrc_t rws = __builtin_amdgcn_make_buffer_rsrc(p.ws, 0, WS_USED, RS3);
  const rsrc_t rk = __builtin_amdgcn_make_buffer_rsrc((char *)p.k + (size_t)kh * SK * 256, 0, SK * 256, RS3);
  const rsrc_t rv = __builtin_amdgcn_make_buffer_rsrc((char *)p.v + (size_t)kh * SK * 256, 0, SK * 256, RS3);
  const int tbase = ORD == 1 ? sp * 524288 + w * 8192 : ORD == 2 ? (sp * 4 + w) * 8192 : (w * 32 + sp) * 8192;
  constexpr int TSTR = ORD == 1 ? 4 * 8192 : 128 * 8192;
  const rsrc_t rx = __builtin_amdgcn_make_buffer_rsrc((void *)p.x, 0, DM * 2, RS3);
  const rsrc_t rwq = __builtin_amdgcn_make_buffer_rsrc((void *)p.wq, 0, DM * 4096 * 2, RS3);
  const unsigned epoch = *(const __attribute__((address_space(4))) unsigned *)((const char *)p.ws + WS_EPOCH);
  const bool hasq = (b & 7) < 4;
  const int pq = (b >> 3) * 4 + (b & 3), hq = pq & 31, rb = pq >> 5;
  const int g4 = lane >> 4, a4 = (lane >> 2) & 3, u4 = lane & 3;
  const int koff = a4 * 256 + (4 * g4 + u4) * 16, voff = lane * 16;
  const unsigned mk0 = u4 == 0 ? 0xFFFFu : u4 == 1 ? 0xFFFF0000u : 0u;
  const unsigned mk1 = u4 == 2 ? 0xFFFFu : u4 == 3 ? 0xFFFF0000u : 0u;
  v4u wqf[8];
  unsigned xq[8];
  Tile ta, tb;
  if (hasq) {
    const v4u xr = ld16<0>(rx, lane * 16, 0);
    const unsigned xown = __builtin_amdgcn_raw_buffer_load_b32(rx, (64 * rb + 16 * w + (lane & 15)) * 4, 0, 0);
    const int wqoff = (128 * rb + 32 * w + g4) * 8192 + hq * 256 + (lane & 15) * 16;
#pragma unroll
    for (int i = 0; i < 8; ++i) wqf[i] = ld16<WAUX>(rwq, wqoff, i * 4 * 8192);
    SB();
    ((LAS unsigned *)L)[L_XN + w * 64 + lane] = ln_pair(xown, ln_stats(xr));
    LDS_WAIT();
#pragma unroll
    for (int i = 0; i < 8; ++i) xq[i] = ((LAS unsigned short *)(L + L_XN + w * 64))[4 * i + g4];
    STAMP_AT(12);
  }
  if (hasq) {
    f32x4 c0 = {0.f, 0.f, 0.f, 0.f}, c1 = c0;
#pragma unroll
    for (int i = 0; i < 8; ++i) {
      gemv_step(xq[i] | (xq[i] << 16), mk0, mk1, wqf[i], c0, c1);
    }
    if (lane < 16) {
      *(LAS f32x4 *)(L + L_TR + w * 128 + 8 * lane) = c0;
      *(LAS f32x4 *)(L + L_TR + w * 128 + 8 * lane + 4) = c1;
    }
    STAMP_AT(13);
    WG_BARRIER();
    if (w < QREP) {
      f32x2 y = {0.f, 0.f};
#pragma unroll
      for (int ww = 0; ww < 4; ++ww) y += *(LAS f32x2 *)(L + L_TR + ww * 128 + 2 * lane);
      st16(rws, WS_QG + w * 131072 + ((hq * 4 + rb) * 128 + 2 * lane) * 8, v4u{asu(y[0]), epoch + 1u, asu(y[1]), epoch + 1u});
    }
  }
  STAMP_AT(1);
#pragma unroll
  for (int g = 0; g < 8; ++g) ta.k[g] = ld16<KAUX>(rk, koff, tbase + g * 1024);
#pragma unroll
  for (int g = 0; g < 8; ++g) ta.v[g] = ld16<VAUX>(rv, voff, tbase + g * 1024);
  SB();
  const unsigned tag = epoch + 1u;
  while ((unsigned)__builtin_amdgcn_s_memrealtime() - tstart < (unsigned)TPOLL) __builtin_amdgcn_s_sleep(1);
  v4u q;
  {
    const int qoff = WS_QG + (sp & (QREP - 1)) * 131072 + (4 * kh + w) * 4096 + lane * 16;
    v4u x[4];
#pragma unroll
    for (int i = 0; i < 4; ++i) x[i] = ld16<AUX_SC1>(rws, qoff, i * 1024);
    SB();
    asm volatile("s_barrier" ::: "memory");
    SB();
#pragma unroll
    for (int g = 0; g < 8; ++g) tb.k[g] = ld16<KAUX>(rk, koff, tbase + TSTR + g * 1024);
#pragma unroll
    for (int g = 0; g < 8; ++g) tb.v[g] = ld16<VAUX>(rv, voff, tbase + TSTR + g * 1024);
    SB();
    bool ok = true;
#pragma unroll
    for (int i = 0; i < 4; ++i) ok = ok & (x[i][1] == tag) & (x[i][3] == tag);
    if (__builtin_expect(!__all(ok), 0)) {
      for (unsigned spins = 0; spins < SPIN_CAP; ++spins) {
        bool ok2 = true;
        asm volatile("" ::: "memory");
#pragma unroll
        for (int i = 0; i < 4; ++i) x[i] = ld16<AUX_SC1>(rws, qoff, i * 1024);
#pragma unroll
        for (int i = 0; i < 4; ++i) ok2 = ok2 & (x[i][1] == tag) & (x[i][3] == tag);
        if (__all(ok2)) break;
      }
      __builtin_amdgcn_s_waitcnt(0x0F70);
    }
    ((LAS unsigned *)L)[L_Q + w * 64 + lane] = cvtpk((asf(x[0][0]) + asf(x[1][0])) + (asf(x[2][0]) + asf(x[3][0])),
                                                     (asf(x[0][2]) + asf(x[1][2])) + (asf(x[2][2]) + asf(x[3][2])));
    WG_BARRIER();
    q = *(LAS v4u *)(L + L_Q + a4 * 64 + (4 * g4 + u4) * 4);
  }
  STAMP_AT(2);
  float m2 = -3.0e38f, lsum = 0.f;
  f32x4 acc0 = {0.f, 0.f, 0.f, 0.f}, acc1 = acc0;
  for (int it = 0; it < 7; ++it) {
    const int s0 = tbase + (2 * it + 2) * TSTR;
    if (TAU) { while ((unsigned)__builtin_amdgcn_s_memrealtime() - tstart < (unsigned)(TOFF + 2 * it * TAU)) __builtin_amdgcn_s_sleep(1); }
    tile_step<true>(ta, q, rk, rv, koff, voff, s0, mk0, mk1, m2, lsum, acc0, acc1);
    if (TAU) { while ((unsigned)__builtin_amdgcn_s_memrealtime() - tstart < (unsigned)(TOFF + (2 * it + 1) * TAU)) __builtin_amdgcn_s_sleep(1); }
    tile_step<true>(tb, q, rk, rv, koff, voff, s0 + TSTR, mk0, mk1, m2, lsum, acc0, acc1);
  }
  const bool role_wo = b < NPW;
  const rsrc_t rwo = __builtin_amdgcn_make_buffer_rsrc((char *)p.wo + (size_t)(role_wo ? b : 0) * 32768, 0, role_wo ? 32768 : 0, RS3);
  const int foff = g4 * 1024 + w * 256 + (lane & 15) * 16;
  v4u wo[8];
#pragma unroll
  for (int i = 0; i < 8; ++i) wo[i] = ld16<WAUX>(rwo, foff, i * 4096);
  SB();
  tile_step<false>(ta, q, rk, rv, koff, voff, 0, mk0, mk1, m2, lsum, acc0, acc1);
  tile_step<false>(tb, q, rk, rv, koff, voff, 0, mk0, mk1, m2, lsum, acc0, acc1);
  STAMP_AT(3);
  {
    const float lw = xr16_sum(lsum);
    *(LAS f32x4 *)(L + L_MRG + w * 512 + g4 * 128 + 8 * (lane & 15)) = acc0;
    *(LAS f32x4 *)(L + L_MRG + w * 512 + g4 * 128 + 8 * (lane & 15) + 4) = acc1;
    if (lane < 16 && u4 == 0) { f32x2 y = {m2, lw}; *(LAS f32x2 *)(L + L_ML + (w * 4 + a4) * 2) = y; }
    WG_BARRIER();
    f32x2 ml[4];
#pragma unroll
    for (int ww = 0; ww < 4; ++ww) ml[ww] = *(LAS f32x2 *)(L + L_ML + (ww * 4 + w) * 2);
    const float M = fmaxf(fmaxf(ml[0][0], ml[1][0]), fmaxf(ml[2][0], ml[3][0]));
    float Ls = 0.f; f32x2 o = {0.f, 0.f};
#pragma unroll
    for (int ww = 0; ww < 4; ++ww) {
      const float e = __builtin_amdgcn_exp2f(ml[ww][0] - M);
      Ls = fmaf(e, ml[ww][1], Ls);
      o += e * *(LAS f32x2 *)(L + L_MRG + ww * 512 + w * 128 + 2 * lane);
    }
    const int c4 = (4 * kh + w) * 4;
    st8(rws, WS_OP + ((c4 + g4) * 32 + sp) * 128 + (lane & 15) * 8, v2u{cvtpk(o[0], o[1]), tag});
    if (lane < 4) st16(rws, WS_ML + ((c4 + lane) * 32 + sp) * 16, v4u{asu(M), tag, asu(Ls), tag});
  }
  STAMP_AT(4);
  if (role_wo) {
    const int off[2] = {WS_OP + b * 4096 + tid * 16, WS_ML + (b * 32 + (tid >> 3)) * 16};
    v4u x[2];
    GATHER(2, off, x);
    STAMP_AT(5);
    const float ms = asf(x[1][0]), ls = asf(x[1][2]);
    const float Mw = xr16_max(fmaxf(ms, dpp<ROR8>(ms)));
    const float e = __builtin_amdgcn_exp2f(ms - Mw);
    auto sa = __builtin_amdgcn_permlane32_swap(asu(e * lo16(x[0][0])), asu(e * lo16(x[0][2])), false, false);
    auto sb = __builtin_amdgcn_permlane32_swap(asu(e * hi16(x[0][0])), asu(e * hi16(x[0][2])), false, false);
    auto sc = __builtin_amdgcn_permlane16_swap(asu(asf(sa[0]) + asf(sa[1])), asu(asf(sb[0]) + asf(sb[1])), false, false);
    float v = asf(sc[0]) + asf(sc[1]);
    v += dpp<ROR8>(v);
    const float lw = strided_sum<8>(e * ls);
    if ((lane & 8) == 0) L[L_RED2 + w * 64 + (2 * (lane >> 5) + ((lane >> 4) & 1)) * 8 + (lane & 7)] = v;
    if (lane == 0) *(LAS f32x2 *)(L + L_RED2 + w * 64 + 32) = f32x2{Mw, lw};
    WG_BARRIER();
    f32x2 ml[4];
#pragma unroll
    for (int ww = 0; ww < 4; ++ww) ml[ww] = *(LAS f32x2 *)(L + L_RED2 + ww * 64 + 32);
    const float M = fmaxf(fmaxf(ml[0][0], ml[1][0]), fmaxf(ml[2][0], ml[3][0]));
    f32x4 oa = {0.f, 0.f, 0.f, 0.f}, ob = oa;
    float Lt = 0.f;
#pragma unroll
    for (int ww = 0; ww < 4; ++ww) {
      const float ew = __builtin_amdgcn_exp2f(ml[ww][0] - M);
      Lt = fmaf(ew, ml[ww][1], Lt);
      oa += ew * *(LAS f32x4 *)(L + L_RED2 + ww * 64 + g4 * 8);
      ob += ew * *(LAS f32x4 *)(L + L_RED2 + ww * 64 + g4 * 8 + 4);
    }
    const float rl = __builtin_amdgcn_rcpf(Lt);
    f32x4 c0 = {0.f, 0.f, 0.f, 0.f}, c1 = c0;
#pragma unroll
    for (int i = 0; i < 8; ++i) { const float o = (i < 4 ? oa[i & 3] : ob[i & 3]) * rl; gemv_step(cvtpk(o, o), mk0, mk1, wo[i], c0, c1); }
    publish512(rws, WS_PB, NPW, b, c0, c1, L + L_TR, w, lane, tag);
    KEEP(2, x);
    STAMP_AT(6);
  } else if (b < NPW + NPF) {
    const int pf = (b & 7) * 7 + ((b - NPW) >> 3);
    const rsrc_t rwg = __builtin_amdgcn_make_buffer_rsrc((char *)p.wg + pf * 64, 0, DM * FF * 2 - pf * 64, RS3);
    const rsrc_t rwu = __builtin_amdgcn_make_buffer_rsrc((char *)p.wu + pf * 64, 0, DM * FF * 2 - pf * 64, RS3);
    const rsrc_t rwd = __builtin_amdgcn_make_buffer_rsrc((char *)p.wd + (size_t)pf * 32768, 0, 32768, RS3);
    const int go = (128 * w + 4 * g4 + a4) * (FF * 2) + u4 * 16;
    v4u wg[8], wu[8], wd[8];
#pragma unroll
    for (int i = 0; i < 8; ++i) { wg[i] = ld16<WAUX>(rwg, go, i * 16 * FF * 2); wu[i] = ld16<WAUX>(rwu, go, i * 16 * FF * 2); }
#pragma unroll
    for (int i = 0; i < 8; ++i) wd[i] = ld16<WAUX>(rwd, foff, i * 4096);
    SB();
      v4u x[1];
      const int off[1] = {WS_HG + (b % REP) * 2048 + (32 * w + (lane & 31)) * 16};
      GATHER(1, off, x);
      if (lane < 32) *(LAS v2u *)((LAS unsigned *)L + L_Q + 64 * w + 2 * lane) = v2u{x[0][0], x[0][2]};
    WG_BARRIER();
    STAMP_AT(9);
    {
      const v4u hb = *(LAS v4u *)(L + L_Q + 4 * lane);
      const unsigned own = ((LAS unsigned *)L)[L_Q + 64 * w + lane];
      ((LAS unsigned *)L)[L_XN + w * 64 + lane] = ln_pair(own, ln_stats(hb));
      LDS_WAIT();
    }
    STAMP_AT(12);
    f32x4 g0 = {0.f, 0.f, 0.f, 0.f}, g1 = g0, u0 = g0, u1 = g0;
#pragma unroll
    for (int i = 0; i < 8; ++i) {
      const unsigned xv = ((LAS unsigned short *)(L + L_XN + w * 64))[16 * i + 4 * g4 + a4];
      const unsigned pp = xv | (xv << 16);
      gemv_step(pp, mk0, mk1, wg[i], g0, g1);
      gemv_step(pp, mk0, mk1, wu[i], u0, u1);
    }
    if (g4 == a4) {
      LAS float *gu = L + L_MRG + (w * 4 + g4) * 64 + 8 * u4;
      *(LAS f32x4 *)(gu) = g0; *(LAS f32x4 *)(gu + 4) = g1; *(LAS f32x4 *)(gu + 32) = u0; *(LAS f32x4 *)(gu + 36) = u1;
    }
    STAMP_AT(13);
    WG_BARRIER();
    {
      const int c = tid >> 3, part = tid & 7;
      LAS float *gu = L + L_MRG + part * 128 + c;
      float g = gu[0] + gu[64], u = gu[32] + gu[96];
      g += dpp<XOR1>(g); u += dpp<XOR1>(u); g += dpp<XOR2>(g); u += dpp<XOR2>(u); g += dpp<HMIR>(g); u += dpp<HMIR>(u);
      const float hv = rbf(g) * rbf(u);
      if (part == 0) ((LAS unsigned *)L)[L_HID + (c & 3) * 8 + (c >> 2)] = cvtpk(hv, hv);
    }
    WG_BARRIER();
    STAMP_AT(14);
    const v4u ha = *(LAS v4u *)(L + L_HID + g4 * 8), hc = *(LAS v4u *)(L + L_HID + g4 * 8 + 4);
    f32x4 c0 = {0.f, 0.f, 0.f, 0.f}, c1 = c0;
#pragma unroll
    for (int i = 0; i < 8; ++i) gemv_step(i < 4 ? ha[i & 3] : hc[i & 3], mk0, mk1, wd[i], c0, c1);
    publish512(rws, WS_PD, NPF, pf, c0, c1, L + L_TR, w, lane, tag);
    KEEP(1, x);
    STAMP_AT(10);
  } else if (b < NPW + NPF + 32) {
    const int j = b - (NPW + NPF);
    const v2u xv = __builtin_amdgcn_raw_buffer_load_b64(rx, (16 * j + 4 * (tid & 3)) * 2, 0, 0);
    const int off[2] = {WS_PB + j * (NPW * 64) + tid * 16, WS_PB + j * (NPW * 64) + 4096 + tid * 16};
    v4u x[2];
    GATHER(2, off, x);
    STAMP_AT(7);
    const f32x4 r = reduce_cols<2>(x, true, L + L_RED, w, lane);
    const unsigned h0 = cvtpk(lo16(xv[0]) + rbf(r[0]), hi16(xv[0]) + rbf(r[1])), h1 = cvtpk(lo16(xv[1]) + rbf(r[2]), hi16(xv[1]) + rbf(r[3]));
    if (tid < 4 * REP) st16(rws, WS_HG + (tid >> 2) * 2048 + (4 * j + (tid & 3)) * 16, v4u{h0, tag, h1, tag});
    KEEP(2, x);
    STAMP_AT(8);
  } else if (b < NPW + NPF + 64) {
    const int j = b - (NPW + NPF + 32);
    const bool live = tid < NPF * 4;
    const int off[2] = {WS_HG + (b % REP) * 2048 + (4 * j + (tid & 3)) * 16, WS_PD + j * (NPF * 64) + (live ? tid : tid - 128) * 16};
    v4u x[2];
    GATHER(2, off, x);
    const v4u y[1] = {x[1]};
    const f32x4 r = reduce_cols<1>(y, live, L + L_RED, w, lane);
    const v2u o = {cvtpk(lo16(x[0][0]) + rbf(r[0]), hi16(x[0][0]) + rbf(r[1])), cvtpk(lo16(x[0][2]) + rbf(r[2]), hi16(x[0][2]) + rbf(r[3]))};
    if (tid < 4) *(v2u *)((char *)p.out + (16 * j + 4 * tid) * 2) = o;
    if (j == 0 && tid == 0) __builtin_amdgcn_raw_buffer_store_b32(tag, rws, WS_EPOCH, 0, AUX_SC1);
    KEEP(2, x);
  }
  STAMP_AT(11);
  while ((unsigned)__builtin_amdgcn_s_memrealtime() - tstart < 10000u) __builtin_amdgcn_s_sleep(2);
  STAMP_FLUSH();
}
}

extern "C" void kernel_launch(void *const *d_in, const int *in_sizes, int n_in, void *d_out, int out_size, void *d_ws, size_t ws_size,
                              hipStream_t stream) {
  Params p;
  p.x = d_in[0]; p.k = d_in[1]; p.v = d_in[2]; p.wq = d_in[3]; p.wo = d_in[4];
  p.wg = d_in[6]; p.wu = d_in[7]; p.wd = d_in[8];
  p.out = d_out; p.ws = d_ws;
  (void)hipFuncSetAttribute((const void *)fused_decode_kernel, hipFuncAttributeMaxDynamicSharedMemorySize, LDS_BYTES);
  fused_decode_kernel<<<NWG, NTHR, LDS_BYTES, stream>>>(p);
}
```

```cpp
#include <hip/hip_runtime.h>
#include <stdint.h>

#define STAMP 0
#define REP 16
#define KAUX 2
#define VAUX 2
#define WAUX 2
#define PINARGS 1
#define ORD 1
#define HM 1
#define TAU 0
#define TOFF 500
#define TK 60
#define TPOLL 280
#define DREP 8
#define DEFER 0
#define PIPEPOLL 0
#define QREP 4

namespace {
typedef unsigned v4u __attribute__((ext_vector_type(4)));
typedef unsigned v2u __attribute__((ext_vector_type(2)));
typedef float f32x4 __attribute__((ext_vector_type(4)));
typedef float f32x2 __attribute__((ext_vector_type(2)));
typedef short s16x4 __attribute__((ext_vector_type(4)));
typedef __bf16 bf16x8 __attribute__((ext_vector_type(8)));
typedef __bf16 bf16x2 __attribute__((ext_vector_type(2)));
typedef __amdgpu_buffer_rsrc_t rsrc_t;
#define LAS __attribute__((address_space(3)))

constexpr int DM = 512, SK = 65536, DH = 128, FF = 1792;
constexpr int NWG = 256, NTHR = 256;
constexpr int NPW = 128, NPF = 56;
constexpr int AUX_NT = 2, AUX_SC1 = 16;
constexpr int RS3 = 0x00020000;
constexpr int WS_EPOCH = 0;
constexpr int WS_ML = 4096;
constexpr int WS_HG = WS_ML + 65536;
constexpr int WS_DN = WS_HG + 65536;
constexpr int WS_OP = 1 << 20;
constexpr int WS_PB = 2 << 20;
constexpr int WS_PD = 3 << 20;
constexpr int WS_QG = 4 << 20;
constexpr int WS_USED = 5 << 20;
constexpr int WS_STAMP = 8 << 20;
constexpr unsigned SPIN_CAP = 1u << 14;
constexpr float LOG2E = 1.4426950408889634f;
constexpr float SCALE = 0.08838834764831845f;

struct Params {
  const void *x, *k, *v, *wq, *wo, *wg, *wu, *wd;
  void *out, *ws;
};

__device__ __forceinline__ float asf(unsigned u) { return __builtin_bit_cast(float, u); }
__device__ __forceinline__ unsigned asu(float f) { return __builtin_bit_cast(unsigned, f); }
template <int C> __device__ __forceinline__ float dpp(float x) {
  return __builtin_bit_cast(float, __builtin_amdgcn_mov_dpp(__builtin_bit_cast(int, x), C, 0xf, 0xf, true));
}
constexpr int XOR1 = 0xB1, XOR2 = 0x4E, HMIR = 0x141, MIR = 0x140, ROR4 = 0x124, ROR8 = 0x128;
__device__ __forceinline__ unsigned cvtpk(float lo, float hi) {
  f32x2 v = {lo, hi};
  bf16x2 r = __builtin_convertvector(v, bf16x2);
  return __builtin_bit_cast(unsigned, r);
}
__device__ __forceinline__ float lo16(unsigned u) { return asf(u << 16); }
__device__ __forceinline__ float hi16(unsigned u) { return asf(u & 0xffff0000u); }
__device__ __forceinline__ float rbf(float x) { return lo16(cvtpk(x, x)); }
__device__ __forceinline__ float xr16_max(float x) {
  auto s = __builtin_amdgcn_permlane16_swap(asu(x), asu(x), false, false);
  x = fmaxf(asf(s[0]), asf(s[1]));
  auto t = __builtin_amdgcn_permlane32_swap(asu(x), asu(x), false, false);
  return fmaxf(asf(t[0]), asf(t[1]));
}
__device__ __forceinline__ float xr16_sum(float x) {
  auto s = __builtin_amdgcn_permlane16_swap(asu(x), asu(x), false, false);
  x = asf(s[0]) + asf(s[1]);
  auto t = __builtin_amdgcn_permlane32_swap(asu(x), asu(x), false, false);
  return asf(t[0]) + asf(t[1]);
}
__device__ __forceinline__ float row_sum(float x) {
  x += dpp<XOR1>(x); x += dpp<XOR2>(x); x += dpp<HMIR>(x); x += dpp<MIR>(x); return x;
}
__device__ __forceinline__ float row_max(float x) {
  x = fmaxf(x, dpp<XOR1>(x)); x = fmaxf(x, dpp<XOR2>(x)); x = fmaxf(x, dpp<HMIR>(x)); x = fmaxf(x, dpp<MIR>(x)); return x;
}
__device__ __forceinline__ float wave_sum(float x) { return xr16_sum(row_sum(x)); }
__device__ __forceinline__ f32x4 mfma32(v4u a, v4u b, f32x4 c) {
  return __builtin_amdgcn_mfma_f32_16x16x32_bf16(__builtin_bit_cast(bf16x8, a), __builtin_bit_cast(bf16x8, b), c, 0, 0, 0);
}
__device__ __forceinline__ f32x4 mfma16(v2u a, unsigned b0, unsigned b1, f32x4 c) {
  v2u b = {b0, b1};
  return __builtin_amdgcn_mfma_f32_16x16x16bf16_1k(__builtin_bit_cast(s16x4, a), __builtin_bit_cast(s16x4, b), c, 0, 0, 0);
}
template <int AUX> __device__ __forceinline__ v4u ld16(rsrc_t r, int voff, int soff) {
  return __builtin_amdgcn_raw_buffer_load_b128(r, voff, soff, AUX);
}
__device__ __forceinline__ void st16(rsrc_t r, int voff, v4u v) { __builtin_amdgcn_raw_buffer_store_b128(v, r, voff, 0, AUX_SC1); }
__device__ __forceinline__ void st8(rsrc_t r, int voff, v2u v) { __builtin_amdgcn_raw_buffer_store_b64(v, r, voff, 0, AUX_SC1); }
#define WG_BARRIER() asm volatile("s_waitcnt lgkmcnt(0)\n\ts_barrier" ::: "memory")
#define LDS_WAIT() asm volatile("s_waitcnt lgkmcnt(0)" ::: "memory")
#define SB() __builtin_amdgcn_sched_barrier(0)

struct LnStat { float mb, rden; float den; };
__device__ __forceinline__ LnStat ln_stats(v4u p) {
  float s = 0.f, q = 0.f;
#pragma unroll
  for (int i = 0; i < 4; ++i) {
    const float a = lo16(p[i]), b = hi16(p[i]);
    s += a + b;
    q = fmaf(a, a, fmaf(b, b, q));
  }
  s += dpp<XOR1>(s); q += dpp<XOR1>(q); s += dpp<XOR2>(s); q += dpp<XOR2>(q);
  s += dpp<HMIR>(s); q += dpp<HMIR>(q); s += dpp<MIR>(s); q += dpp<MIR>(q);
  auto t = __builtin_amdgcn_permlane16_swap(asu(s), asu(q), false, false);
  float y = asf(t[0]) + asf(t[1]);
  auto u = __builtin_amdgcn_permlane32_swap(asu(y), asu(y), false, false);
  y = asf(u[0]) + asf(u[1]);
  auto v = __builtin_amdgcn_permlane16_swap(asu(y), asu(y), false, false);
  const float mean = asf(v[0]) * (1.f / DM);
  const float var = fmaf(-mean, mean, asf(v[1]) * (1.f / DM));
  LnStat r; r.mb = rbf(mean); r.den = rbf(__builtin_amdgcn_sqrtf(rbf(rbf(var) + 1e-5f))); r.rden = __builtin_amdgcn_rcpf(r.den);
  return r;
}
__device__ __forceinline__ unsigned ln_pair(unsigned p, LnStat st) { return cvtpk(rbf(lo16(p) - st.mb) * st.rden, rbf(hi16(p) - st.mb) * st.rden); }
struct Tile { v4u k[8], v[8]; };

template <bool REFILL>
__device__ __forceinline__ void tile_step(Tile &t, v4u q, rsrc_t rk, rsrc_t rv, int koff, int voff, int snext, unsigned mk0, unsigned mk1,
                                          float &m2, float &lsum, f32x4 &acc0, f32x4 &acc1) {
  float s[8];
  const f32x4 z = {0.f, 0.f, 0.f, 0.f};
  f32x4 d[8];
#pragma unroll
  for (int g = 0; g < 8; ++g) {
    d[g] = mfma32(t.k[g], q, z);
    if (REFILL) t.k[g] = ld16<KAUX>(rk, koff, snext + g * 1024);
    SB();
  }
#pragma unroll
  for (int g = 0; g < 8; ++g) {
    s[g] = ((dpp<0x00>(d[g][0]) + dpp<0x55>(d[g][1])) + dpp<0xAA>(d[g][2])) + dpp<0xFF>(d[g][3]);
    asm("" : "+v"(s[g]));
  }
#pragma unroll
  for (int g = 0; g < 8; g += 2) {
    const unsigned a = cvtpk(s[g], s[g + 1]);
    const unsigned b = cvtpk(lo16(a) * SCALE, hi16(a) * SCALE);
    s[g] = lo16(b); s[g + 1] = hi16(b);
  }
  float mx = fmaxf(fmaxf(fmaxf(s[0], s[1]), fmaxf(s[2], s[3])), fmaxf(fmaxf(s[4], s[5]), fmaxf(s[6], s[7])));
  mx = xr16_max(mx);
  const float mn = fmaxf(m2, mx * LOG2E);
  const float alpha = __builtin_amdgcn_exp2f(m2 - mn);
  m2 = mn;
  lsum *= alpha;
  int ai = __builtin_bit_cast(int, alpha), ar;
  ar = __builtin_amdgcn_update_dpp(ai, ai, 0x150, 0x1, 0xf, false);
  ar = __builtin_amdgcn_update_dpp(ar, ai, 0x154, 0x2, 0xf, false);
  ar = __builtin_amdgcn_update_dpp(ar, ai, 0x158, 0x4, 0xf, false);
  ar = __builtin_amdgcn_update_dpp(ar, ai, 0x15c, 0x8, 0xf, false);
  const float al = __builtin_bit_cast(float, ar);
  acc0 *= al; acc1 *= al;
#pragma unroll
  for (int g = 0; g < 8; ++g) {
    const float p = __builtin_amdgcn_exp2f(fmaf(s[g], LOG2E, -mn));
    lsum += p;
    const unsigned pp = cvtpk(p, p);
    const v2u a = {pp & mk0, pp & mk1};
    acc0 = mfma16(a, t.v[g][0], t.v[g][1], acc0);
    acc1 = mfma16(a, t.v[g][2], t.v[g][3], acc1);
    if (REFILL) t.v[g] = ld16<VAUX>(rv, voff, snext + g * 1024);
    SB();
  }
}

template <int N>
__device__ __forceinline__ bool tags_ok(const v4u (&x)[N], unsigned tag) {
  bool ok = true;
#pragma unroll
  for (int i = 0; i < N; ++i) ok = ok & (x[i][1] == tag) & (x[i][3] == tag);
  return __all(ok);
}
template <int N>
__device__ __forceinline__ void sweep(rsrc_t rs, const int (&off)[N], v4u (&x)[N]) {
  asm volatile("" ::: "memory");
#pragma unroll
  for (int i = 0; i < N; ++i) x[i] = ld16<AUX_SC1>(rs, off[i], 0);
}
template <int N>
__device__ __forceinline__ void gather(rsrc_t rs, const int (&off)[N], unsigned tag, v4u (&x)[N]) {
  for (unsigned spins = 0;; ++spins) {
    sweep<N>(rs, off, x);
    if (tags_ok<N>(x, tag) || spins > SPIN_CAP) break;
    __builtin_amdgcn_s_sleep(1);
  }
}
#define GATHER(N, off, x) gather<N>(rws, off, tag, x)
#define KEEP(N, x) do { } while (0)
template <int S> __device__ __forceinline__ float strided_sum(float x) {
  if (S <= 1) x += dpp<XOR1>(x);
  if (S <= 2) x += dpp<XOR2>(x);
  if (S <= 8) x += dpp<ROR8>(x);
  if (S <= 4) x += dpp<ROR4>(x);
  return xr16_sum(x);
}
__device__ __forceinline__ void publish512(rsrc_t rs, int base, int nprod, int p, f32x4 a0, f32x4 a1, LAS float *tr, int w, int lane, unsigned tag) {
  LAS unsigned *t = (LAS unsigned *)tr + w * 64;
  if (lane < 16) *(LAS v4u *)(t + 4 * lane) = v4u{cvtpk(a0[0], a0[1]), cvtpk(a0[2], a0[3]), cvtpk(a1[0], a1[1]), cvtpk(a1[2], a1[3])};
  LDS_WAIT();
  const v2u y = *(LAS v2u *)(t + 2 * (lane & 31));
  if (lane < 32) st16(rs, base + ((8 * w + (lane >> 2)) * nprod + p) * 64 + (lane & 3) * 16, v4u{y[0], tag, y[1], tag});
}
template <int N>
__device__ __forceinline__ f32x4 reduce_cols(const v4u (&x)[N], bool last_live, LAS float *red, int w, int lane) {
  f32x4 s = {0.f, 0.f, 0.f, 0.f};
#pragma unroll
  for (int i = 0; i < N; ++i) {
    const f32x4 v = {lo16(x[i][0]), hi16(x[i][0]), lo16(x[i][2]), hi16(x[i][2])};
    if (i + 1 < N) s += v; else s += last_live ? v : f32x4{0.f, 0.f, 0.f, 0.f};
  }
  auto sa = __builtin_amdgcn_permlane32_swap(asu(s[0]), asu(s[2]), false, false);
  auto sb = __builtin_amdgcn_permlane32_swap(asu(s[1]), asu(s[3]), false, false);
  auto sc = __builtin_amdgcn_permlane16_swap(asu(asf(sa[0]) + asf(sa[1])), asu(asf(sb[0]) + asf(sb[1])), false, false);
  float v = asf(sc[0]) + asf(sc[1]);
  v += dpp<ROR8>(v); v += dpp<ROR4>(v);
  if ((lane & 12) == 0) red[w * 64 + 4 * (lane & 3) + 2 * (lane >> 5) + ((lane >> 4) & 1)] = v;
  WG_BARRIER();
  f32x4 r = {0.f, 0.f, 0.f, 0.f};
#pragma unroll
  for (int ww = 0; ww < 4; ++ww) r += *(LAS f32x4 *)(red + ww * 64 + 4 * (lane & 3));
  return r;
}
__device__ __forceinline__ void gemv_step(unsigned pp, unsigned mk0, unsigned mk1, v4u f, f32x4 &c0, f32x4 &c1) {
  const v2u a = {pp & mk0, pp & mk1};
  c0 = mfma16(a, f[0], f[1], c0);
  c1 = mfma16(a, f[2], f[3], c1);
}

#define STAMP_AT(i) do { } while (0)
#define STAMP_FLUSH() do { } while (0)

constexpr int L_MRG = 0;
constexpr int L_ML = L_MRG + 2048;
constexpr int L_RED = L_ML + 32;
constexpr int L_RED2 = L_RED + 256;
constexpr int L_TR = L_RED2 + 256;
constexpr int L_Q = L_TR + 512;
constexpr int L_XN = L_Q + 256;
constexpr int L_HID = L_XN + 1024;
constexpr int L_END = L_HID + 32;
constexpr int LDS_BYTES = 84 * 1024;

__global__ void __launch_bounds__(NTHR, 2) fused_decode_kernel(Params p) {
  extern __shared__ __attribute__((aligned(16))) float smem[];
  LAS float *L = (LAS float *)smem;
  const int tid = threadIdx.x, lane = tid & 63, w = __builtin_amdgcn_readfirstlane(tid >> 6), b = blockIdx.x;
  asm volatile("" :: "s"(p.x), "s"(p.k), "s"(p.v), "s"(p.wq), "s"(p.wo), "s"(p.wg), "s"(p.wu), "s"(p.wd), "s"(p.out), "s"(p.ws));
  STAMP_AT(0);
  const unsigned tstart = (unsigned)__builtin_amdgcn_s_memrealtime();
  const int kh = HM ? b >> 5 : b & 7, sp = HM ? b & 31 : b >> 3;
  const rsrc_t rws = __builtin_amdgcn_make_buffer_rsrc(p.ws, 0, WS_USED, RS3);
  const rsrc_t rk = __builtin_amdgcn_make_buffer_rsrc((char *)p.k + (size_t)kh * SK * 256, 0, SK * 256, RS3);
  const rsrc_t rv = __builtin_amdgcn_make_buffer_rsrc((char *)p.v + (size_t)kh * SK * 256, 0, SK * 256, RS3);
  const int tbase = ORD == 1 ? sp * 524288 + w * 8192 : ORD == 2 ? (sp * 4 + w) * 8192 : (w * 32 + sp) * 8192;
  constexpr int TSTR = ORD == 1 ? 4 * 8192 : 128 * 8192;
  const rsrc_t rx = __builtin_amdgcn_make_buffer_rsrc((void *)p.x, 0, DM * 2, RS3);
  const rsrc_t rwq = __builtin_amdgcn_make_buffer_rsrc((void *)p.wq, 0, DM * 4096 * 2, RS3);
  const unsigned epoch = *(const __attribute__((address_space(4))) unsigned *)((const char *)p.ws + WS_EPOCH);
  const bool hasq = (b & 7) < 4;
  const int pq = (b >> 3) * 4 + (b & 3), hq = pq & 31, rb = pq >> 5;
  const int g4 = lane >> 4, a4 = (lane >> 2) & 3, u4 = lane & 3;
  const int koff = a4 * 256 + (4 * g4 + u4) * 16, voff = lane * 16;
  const unsigned mk0 = u4 == 0 ? 0xFFFFu : u4 == 1 ? 0xFFFF0000u : 0u;
  const unsigned mk1 = u4 == 2 ? 0xFFFFu : u4 == 3 ? 0xFFFF0000u : 0u;
  v4u wqf[8];
  unsigned xq[8];
  Tile ta, tb;
  if (hasq) {
    const v4u xr = ld16<0>(rx, lane * 16, 0);
    const unsigned xown = __builtin_amdgcn_raw_buffer_load_b32(rx, (64 * rb + 16 * w + (lane & 15)) * 4, 0, 0);
    const int wqoff = (128 * rb + 32 * w + g4) * 8192 + hq * 256 + (lane & 15) * 16;
#pragma unroll
    for (int i = 0; i < 8; ++i) wqf[i] = ld16<WAUX>(rwq, wqoff, i * 4 * 8192);
    SB();
    ((LAS unsigned *)L)[L_XN + w * 64 + lane] = ln_pair(xown, ln_stats(xr));
    LDS_WAIT();
#pragma unroll
    for (int i = 0; i < 8; ++i) xq[i] = ((LAS unsigned short *)(L + L_XN + w * 64))[4 * i + g4];
    STAMP_AT(12);
  }
  if (hasq) {
    f32x4 c0 = {0.f, 0.f, 0.f, 0.f}, c1 = c0;
#pragma unroll
    for (int i = 0; i < 8; ++i) {
      gemv_step(xq[i] | (xq[i] << 16), mk0, mk1, wqf[i], c0, c1);
    }
    if (lane < 16) {
      *(LAS f32x4 *)(L + L_TR + w * 128 + 8 * lane) = c0;
      *(LAS f32x4 *)(L + L_TR + w * 128 + 8 * lane + 4) = c1;
    }
    STAMP_AT(13);
    WG_BARRIER();
    if (w < QREP) {
      f32x2 y = {0.f, 0.f};
#pragma unroll
      for (int ww = 0; ww < 4; ++ww) y += *(LAS f32x2 *)(L + L_TR + ww * 128 + 2 * lane);
      st16(rws, WS_QG + w * 131072 + ((hq * 4 + rb) * 128 + 2 * lane) * 8, v4u{asu(y[0]), epoch + 1u, asu(y[1]), epoch + 1u});
    }
  }
  STAMP_AT(1);
#pragma unroll
  for (int g = 0; g < 8; ++g) ta.k[g] = ld16<KAUX>(rk, koff, tbase + g * 1024);
#pragma unroll
  for (int g = 0; g < 8; ++g) ta.v[g] = ld16<VAUX>(rv, voff, tbase + g * 1024);
  SB();
  const unsigned tag = epoch + 1u;
  while ((unsigned)__builtin_amdgcn_s_memrealtime() - tstart < (unsigned)TPOLL) __builtin_amdgcn_s_sleep(1);
  v4u q;
  {
    const int qoff = WS_QG + (sp & (QREP - 1)) * 131072 + (4 * kh + w) * 4096 + lane * 16;
    v4u x[4];
#pragma unroll
    for (int i = 0; i < 4; ++i) x[i] = ld16<AUX_SC1>(rws, qoff, i * 1024);
    SB();
    asm volatile("s_barrier" ::: "memory");
    SB();
#pragma unroll
    for (int g = 0; g < 8; ++g) tb.k[g] = ld16<KAUX>(rk, koff, tbase + TSTR + g * 1024);
#pragma unroll
    for (int g = 0; g < 8; ++g) tb.v[g] = ld16<VAUX>(rv, voff, tbase + TSTR + g * 1024);
    SB();
    bool ok = true;
#pragma unroll
    for (int i = 0; i < 4; ++i) ok = ok & (x[i][1] == tag) & (x[i][3] == tag);
    if (__builtin_expect(!__all(ok), 0)) {
      for (unsigned spins = 0; spins < SPIN_CAP; ++spins) {
        bool ok2 = true;
        asm volatile("" ::: "memory");
#pragma unroll
        for (int i = 0; i < 4; ++i) x[i] = ld16<AUX_SC1>(rws, qoff, i * 1024);
#pragma unroll
        for (int i = 0; i < 4; ++i) ok2 = ok2 & (x[i][1] == tag) & (x[i][3] == tag);
        if (__all(ok2)) break;
      }
      __builtin_amdgcn_s_waitcnt(0x0F70);
    }
    ((LAS unsigned *)L)[L_Q + w * 64 + lane] = cvtpk((asf(x[0][0]) + asf(x[1][0])) + (asf(x[2][0]) + asf(x[3][0])),
                                                     (asf(x[0][2]) + asf(x[1][2])) + (asf(x[2][2]) + asf(x[3][2])));
    WG_BARRIER();
    q = *(LAS v4u *)(L + L_Q + a4 * 64 + (4 * g4 + u4) * 4);
  }
  STAMP_AT(2);
  float m2 = -3.0e38f, lsum = 0.f;
  f32x4 acc0 = {0.f, 0.f, 0.f, 0.f}, acc1 = acc0;
  for (int it = 0; it < 7; ++it) {
    const int s0 = tbase + (2 * it + 2) * TSTR;
    if (TAU) { while ((unsigned)__builtin_amdgcn_s_memrealtime() - tstart < (unsigned)(TOFF + 2 * it * TAU)) __builtin_amdgcn_s_sleep(1); }
    tile_step<true>(ta, q, rk, rv, koff, voff, s0, mk0, mk1, m2, lsum, acc0, acc1);
    if (TAU) { while ((unsigned)__builtin_amdgcn_s_memrealtime() - tstart < (unsigned)(TOFF + (2 * it + 1) * TAU)) __builtin_amdgcn_s_sleep(1); }
    tile_step<true>(tb, q, rk, rv, koff, voff, s0 + TSTR, mk0, mk1, m2, lsum, acc0, acc1);
  }
  const bool role_wo = b < NPW;
  const rsrc_t rwo = __builtin_amdgcn_make_buffer_rsrc((char *)p.wo + (size_t)(role_wo ? b : 0) * 32768, 0, role_wo ? 32768 : 0, RS3);
  const int foff = g4 * 1024 + w * 256 + (lane & 15) * 16;
  v4u wo[8];
#pragma unroll
  for (int i = 0; i < 8; ++i) wo[i] = ld16<WAUX>(rwo, foff, i * 4096);
  SB();
  tile_step<false>(ta, q, rk, rv, koff, voff, 0, mk0, mk1, m2, lsum, acc0, acc1);
  tile_step<false>(tb, q, rk, rv, koff, voff, 0, mk0, mk1, m2, lsum, acc0, acc1);
  STAMP_AT(3);
  {
    const float lw = xr16_sum(lsum);
    *(LAS f32x4 *)(L + L_MRG + w * 512 + g4 * 128 + 8 * (lane & 15)) = acc0;
    *(LAS f32x4 *)(L + L_MRG + w * 512 + g4 * 128 + 8 * (lane & 15) + 4) = acc1;
    if (lane < 16 && u4 == 0) { f32x2 y = {m2, lw}; *(LAS f32x2 *)(L + L_ML + (w * 4 + a4) * 2) = y; }
    WG_BARRIER();
    f32x2 ml[4];
#pragma unroll
    for (int ww = 0; ww < 4; ++ww) ml[ww] = *(LAS f32x2 *)(L + L_ML + (ww * 4 + w) * 2);
    const float M = fmaxf(fmaxf(ml[0][0], ml[1][0]), fmaxf(ml[2][0], ml[3][0]));
    float Ls = 0.f; f32x2 o = {0.f, 0.f};
#pragma unroll
    for (int ww = 0; ww < 4; ++ww) {
      const float e = __builtin_amdgcn_exp2f(ml[ww][0] - M);
      Ls = fmaf(e, ml[ww][1], Ls);
      o += e * *(LAS f32x2 *)(L + L_MRG + ww * 512 + w * 128 + 2 * lane);
    }
    const int c4 = (4 * kh + w) * 4;
    st8(rws, WS_OP + ((c4 + g4) * 32 + sp) * 128 + (lane & 15) * 8, v2u{cvtpk(o[0], o[1]), tag});
    if (lane < 4) st16(rws, WS_ML + ((c4 + lane) * 32 + sp) * 16, v4u{asu(M), tag, asu(Ls), tag});
  }
  STAMP_AT(4);
  if (role_wo) {
    const int off[2] = {WS_OP + b * 4096 + tid * 16, WS_ML + (b * 32 + (tid >> 3)) * 16};
    v4u x[2];
    GATHER(2, off, x);
    STAMP_AT(5);
    const float ms = asf(x[1][0]), ls = asf(x[1][2]);
    const float Mw = xr16_max(fmaxf(ms, dpp<ROR8>(ms)));
    const float e = __builtin_amdgcn_exp2f(ms - Mw);
    auto sa = __builtin_amdgcn_permlane32_swap(asu(e * lo16(x[0][0])), asu(e * lo16(x[0][2])), false, false);
    auto sb = __builtin_amdgcn_permlane32_swap(asu(e * hi16(x[0][0])), asu(e * hi16(x[0][2])), false, false);
    auto sc = __builtin_amdgcn_permlane16_swap(asu(asf(sa[0]) + asf(sa[1])), asu(asf(sb[0]) + asf(sb[1])), false, false);
    float v = asf(sc[0]) + asf(sc[1]);
    v += dpp<ROR8>(v);
    const float lw = strided_sum<8>(e * ls);
    if ((lane & 8) == 0) L[L_RED2 + w * 64 + (2 * (lane >> 5) + ((lane >> 4) & 1)) * 8 + (lane & 7)] = v;
    if (lane == 0) *(LAS f32x2 *)(L + L_RED2 + w * 64 + 32) = f32x2{Mw, lw};
    WG_BARRIER();
    f32x2 ml[4];
#pragma unroll
    for (int ww = 0; ww < 4; ++ww) ml[ww] = *(LAS f32x2 *)(L + L_RED2 + ww * 64 + 32);
    const float M = fmaxf(fmaxf(ml[0][0], ml[1][0]), fmaxf(ml[2][0], ml[3][0]));
    f32x4 oa = {0.f, 0.f, 0.f, 0.f}, ob = oa;
    float Lt = 0.f;
#pragma unroll
    for (int ww = 0; ww < 4; ++ww) {
      const float ew = __builtin_amdgcn_exp2f(ml[ww][0] - M);
      Lt = fmaf(ew, ml[ww][1], Lt);
      oa += ew * *(LAS f32x4 *)(L + L_RED2 + ww * 64 + g4 * 8);
      ob += ew * *(LAS f32x4 *)(L + L_RED2 + ww * 64 + g4 * 8 + 4);
    }
    const float rl = __builtin_amdgcn_rcpf(Lt);
    f32x4 c0 = {0.f, 0.f, 0.f, 0.f}, c1 = c0;
#pragma unroll
    for (int i = 0; i < 8; ++i) { const float o = (i < 4 ? oa[i & 3] : ob[i & 3]) * rl; gemv_step(cvtpk(o, o), mk0, mk1, wo[i], c0, c1); }
    publish512(rws, WS_PB, NPW, b, c0, c1, L + L_TR, w, lane, tag);
    KEEP(2, x);
    STAMP_AT(6);
  } else if (b < NPW + NPF) {
    const int pf = (b & 7) * 7 + ((b - NPW) >> 3);
    const rsrc_t rwg = __builtin_amdgcn_make_buffer_rsrc((char *)p.wg + pf * 64, 0, DM * FF * 2 - pf * 64, RS3);
    const rsrc_t rwu = __builtin_amdgcn_make_buffer_rsrc((char *)p.wu + pf * 64, 0, DM * FF * 2 - pf * 64, RS3);
    const rsrc_t rwd = __builtin_amdgcn_make_buffer_rsrc((char *)p.wd + (size_t)pf * 32768, 0, 32768, RS3);
    const int go = (128 * w + 4 * g4 + a4) * (FF * 2) + u4 * 16;
    v4u wg[8], wu[8], wd[8];
#pragma unroll
    for (int i = 0; i < 8; ++i) { wg[i] = ld16<WAUX>(rwg, go, i * 16 * FF * 2); wu[i] = ld16<WAUX>(rwu, go, i * 16 * FF * 2); }
#pragma unroll
    for (int i = 0; i < 8; ++i) wd[i] = ld16<WAUX>(rwd, foff, i * 4096);
    SB();
      v4u x[1];
      const int off[1] = {WS_HG + (b % REP) * 2048 + (32 * w + (lane & 31)) * 16};
      GATHER(1, off, x);
      if (lane < 32) *(LAS v2u *)((LAS unsigned *)L + L_Q + 64 * w + 2 * lane) = v2u{x[0][0], x[0][2]};
    WG_BARRIER();
    STAMP_AT(9);
    {
      const v4u hb = *(LAS v4u *)(L + L_Q + 4 * lane);
      const unsigned own = ((LAS unsigned *)L)[L_Q + 64 * w + lane];
      ((LAS unsigned *)L)[L_XN + w * 64 + lane] = ln_pair(own, ln_stats(hb));
      LDS_WAIT();
    }
    STAMP_AT(12);
    f32x4 g0 = {0.f, 0.f, 0.f, 0.f}, g1 = g0, u0 = g0, u1 = g0;
#pragma unroll
    for (int i = 0; i < 8; ++i) {
      const unsigned xv = ((LAS unsigned short *)(L + L_XN + w * 64))[16 * i + 4 * g4 + a4];
      const unsigned pp = xv | (xv << 16);
      gemv_step(pp, mk0, mk1, wg[i], g0, g1);
      gemv_step(pp, mk0, mk1, wu[i], u0, u1);
    }
    if (g4 == a4) {
      LAS float *gu = L + L_MRG + (w * 4 + g4) * 64 + 8 * u4;
      *(LAS f32x4 *)(gu) = g0; *(LAS f32x4 *)(gu + 4) = g1; *(LAS f32x4 *)(gu + 32) = u0; *(LAS f32x4 *)(gu + 36) = u1;
    }
    STAMP_AT(13);
    WG_BARRIER();
    {
      const int c = tid >> 3, part = tid & 7;
      LAS float *gu = L + L_MRG + part * 128 + c;
      float g = gu[0] + gu[64], u = gu[32] + gu[96];
      g += dpp<XOR1>(g); u += dpp<XOR1>(u); g += dpp<XOR2>(g); u += dpp<XOR2>(u); g += dpp<HMIR>(g); u += dpp<HMIR>(u);
      const float hv = rbf(g) * rbf(u);
      if (part == 0) ((LAS unsigned *)L)[L_HID + (c & 3) * 8 + (c >> 2)] = cvtpk(hv, hv);
    }
    WG_BARRIER();
    STAMP_AT(14);
    const v4u ha = *(LAS v4u *)(L + L_HID + g4 * 8), hc = *(LAS v4u *)(L + L_HID + g4 * 8 + 4);
    f32x4 c0 = {0.f, 0.f, 0.f, 0.f}, c1 = c0;
#pragma unroll
    for (int i = 0; i < 8; ++i) gemv_step(i < 4 ? ha[i & 3] : hc[i & 3], mk0, mk1, wd[i], c0, c1);
    publish512(rws, WS_PD, NPF, pf, c0, c1, L + L_TR, w, lane, tag);
    KEEP(1, x);
    STAMP_AT(10);
  } else if (b < NPW + NPF + 32) {
    const int j = b - (NPW + NPF);
    const v2u xv = __builtin_amdgcn_raw_buffer_load_b64(rx, (16 * j + 4 * (tid & 3)) * 2, 0, 0);
    const int off[2] = {WS_PB + j * (NPW * 64) + tid * 16, WS_PB + j * (NPW * 64) + 4096 + tid * 16};
    v4u x[2];
    GATHER(2, off, x);
    STAMP_AT(7);
    const f32x4 r = reduce_cols<2>(x, true, L + L_RED, w, lane);
    const unsigned h0 = cvtpk(lo16(xv[0]) + rbf(r[0]), hi16(xv[0]) + rbf(r[1])), h1 = cvtpk(lo16(xv[1]) + rbf(r[2]), hi16(xv[1]) + rbf(r[3]));
    if (tid < 4 * REP) st16(rws, WS_HG + (tid >> 2) * 2048 + (4 * j + (tid & 3)) * 16, v4u{h0, tag, h1, tag});
    KEEP(2, x);
    STAMP_AT(8);
  } else if (b < NPW + NPF + 64) {
    const int j = b - (NPW + NPF + 32);
    const bool live = tid < NPF * 4;
    const int off[2] = {WS_HG + (b % REP) * 2048 + (4 * j + (tid & 3)) * 16, WS_PD + j * (NPF * 64) + (live ? tid : tid - 128) * 16};
    v4u x[2];
    GATHER(2, off, x);
    const v4u y[1] = {x[1]};
    const f32x4 r = reduce_cols<1>(y, live, L + L_RED, w, lane);
    const v2u o = {cvtpk(lo16(x[0][0]) + rbf(r[0]), hi16(x[0][0]) + rbf(r[1])), cvtpk(lo16(x[0][2]) + rbf(r[2]), hi16(x[0][2]) + rbf(r[3]))};
    if (tid < 4) *(v2u *)((char *)p.out + (16 * j + 4 * tid) * 2) = o;
    if (j == 0 && tid == 0) __builtin_amdgcn_raw_buffer_store_b32(tag, rws, WS_EPOCH, 0, AUX_SC1);
    KEEP(2, x);
  }
  STAMP_AT(11);
  STAMP_FLUSH();
}
}

extern "C" void kernel_launch(void *const *d_in, const int *in_sizes, int n_in, void *d_out, int out_size, void *d_ws, size_t ws_size,
                              hipStream_t stream) {
  Params p;
  p.x = d_in[0]; p.k = d_in[1]; p.v = d_in[2]; p.wq = d_in[3]; p.wo = d_in[4];
  p.wg = d_in[6]; p.wu = d_in[7]; p.wd = d_in[8];
  p.out = d_out; p.ws = d_ws;
  (void)hipFuncSetAttribute((const void *)fused_decode_kernel, hipFuncAttributeMaxDynamicSharedMemorySize, LDS_BYTES);
  fused_decode_kernel<<<NWG, NTHR, LDS_BYTES, stream>>>(p);
}
```

```cpp
#include <hip/hip_runtime.h>
#include <stdint.h>

#define STAMP 0
#define REP 16
#define KAUX 2
#define VAUX 2
#define WAUX 2
#define PINARGS 1
#define ORD 1
#define HM 1
#define TAU 0
#define TOFF 500
#define TK 60
#define TPOLL 300
#define DREP 8
#define DEFER 0
#define PIPEPOLL 0
#define TOUCH_OUT 1
#define EARLYK 4
#define OUTAUX 0
#define QREP 4

namespace {
typedef unsigned v4u __attribute__((ext_vector_type(4)));
typedef unsigned v2u __attribute__((ext_vector_type(2)));
typedef float f32x4 __attribute__((ext_vector_type(4)));
typedef float f32x2 __attribute__((ext_vector_type(2)));
typedef short s16x4 __attribute__((ext_vector_type(4)));
typedef __bf16 bf16x8 __attribute__((ext_vector_type(8)));
typedef __bf16 bf16x2 __attribute__((ext_vector_type(2)));
typedef __amdgpu_buffer_rsrc_t rsrc_t;
#define LAS __attribute__((address_space(3)))

constexpr int DM = 512, SK = 65536, DH = 128, FF = 1792;
constexpr int NWG = 256, NTHR = 256;
constexpr int NPW = 128, NPF = 56;
constexpr int AUX_NT = 2, AUX_SC1 = 16;
constexpr int RS3 = 0x00020000;
constexpr int WS_EPOCH = 0;
constexpr int WS_ML = 0x1000;
constexpr int WS_HG = 0x11000;
constexpr int WS_PD = 0x30000;
constexpr int WS_PB = 0x50000;
constexpr int WS_OP = 0x90000;
constexpr int WS_QG = 0x110000;
constexpr int WS_USED = 0x190000;
static_assert(WS_HG + 32 * 2048 <= WS_PD && WS_PD + 32 * 56 * 64 <= WS_PB && WS_PB + 32 * 128 * 64 <= WS_OP && WS_OP + 128 * 32 * 128 <= WS_QG, "");
constexpr int WS_STAMP = 8 << 20;
constexpr unsigned SPIN_CAP = 1u << 14;
constexpr float LOG2E = 1.4426950408889634f;
constexpr float SCALE = 0.08838834764831845f;

struct Params {
  const void *x, *k, *v, *wq, *wo, *wg, *wu, *wd;
  void *out, *ws;
};

__device__ __forceinline__ float asf(unsigned u) { return __builtin_bit_cast(float, u); }
__device__ __forceinline__ unsigned asu(float f) { return __builtin_bit_cast(unsigned, f); }
template <int C> __device__ __forceinline__ float dpp(float x) {
  return __builtin_bit_cast(float, __builtin_amdgcn_mov_dpp(__builtin_bit_cast(int, x), C, 0xf, 0xf, true));
}
constexpr int XOR1 = 0xB1, XOR2 = 0x4E, HMIR = 0x141, MIR = 0x140, ROR4 = 0x124, ROR8 = 0x128;
__device__ __forceinline__ unsigned cvtpk(float lo, float hi) {
  f32x2 v = {lo, hi};
  bf16x2 r = __builtin_convertvector(v, bf16x2);
  return __builtin_bit_cast(unsigned, r);
}
__device__ __forceinline__ float lo16(unsigned u) { return asf(u << 16); }
__device__ __forceinline__ float hi16(unsigned u) { return asf(u & 0xffff0000u); }
__device__ __forceinline__ float rbf(float x) { return lo16(cvtpk(x, x)); }
__device__ __forceinline__ float xr16_max(float x) {
  auto s = __builtin_amdgcn_permlane16_swap(asu(x), asu(x), false, false);
  x = fmaxf(asf(s[0]), asf(s[1]));
  auto t = __builtin_amdgcn_permlane32_swap(asu(x), asu(x), false, false);
  return fmaxf(asf(t[0]), asf(t[1]));
}
__device__ __forceinline__ float xr16_sum(float x) {
  auto s = __builtin_amdgcn_permlane16_swap(asu(x), asu(x), false, false);
  x = asf(s[0]) + asf(s[1]);
  auto t = __builtin_amdgcn_permlane32_swap(asu(x), asu(x), false, false);
  return asf(t[0]) + asf(t[1]);
}
__device__ __forceinline__ float row_sum(float x) {
  x += dpp<XOR1>(x); x += dpp<XOR2>(x); x += dpp<HMIR>(x); x += dpp<MIR>(x); return x;
}
__device__ __forceinline__ float row_max(float x) {
  x = fmaxf(x, dpp<XOR1>(x)); x = fmaxf(x, dpp<XOR2>(x)); x = fmaxf(x, dpp<HMIR>(x)); x = fmaxf(x, dpp<MIR>(x)); return x;
}
__device__ __forceinline__ float wave_sum(float x) { return xr16_sum(row_sum(x)); }
__device__ __forceinline__ f32x4 mfma32(v4u a, v4u b, f32x4 c) {
  return __builtin_amdgcn_mfma_f32_16x16x32_bf16(__builtin_bit_cast(bf16x8, a), __builtin_bit_cast(bf16x8, b), c, 0, 0, 0);
}
__device__ __forceinline__ f32x4 mfma16(v2u a, unsigned b0, unsigned b1, f32x4 c) {
  v2u b = {b0, b1};
  return __builtin_amdgcn_mfma_f32_16x16x16bf16_1k(__builtin_bit_cast(s16x4, a), __builtin_bit_cast(s16x4, b), c, 0, 0, 0);
}
template <int AUX> __device__ __forceinline__ v4u ld16(rsrc_t r, int voff, int soff) {
  return __builtin_amdgcn_raw_buffer_load_b128(r, voff, soff, AUX);
}
__device__ __forceinline__ void st16(rsrc_t r, int voff, v4u v) { __builtin_amdgcn_raw_buffer_store_b128(v, r, voff, 0, AUX_SC1); }
__device__ __forceinline__ void st8(rsrc_t r, int voff, v2u v) { __builtin_amdgcn_raw_buffer_store_b64(v, r, voff, 0, AUX_SC1); }
#define WG_BARRIER() asm volatile("s_waitcnt lgkmcnt(0)\n\ts_barrier" ::: "memory")
#define LDS_WAIT() asm volatile("s_waitcnt lgkmcnt(0)" ::: "memory")
#define SB() __builtin_amdgcn_sched_barrier(0)

struct LnStat { float mb, rden; float den; };
__device__ __forceinline__ LnStat ln_stats(v4u p) {
  float s = 0.f, q = 0.f;
#pragma unroll
  for (int i = 0; i < 4; ++i) {
    const float a = lo16(p[i]), b = hi16(p[i]);
    s += a + b;
    q = fmaf(a, a, fmaf(b, b, q));
  }
  s += dpp<XOR1>(s); q += dpp<XOR1>(q); s += dpp<XOR2>(s); q += dpp<XOR2>(q);
  s += dpp<HMIR>(s); q += dpp<HMIR>(q); s += dpp<MIR>(s); q += dpp<MIR>(q);
  auto t = __builtin_amdgcn_permlane16_swap(asu(s), asu(q), false, false);
  float y = asf(t[0]) + asf(t[1]);
  auto u = __builtin_amdgcn_permlane32_swap(asu(y), asu(y), false, false);
  y = asf(u[0]) + asf(u[1]);
  auto v = __builtin_amdgcn_permlane16_swap(asu(y), asu(y), false, false);
  const float mean = asf(v[0]) * (1.f / DM);
  const float var = fmaf(-mean, mean, asf(v[1]) * (1.f / DM));
  LnStat r; r.mb = rbf(mean); r.den = rbf(__builtin_amdgcn_sqrtf(rbf(rbf(var) + 1e-5f))); r.rden = __builtin_amdgcn_rcpf(r.den);
  return r;
}
__device__ __forceinline__ unsigned ln_pair(unsigned p, LnStat st) { return cvtpk(rbf(lo16(p) - st.mb) * st.rden, rbf(hi16(p) - st.mb) * st.rden); }
struct Tile { v4u k[8], v[8]; };

template <bool REFILL>
__device__ __forceinline__ void tile_step(Tile &t, v4u q, rsrc_t rk, rsrc_t rv, int koff, int voff, int snext, unsigned mk0, unsigned mk1,
                                          float &m2, float &lsum, f32x4 &acc0, f32x4 &acc1) {
  float s[8];
  const f32x4 z = {0.f, 0.f, 0.f, 0.f};
  f32x4 d[8];
#pragma unroll
  for (int g = 0; g < 8; ++g) {
    d[g] = mfma32(t.k[g], q, z);
    if (REFILL) t.k[g] = ld16<KAUX>(rk, koff, snext + g * 1024);
    SB();
  }
#pragma unroll
  for (int g = 0; g < 8; ++g) {
    s[g] = ((dpp<0x00>(d[g][0]) + dpp<0x55>(d[g][1])) + dpp<0xAA>(d[g][2])) + dpp<0xFF>(d[g][3]);
    asm("" : "+v"(s[g]));
  }
#pragma unroll
  for (int g = 0; g < 8; g += 2) {
    const unsigned a = cvtpk(s[g], s[g + 1]);
    const unsigned b = cvtpk(lo16(a) * SCALE, hi16(a) * SCALE);
    s[g] = lo16(b); s[g + 1] = hi16(b);
  }
  float mx = fmaxf(fmaxf(fmaxf(s[0], s[1]), fmaxf(s[2], s[3])), fmaxf(fmaxf(s[4], s[5]), fmaxf(s[6], s[7])));
  mx = xr16_max(mx);
  const float mn = fmaxf(m2, mx * LOG2E);
  const float alpha = __builtin_amdgcn_exp2f(m2 - mn);
  m2 = mn;
  lsum *= alpha;
  int ai = __builtin_bit_cast(int, alpha), ar;
  ar = __builtin_amdgcn_update_dpp(ai, ai, 0x150, 0x1, 0xf, false);
  ar = __builtin_amdgcn_update_dpp(ar, ai, 0x154, 0x2, 0xf, false);
  ar = __builtin_amdgcn_update_dpp(ar, ai, 0x158, 0x4, 0xf, false);
  ar = __builtin_amdgcn_update_dpp(ar, ai, 0x15c, 0x8, 0xf, false);
  const float al = __builtin_bit_cast(float, ar);
  acc0 *= al; acc1 *= al;
#pragma unroll
  for (int g = 0; g < 8; ++g) {
    const float p = __builtin_amdgcn_exp2f(fmaf(s[g], LOG2E, -mn));
    lsum += p;
    const unsigned pp = cvtpk(p, p);
    const v2u a = {pp & mk0, pp & mk1};
    acc0 = mfma16(a, t.v[g][0], t.v[g][1], acc0);
    acc1 = mfma16(a, t.v[g][2], t.v[g][3], acc1);
    if (REFILL) t.v[g] = ld16<VAUX>(rv, voff, snext + g * 1024);
    SB();
  }
}

template <int N>
__device__ __forceinline__ bool tags_ok(const v4u (&x)[N], unsigned tag) {
  bool ok = true;
#pragma unroll
  for (int i = 0; i < N; ++i) ok = ok & (x[i][1] == tag) & (x[i][3] == tag);
  return __all(ok);
}
template <int N>
__device__ __forceinline__ void sweep(rsrc_t rs, const int (&off)[N], v4u (&x)[N]) {
  asm volatile("" ::: "memory");
#pragma unroll
  for (int i = 0; i < N; ++i) x[i] = ld16<AUX_SC1>(rs, off[i], 0);
}
template <int N>
__device__ __forceinline__ void gather(rsrc_t rs, const int (&off)[N], unsigned tag, v4u (&x)[N]) {
  for (unsigned spins = 0;; ++spins) {
    sweep<N>(rs, off, x);
    if (tags_ok<N>(x, tag) || spins > SPIN_CAP) break;
    __builtin_amdgcn_s_sleep(1);
  }
}
#define GATHER(N, off, x) gather<N>(rws, off, tag, x)
#define KEEP(N, x) do { } while (0)
template <int S> __device__ __forceinline__ float strided_sum(float x) {
  if (S <= 1) x += dpp<XOR1>(x);
  if (S <= 2) x += dpp<XOR2>(x);
  if (S <= 8) x += dpp<ROR8>(x);
  if (S <= 4) x += dpp<ROR4>(x);
  return xr16_sum(x);
}
__device__ __forceinline__ void publish512(rsrc_t rs, int base, int nprod, int p, f32x4 a0, f32x4 a1, LAS float *tr, int w, int lane, unsigned tag) {
  LAS unsigned *t = (LAS unsigned *)tr + w * 64;
  if (lane < 16) *(LAS v4u *)(t + 4 * lane) = v4u{cvtpk(a0[0], a0[1]), cvtpk(a0[2], a0[3]), cvtpk(a1[0], a1[1]), cvtpk(a1[2], a1[3])};
  LDS_WAIT();
  const v2u y = *(LAS v2u *)(t + 2 * (lane & 31));
  if (lane < 32) st16(rs, base + ((8 * w + (lane >> 2)) * nprod + p) * 64 + (lane & 3) * 16, v4u{y[0], tag, y[1], tag});
}
template <int N>
__device__ __forceinline__ f32x4 reduce_cols(const v4u (&x)[N], bool last_live, LAS float *red, int w, int lane) {
  f32x4 s = {0.f, 0.f, 0.f, 0.f};
#pragma unroll
  for (int i = 0; i < N; ++i) {
    const f32x4 v = {lo16(x[i][0]), hi16(x[i][0]), lo16(x[i][2]), hi16(x[i][2])};
    if (i + 1 < N) s += v; else s += last_live ? v : f32x4{0.f, 0.f, 0.f, 0.f};
  }
  auto sa = __builtin_amdgcn_permlane32_swap(asu(s[0]), asu(s[2]), false, false);
  auto sb = __builtin_amdgcn_permlane32_swap(asu(s[1]), asu(s[3]), false, false);
  auto sc = __builtin_amdgcn_permlane16_swap(asu(asf(sa[0]) + asf(sa[1])), asu(asf(sb[0]) + asf(sb[1])), false, false);
  float v = asf(sc[0]) + asf(sc[1]);
  v += dpp<ROR8>(v); v += dpp<ROR4>(v);
  if ((lane & 12) == 0) red[w * 64 + 4 * (lane & 3) + 2 * (lane >> 5) + ((lane >> 4) & 1)] = v;
  WG_BARRIER();
  f32x4 r = {0.f, 0.f, 0.f, 0.f};
#pragma unroll
  for (int ww = 0; ww < 4; ++ww) r += *(LAS f32x4 *)(red + ww * 64 + 4 * (lane & 3));
  return r;
}
__device__ __forceinline__ void gemv_step(unsigned pp, unsigned mk0, unsigned mk1, v4u f, f32x4 &c0, f32x4 &c1) {
  const v2u a = {pp & mk0, pp & mk1};
  c0 = mfma16(a, f[0], f[1], c0);
  c1 = mfma16(a, f[2], f[3], c1);
}

#define STAMP_AT(i) do { } while (0)
#define STAMP_FLUSH() do { } while (0)

constexpr int L_MRG = 0;
constexpr int L_ML = L_MRG + 2048;
constexpr int L_RED = L_ML + 32;
constexpr int L_RED2 = L_RED + 256;
constexpr int L_TR = L_RED2 + 256;
constexpr int L_Q = L_TR + 512;
constexpr int L_XN = L_Q + 256;
constexpr int L_HID = L_XN + 1024;
constexpr int L_END = L_HID + 32;
constexpr int LDS_BYTES = 84 * 1024;

__global__ void __launch_bounds__(NTHR, 2) fused_decode_kernel(Params p) {
  extern __shared__ __attribute__((aligned(16))) float smem[];
  LAS float *L = (LAS float *)smem;
  const int tid = threadIdx.x, lane = tid & 63, w = __builtin_amdgcn_readfirstlane(tid >> 6), b = blockIdx.x;
  asm volatile("" :: "s"(p.x), "s"(p.k), "s"(p.v), "s"(p.wq), "s"(p.wo), "s"(p.wg), "s"(p.wu), "s"(p.wd), "s"(p.out), "s"(p.ws));
  STAMP_AT(0);
  const unsigned tstart = (unsigned)__builtin_amdgcn_s_memrealtime();
  const int kh = HM ? b >> 5 : b & 7, sp = HM ? b & 31 : b >> 3;
  const rsrc_t rws = __builtin_amdgcn_make_buffer_rsrc(p.ws, 0, WS_USED, RS3);
  const rsrc_t rk = __builtin_amdgcn_make_buffer_rsrc((char *)p.k + (size_t)kh * SK * 256, 0, SK * 256, RS3);
  const rsrc_t rv = __builtin_amdgcn_make_buffer_rsrc((char *)p.v + (size_t)kh * SK * 256, 0, SK * 256, RS3);
  const int tbase = ORD == 1 ? sp * 524288 + w * 8192 : ORD == 2 ? (sp * 4 + w) * 8192 : (w * 32 + sp) * 8192;
  constexpr int TSTR = ORD == 1 ? 4 * 8192 : 128 * 8192;
  const rsrc_t rx = __builtin_amdgcn_make_buffer_rsrc((void *)p.x, 0, DM * 2, RS3);
  const unsigned epoch = *(const __attribute__((address_space(4))) unsigned *)((const char *)p.ws + WS_EPOCH);
  const bool hasq = (b & 7) < 4;
  const int pq = (b >> 3) * 4 + (b & 3), hq = pq & 31, rb = pq >> 5;
  const int g4 = lane >> 4, a4 = (lane >> 2) & 3, u4 = lane & 3;
  const int koff = a4 * 256 + (4 * g4 + u4) * 16, voff = lane * 16;
  const unsigned mk0 = u4 == 0 ? 0xFFFFu : u4 == 1 ? 0xFFFF0000u : 0u;
  const unsigned mk1 = u4 == 2 ? 0xFFFFu : u4 == 3 ? 0xFFFF0000u : 0u;
  v4u wqf[8];
  unsigned xq[8];
  Tile ta, tb;
  const rsrc_t rxq = __builtin_amdgcn_make_buffer_rsrc((void *)p.x, 0, hasq ? DM * 2 : 0, RS3);
  const rsrc_t rwq = __builtin_amdgcn_make_buffer_rsrc((void *)p.wq, 0, hasq ? DM * 4096 * 2 : 0, RS3);
  const v4u xr = ld16<0>(rxq, lane * 16, 0);
  const unsigned xown = __builtin_amdgcn_raw_buffer_load_b32(rxq, (64 * rb + 16 * w + (lane & 15)) * 4, 0, 0);
  const int wqoff = (128 * rb + 32 * w + g4) * 8192 + hq * 256 + (lane & 15) * 16;
#pragma unroll
  for (int i = 0; i < 8; ++i) wqf[i] = ld16<WAUX>(rwq, wqoff, i * 4 * 8192);
#pragma unroll
  for (int g = 0; g < EARLYK; ++g) ta.k[g] = ld16<KAUX>(rk, koff, tbase + g * 1024);
  SB();
  if (hasq) {
    ((LAS unsigned *)L)[L_XN + w * 64 + lane] = ln_pair(xown, ln_stats(xr));
    LDS_WAIT();
#pragma unroll
    for (int i = 0; i < 8; ++i) xq[i] = ((LAS unsigned short *)(L + L_XN + w * 64))[4 * i + g4];
    STAMP_AT(12);
    f32x4 c0 = {0.f, 0.f, 0.f, 0.f}, c1 = c0;
#pragma unroll
    for (int i = 0; i < 8; ++i) {
      gemv_step(xq[i] | (xq[i] << 16), mk0, mk1, wqf[i], c0, c1);
    }
    if (lane < 16) {
      *(LAS f32x4 *)(L + L_TR + w * 128 + 8 * lane) = c0;
      *(LAS f32x4 *)(L + L_TR + w * 128 + 8 * lane + 4) = c1;
    }
    STAMP_AT(13);
    WG_BARRIER();
    if (w < QREP) {
      f32x2 y = {0.f, 0.f};
#pragma unroll
      for (int ww = 0; ww < 4; ++ww) y += *(LAS f32x2 *)(L + L_TR + ww * 128 + 2 * lane);
      st16(rws, WS_QG + w * 131072 + ((hq * 4 + rb) * 128 + 2 * lane) * 8, v4u{asu(y[0]), epoch + 1u, asu(y[1]), epoch + 1u});
    }
  }
  STAMP_AT(1);
#pragma unroll
  for (int g = EARLYK; g < 8; ++g) ta.k[g] = ld16<KAUX>(rk, koff, tbase + g * 1024);
#pragma unroll
  for (int g = 0; g < 8; ++g) ta.v[g] = ld16<VAUX>(rv, voff, tbase + g * 1024);
  SB();
  unsigned tag = epoch + 1u;
  asm volatile("" : "+s"(tag));
  while ((unsigned)__builtin_amdgcn_s_memrealtime() - tstart < (unsigned)TPOLL) __builtin_amdgcn_s_sleep(1);
  v4u q;
  {
    const int qoff = WS_QG + (sp & (QREP - 1)) * 131072 + (4 * kh + w) * 4096 + lane * 16;
    unsigned touch;
    {
      const unsigned long long ta_ = (unsigned long long)(b >= NPW + NPF + 32 ? (const void *)p.out : p.x);
      const v4u td = {(unsigned)ta_, (unsigned)(ta_ >> 32) & 0xffffu, (TOUCH_OUT && b >= NPW + NPF && b < NPW + NPF + 64) ? 4u : 0u, (unsigned)RS3};
      asm volatile("buffer_load_dword %0, off, %1, 0" : "=v"(touch) : "s"(td));
    }
    v4u x[4];
#pragma unroll
    for (int i = 0; i < 4; ++i) x[i] = ld16<AUX_SC1>(rws, qoff, i * 1024);
    SB();
    asm volatile("s_barrier" ::: "memory");
    SB();
#pragma unroll
    for (int g = 0; g < 8; ++g) tb.k[g] = ld16<KAUX>(rk, koff, tbase + TSTR + g * 1024);
#pragma unroll
    for (int g = 0; g < 8; ++g) tb.v[g] = ld16<VAUX>(rv, voff, tbase + TSTR + g * 1024);
    SB();
    bool ok = true;
#pragma unroll
    for (int i = 0; i < 4; ++i) ok = ok & (x[i][1] == tag) & (x[i][3] == tag);
    asm volatile("" :: "v"(touch), "s"((int)__all(ok)));
    if (__builtin_expect(!__all(ok), 0)) {
      for (unsigned spins = 0; spins < SPIN_CAP; ++spins) {
        bool ok2 = true;
        asm volatile("" ::: "memory");
#pragma unroll
        for (int i = 0; i < 4; ++i) x[i] = ld16<AUX_SC1>(rws, qoff, i * 1024);
#pragma unroll
        for (int i = 0; i < 4; ++i) ok2 = ok2 & (x[i][1] == tag) & (x[i][3] == tag);
        if (__all(ok2)) break;
      }
      __builtin_amdgcn_s_waitcnt(0x0F70);
    }
    ((LAS unsigned *)L)[L_Q + w * 64 + lane] = cvtpk((asf(x[0][0]) + asf(x[1][0])) + (asf(x[2][0]) + asf(x[3][0])),
                                                     (asf(x[0][2]) + asf(x[1][2])) + (asf(x[2][2]) + asf(x[3][2])));
    WG_BARRIER();
    q = *(LAS v4u *)(L + L_Q + a4 * 64 + (4 * g4 + u4) * 4);
  }
  STAMP_AT(2);
  float m2 = -3.0e38f, lsum = 0.f;
  f32x4 acc0 = {0.f, 0.f, 0.f, 0.f}, acc1 = acc0;
  for (int it = 0; it < 7; ++it) {
    const int s0 = tbase + (2 * it + 2) * TSTR;
    if (TAU) { while ((unsigned)__builtin_amdgcn_s_memrealtime() - tstart < (unsigned)(TOFF + 2 * it * TAU)) __builtin_amdgcn_s_sleep(1); }
    tile_step<true>(ta, q, rk, rv, koff, voff, s0, mk0, mk1, m2, lsum, acc0, acc1);
    if (TAU) { while ((unsigned)__builtin_amdgcn_s_memrealtime() - tstart < (unsigned)(TOFF + (2 * it + 1) * TAU)) __builtin_amdgcn_s_sleep(1); }
    tile_step<true>(tb, q, rk, rv, koff, voff, s0 + TSTR, mk0, mk1, m2, lsum, acc0, acc1);
  }
  const bool role_wo = b < NPW;
  const rsrc_t rwo = __builtin_amdgcn_make_buffer_rsrc((char *)p.wo + (size_t)(role_wo ? b : 0) * 32768, 0, role_wo ? 32768 : 0, RS3);
  const int foff = g4 * 1024 + w * 256 + (lane & 15) * 16;
  v4u wo[8];
#pragma unroll
  for (int i = 0; i < 8; ++i) wo[i] = ld16<WAUX>(rwo, foff, i * 4096);
  SB();
  tile_step<false>(ta, q, rk, rv, koff, voff, 0, mk0, mk1, m2, lsum, acc0, acc1);
  tile_step<false>(tb, q, rk, rv, koff, voff, 0, mk0, mk1, m2, lsum, acc0, acc1);
  STAMP_AT(3);
  {
    const float lw = xr16_sum(lsum);
    *(LAS f32x4 *)(L + L_MRG + w * 512 + g4 * 128 + 8 * (lane & 15)) = acc0;
    *(LAS f32x4 *)(L + L_MRG + w * 512 + g4 * 128 + 8 * (lane & 15) + 4) = acc1;
    if (lane < 16 && u4 == 0) { f32x2 y = {m2, lw}; *(LAS f32x2 *)(L + L_ML + (w * 4 + a4) * 2) = y; }
    WG_BARRIER();
    f32x2 ml[4];
#pragma unroll
    for (int ww = 0; ww < 4; ++ww) ml[ww] = *(LAS f32x2 *)(L + L_ML + (ww * 4 + w) * 2);
    const float M = fmaxf(fmaxf(ml[0][0], ml[1][0]), fmaxf(ml[2][0], ml[3][0]));
    float Ls = 0.f; f32x2 o = {0.f, 0.f};
#pragma unroll
    for (int ww = 0; ww < 4; ++ww) {
      const float e = __builtin_amdgcn_exp2f(ml[ww][0] - M);
      Ls = fmaf(e, ml[ww][1], Ls);
      o += e * *(LAS f32x2 *)(L + L_MRG + ww * 512 + w * 128 + 2 * lane);
    }
    const int c4 = (4 * kh + w) * 4;
    st8(rws, WS_OP + ((c4 + g4) * 32 + sp) * 128 + (lane & 15) * 8, v2u{cvtpk(o[0], o[1]), tag});
    if (lane < 4) st16(rws, WS_ML + ((c4 + lane) * 32 + sp) * 16, v4u{asu(M), tag, asu(Ls), tag});
  }
  STAMP_AT(4);
  if (role_wo) {
    const int off[2] = {WS_OP + b * 4096 + tid * 16, WS_ML + (b * 32 + (tid >> 3)) * 16};
    v4u x[2];
    GATHER(2, off, x);
    STAMP_AT(5);
    const float ms = asf(x[1][0]), ls = asf(x[1][2]);
    const float Mw = xr16_max(fmaxf(ms, dpp<ROR8>(ms)));
    const float e = __builtin_amdgcn_exp2f(ms - Mw);
    auto sa = __builtin_amdgcn_permlane32_swap(asu(e * lo16(x[0][0])), asu(e * lo16(x[0][2])), false, false);
    auto sb = __builtin_amdgcn_permlane32_swap(asu(e * hi16(x[0][0])), asu(e * hi16(x[0][2])), false, false);
    auto sc = __builtin_amdgcn_permlane16_swap(asu(asf(sa[0]) + asf(sa[1])), asu(asf(sb[0]) + asf(sb[1])), false, false);
    float v = asf(sc[0]) + asf(sc[1]);
    v += dpp<ROR8>(v);
    const float lw = strided_sum<8>(e * ls);
    if ((lane & 8) == 0) L[L_RED2 + w * 64 + (2 * (lane >> 5) + ((lane >> 4) & 1)) * 8 + (lane & 7)] = v;
    if (lane == 0) *(LAS f32x2 *)(L + L_RED2 + w * 64 + 32) = f32x2{Mw, lw};
    WG_BARRIER();
    f32x2 ml[4];
#pragma unroll
    for (int ww = 0; ww < 4; ++ww) ml[ww] = *(LAS f32x2 *)(L + L_RED2 + ww * 64 + 32);
    const float M = fmaxf(fmaxf(ml[0][0], ml[1][0]), fmaxf(ml[2][0], ml[3][0]));
    f32x4 oa = {0.f, 0.f, 0.f, 0.f}, ob = oa;
    float Lt = 0.f;
#pragma unroll
    for (int ww = 0; ww < 4; ++ww) {
      const float ew = __builtin_amdgcn_exp2f(ml[ww][0] - M);
      Lt = fmaf(ew, ml[ww][1], Lt);
      oa += ew * *(LAS f32x4 *)(L + L_RED2 + ww * 64 + g4 * 8);
      ob += ew * *(LAS f32x4 *)(L + L_RED2 + ww * 64 + g4 * 8 + 4);
    }
    const float rl = __builtin_amdgcn_rcpf(Lt);
    f32x4 c0 = {0.f, 0.f, 0.f, 0.f}, c1 = c0;
#pragma unroll
    for (int i = 0; i < 8; ++i) { const float o = (i < 4 ? oa[i & 3] : ob[i & 3]) * rl; gemv_step(cvtpk(o, o), mk0, mk1, wo[i], c0, c1); }
    publish512(rws, WS_PB, NPW, b, c0, c1, L + L_TR, w, lane, tag);
    KEEP(2, x);
    STAMP_AT(6);
  } else if (b < NPW + NPF) {
    const int pf = (b & 7) * 7 + ((b - NPW) >> 3);
    const rsrc_t rwg = __builtin_amdgcn_make_buffer_rsrc((char *)p.wg + pf * 64, 0, DM * FF * 2 - pf * 64, RS3);
    const rsrc_t rwu = __builtin_amdgcn_make_buffer_rsrc((char *)p.wu + pf * 64, 0, DM * FF * 2 - pf * 64, RS3);
    const rsrc_t rwd = __builtin_amdgcn_make_buffer_rsrc((char *)p.wd + (size_t)pf * 32768, 0, 32768, RS3);
    const int go = (128 * w + 4 * g4 + a4) * (FF * 2) + u4 * 16;
    v4u wg[8], wu[8], wd[8];
#pragma unroll
    for (int i = 0; i < 8; ++i) { wg[i] = ld16<WAUX>(rwg, go, i * 16 * FF * 2); wu[i] = ld16<WAUX>(rwu, go, i * 16 * FF * 2); }
#pragma unroll
    for (int i = 0; i < 8; ++i) wd[i] = ld16<WAUX>(rwd, foff, i * 4096);
    SB();
      v4u x[1];
      const int off[1] = {WS_HG + (b % REP) * 2048 + (32 * w + (lane & 31)) * 16};
      GATHER(1, off, x);
      if (lane < 32) *(LAS v2u *)((LAS unsigned *)L + L_Q + 64 * w + 2 * lane) = v2u{x[0][0], x[0][2]};
    WG_BARRIER();
    STAMP_AT(9);
    {
      const v4u hb = *(LAS v4u *)(L + L_Q + 4 * lane);
      const unsigned own = ((LAS unsigned *)L)[L_Q + 64 * w + lane];
      ((LAS unsigned *)L)[L_XN + w * 64 + lane] = ln_pair(own, ln_stats(hb));
      LDS_WAIT();
    }
    STAMP_AT(12);
    f32x4 g0 = {0.f, 0.f, 0.f, 0.f}, g1 = g0, u0 = g0, u1 = g0;
#pragma unroll
    for (int i = 0; i < 8; ++i) {
      const unsigned xv = ((LAS unsigned short *)(L + L_XN + w * 64))[16 * i + 4 * g4 + a4];
      const unsigned pp = xv | (xv << 16);
      gemv_step(pp, mk0, mk1, wg[i], g0, g1);
      gemv_step(pp, mk0, mk1, wu[i], u0, u1);
    }
    if (g4 == a4) {
      LAS float *gu = L + L_MRG + (w * 4 + g4) * 64 + 8 * u4;
      *(LAS f32x4 *)(gu) = g0; *(LAS f32x4 *)(gu + 4) = g1; *(LAS f32x4 *)(gu + 32) = u0; *(LAS f32x4 *)(gu + 36) = u1;
    }
    STAMP_AT(13);
    WG_BARRIER();
    {
      const int c = tid >> 3, part = tid & 7;
      LAS float *gu = L + L_MRG + part * 128 + c;
      float g = gu[0] + gu[64], u = gu[32] + gu[96];
      g += dpp<XOR1>(g); u += dpp<XOR1>(u); g += dpp<XOR2>(g); u += dpp<XOR2>(u); g += dpp<HMIR>(g); u += dpp<HMIR>(u);
      const float hv = rbf(g) * rbf(u);
      if (part == 0) ((LAS unsigned *)L)[L_HID + (c & 3) * 8 + (c >> 2)] = cvtpk(hv, hv);
    }
    WG_BARRIER();
    STAMP_AT(14);
    const v4u ha = *(LAS v4u *)(L + L_HID + g4 * 8), hc = *(LAS v4u *)(L + L_HID + g4 * 8 + 4);
    f32x4 c0 = {0.f, 0.f, 0.f, 0.f}, c1 = c0;
#pragma unroll
    for (int i = 0; i < 8; ++i) gemv_step(i < 4 ? ha[i & 3] : hc[i & 3], mk0, mk1, wd[i], c0, c1);
    publish512(rws, WS_PD, NPF, pf, c0, c1, L + L_TR, w, lane, tag);
    KEEP(1, x);
    STAMP_AT(10);
  } else if (b < NPW + NPF + 32) {
    const int j = b - (NPW + NPF);
    const v2u xv = __builtin_amdgcn_raw_buffer_load_b64(rx, (16 * j + 4 * (tid & 3)) * 2, 0, 0);
    const int off[2] = {WS_PB + j * (NPW * 64) + tid * 16, WS_PB + j * (NPW * 64) + 4096 + tid * 16};
    v4u x[2];
    GATHER(2, off, x);
    STAMP_AT(7);
    const f32x4 r = reduce_cols<2>(x, true, L + L_RED, w, lane);
    const unsigned h0 = cvtpk(lo16(xv[0]) + rbf(r[0]), hi16(xv[0]) + rbf(r[1])), h1 = cvtpk(lo16(xv[1]) + rbf(r[2]), hi16(xv[1]) + rbf(r[3]));
    if (tid < 4 * REP) st16(rws, WS_HG + (tid >> 2) * 2048 + (4 * j + (tid & 3)) * 16, v4u{h0, tag, h1, tag});
    KEEP(2, x);
    STAMP_AT(8);
  } else if (b < NPW + NPF + 64) {
    const int j = b - (NPW + NPF + 32);
    const bool live = tid < NPF * 4;
    const int off[2] = {WS_HG + (b % REP) * 2048 + (4 * j + (tid & 3)) * 16, WS_PD + j * (NPF * 64) + (live ? tid : tid - 128) * 16};
    v4u x[2];
    GATHER(2, off, x);
    const v4u y[1] = {x[1]};
    const f32x4 r = reduce_cols<1>(y, live, L + L_RED, w, lane);
    const v2u o = {cvtpk(lo16(x[0][0]) + rbf(r[0]), hi16(x[0][0]) + rbf(r[1])), cvtpk(lo16(x[0][2]) + rbf(r[2]), hi16(x[0][2]) + rbf(r[3]))};
    const rsrc_t rout = __builtin_amdgcn_make_buffer_rsrc(p.out, 0, DM * 2, RS3);
    if (tid < 4) __builtin_amdgcn_raw_buffer_store_b64(o, rout, (16 * j + 4 * tid) * 2, 0, OUTAUX);
    if (j == 0 && tid == 0) __builtin_amdgcn_raw_buffer_store_b32(tag, rws, WS_EPOCH, 0, AUX_SC1);
    KEEP(2, x);
  }
  STAMP_AT(11);
  STAMP_FLUSH();
}
}

extern "C" void kernel_launch(void *const *d_in, const int *in_sizes, int n_in, void *d_out, int out_size, void *d_ws, size_t ws_size,
                              hipStream_t stream) {
  Params p;
  p.x = d_in[0]; p.k = d_in[1]; p.v = d_in[2]; p.wq = d_in[3]; p.wo = d_in[4];
  p.wg = d_in[6]; p.wu = d_in[7]; p.wd = d_in[8];
  p.out = d_out; p.ws = d_ws;
  (void)hipFuncSetAttribute((const void *)fused_decode_kernel, hipFuncAttributeMaxDynamicSharedMemorySize, LDS_BYTES);
  fused_decode_kernel<<<NWG, NTHR, LDS_BYTES, stream>>>(p);
}
```

```cpp
#include <hip/hip_runtime.h>
#include <stdint.h>

#define STAMP 0
#define REP 16
#define KAUX 2
#define VAUX 2
#define WAUX 2
#define PINARGS 1
#define ORD 1
#define HM 1
#define TAU 0
#define TOFF 500
#define TK 60
#define TPOLL 300
#define DREP 8
#define DEFER 0
#define PIPEPOLL 0
#define TOUCH_OUT 1
#define EARLYK 4
#define OUTAUX 0
#define RK1 3
#define RK2 0
#define QREP 4

namespace {
typedef unsigned v4u __attribute__((ext_vector_type(4)));
typedef unsigned v2u __attribute__((ext_vector_type(2)));
typedef float f32x4 __attribute__((ext_vector_type(4)));
typedef float f32x2 __attribute__((ext_vector_type(2)));
typedef short s16x4 __attribute__((ext_vector_type(4)));
typedef __bf16 bf16x8 __attribute__((ext_vector_type(8)));
typedef __bf16 bf16x2 __attribute__((ext_vector_type(2)));
typedef __amdgpu_buffer_rsrc_t rsrc_t;
#define LAS __attribute__((address_space(3)))

constexpr int DM = 512, SK = 65536, DH = 128, FF = 1792;
constexpr int NWG = 256, NTHR = 256;
constexpr int NPW = 128, NPF = 56;
constexpr int AUX_NT = 2, AUX_SC1 = 16;
constexpr int RS3 = 0x00020000;
constexpr int WS_EPOCH = 0;
constexpr int WS_ML = 0x1000;
constexpr int WS_HG = 0x11000;
constexpr int WS_PD = 0x30000;
constexpr int WS_PB = 0x50000;
constexpr int WS_OP = 0x90000;
constexpr int WS_QG = 0x110000;
constexpr int WS_USED = 0x190000;
static_assert(WS_HG + 32 * 2048 <= WS_PD && WS_PD + 32 * 56 * 64 <= WS_PB && WS_PB + 32 * 128 * 64 <= WS_OP && WS_OP + 128 * 32 * 128 <= WS_QG, "");
constexpr int WS_STAMP = 8 << 20;
constexpr unsigned SPIN_CAP = 1u << 14;
constexpr float LOG2E = 1.4426950408889634f;
constexpr float SCALE = 0.08838834764831845f;

struct Params {
  const void *x, *k, *v, *wq, *wo, *wg, *wu, *wd;
  void *out, *ws;
};

__device__ __forceinline__ float asf(unsigned u) { return __builtin_bit_cast(float, u); }
__device__ __forceinline__ unsigned asu(float f) { return __builtin_bit_cast(unsigned, f); }
template <int C> __device__ __forceinline__ float dpp(float x) {
  return __builtin_bit_cast(float, __builtin_amdgcn_mov_dpp(__builtin_bit_cast(int, x), C, 0xf, 0xf, true));
}
constexpr int XOR1 = 0xB1, XOR2 = 0x4E, HMIR = 0x141, MIR = 0x140, ROR4 = 0x124, ROR8 = 0x128;
__device__ __forceinline__ unsigned cvtpk(float lo, float hi) {
  f32x2 v = {lo, hi};
  bf16x2 r = __builtin_convertvector(v, bf16x2);
  return __builtin_bit_cast(unsigned, r);
}
__device__ __forceinline__ float lo16(unsigned u) { return asf(u << 16); }
__device__ __forceinline__ float hi16(unsigned u) { return asf(u & 0xffff0000u); }
__device__ __forceinline__ float rbf(float x) { return lo16(cvtpk(x, x)); }
__device__ __forceinline__ float xr16_max(float x) {
  auto s = __builtin_amdgcn_permlane16_swap(asu(x), asu(x), false, false);
  x = fmaxf(asf(s[0]), asf(s[1]));
  auto t = __builtin_amdgcn_permlane32_swap(asu(x), asu(x), false, false);
  return fmaxf(asf(t[0]), asf(t[1]));
}
__device__ __forceinline__ float xr16_sum(float x) {
  auto s = __builtin_amdgcn_permlane16_swap(asu(x), asu(x), false, false);
  x = asf(s[0]) + asf(s[1]);
  auto t = __builtin_amdgcn_permlane32_swap(asu(x), asu(x), false, false);
  return asf(t[0]) + asf(t[1]);
}
__device__ __forceinline__ float row_sum(float x) {
  x += dpp<XOR1>(x); x += dpp<XOR2>(x); x += dpp<HMIR>(x); x += dpp<MIR>(x); return x;
}
__device__ __forceinline__ float row_max(float x) {
  x = fmaxf(x, dpp<XOR1>(x)); x = fmaxf(x, dpp<XOR2>(x)); x = fmaxf(x, dpp<HMIR>(x)); x = fmaxf(x, dpp<MIR>(x)); return x;
}
__device__ __forceinline__ float wave_sum(float x) { return xr16_sum(row_sum(x)); }
__device__ __forceinline__ f32x4 mfma32(v4u a, v4u b, f32x4 c) {
  return __builtin_amdgcn_mfma_f32_16x16x32_bf16(__builtin_bit_cast(bf16x8, a), __builtin_bit_cast(bf16x8, b), c, 0, 0, 0);
}
__device__ __forceinline__ f32x4 mfma16(v2u a, unsigned b0, unsigned b1, f32x4 c) {
  v2u b = {b0, b1};
  return __builtin_amdgcn_mfma_f32_16x16x16bf16_1k(__builtin_bit_cast(s16x4, a), __builtin_bit_cast(s16x4, b), c, 0, 0, 0);
}
template <int AUX> __device__ __forceinline__ v4u ld16(rsrc_t r, int voff, int soff) {
  return __builtin_amdgcn_raw_buffer_load_b128(r, voff, soff, AUX);
}
__device__ __forceinline__ void st16(rsrc_t r, int voff, v4u v) { __builtin_amdgcn_raw_buffer_store_b128(v, r, voff, 0, AUX_SC1); }
__device__ __forceinline__ void st8(rsrc_t r, int voff, v2u v) { __builtin_amdgcn_raw_buffer_store_b64(v, r, voff, 0, AUX_SC1); }
#define WG_BARRIER() asm volatile("s_waitcnt lgkmcnt(0)\n\ts_barrier" ::: "memory")
#define LDS_WAIT() asm volatile("s_waitcnt lgkmcnt(0)" ::: "memory")
#define SB() __builtin_amdgcn_sched_barrier(0)

struct LnStat { float mb, rden; float den; };
__device__ __forceinline__ LnStat ln_stats(v4u p) {
  float s = 0.f, q = 0.f;
#pragma unroll
  for (int i = 0; i < 4; ++i) {
    const float a = lo16(p[i]), b = hi16(p[i]);
    s += a + b;
    q = fmaf(a, a, fmaf(b, b, q));
  }
  s += dpp<XOR1>(s); q += dpp<XOR1>(q); s += dpp<XOR2>(s); q += dpp<XOR2>(q);
  s += dpp<HMIR>(s); q += dpp<HMIR>(q); s += dpp<MIR>(s); q += dpp<MIR>(q);
  auto t = __builtin_amdgcn_permlane16_swap(asu(s), asu(q), false, false);
  float y = asf(t[0]) + asf(t[1]);
  auto u = __builtin_amdgcn_permlane32_swap(asu(y), asu(y), false, false);
  y = asf(u[0]) + asf(u[1]);
  auto v = __builtin_amdgcn_permlane16_swap(asu(y), asu(y), false, false);
  const float mean = asf(v[0]) * (1.f / DM);
  const float var = fmaf(-mean, mean, asf(v[1]) * (1.f / DM));
  LnStat r; r.mb = rbf(mean); r.den = rbf(__builtin_amdgcn_sqrtf(rbf(rbf(var) + 1e-5f))); r.rden = __builtin_amdgcn_rcpf(r.den);
  return r;
}
__device__ __forceinline__ unsigned ln_pair(unsigned p, LnStat st) { return cvtpk(rbf(lo16(p) - st.mb) * st.rden, rbf(hi16(p) - st.mb) * st.rden); }
struct Tile { v4u k[8], v[8]; };

template <bool REFILL>
__device__ __forceinline__ void tile_step(Tile &t, v4u q, rsrc_t rk, rsrc_t rv, int koff, int voff, int snext, unsigned mk0, unsigned mk1,
                                          float &m2, float &lsum, f32x4 &acc0, f32x4 &acc1) {
  float s[8];
  const f32x4 z = {0.f, 0.f, 0.f, 0.f};
  f32x4 d[8];
#pragma unroll
  for (int g = 0; g < 8; ++g) {
    d[g] = mfma32(t.k[g], q, z);
    if (REFILL) t.k[g] = ld16<KAUX>(rk, koff, snext + g * 1024);
    SB();
  }
#pragma unroll
  for (int g = 0; g < 8; ++g) {
    s[g] = ((dpp<0x00>(d[g][0]) + dpp<0x55>(d[g][1])) + dpp<0xAA>(d[g][2])) + dpp<0xFF>(d[g][3]);
    asm("" : "+v"(s[g]));
  }
#pragma unroll
  for (int g = 0; g < 8; g += 2) {
    const unsigned a = cvtpk(s[g], s[g + 1]);
    const unsigned b = cvtpk(lo16(a) * SCALE, hi16(a) * SCALE);
    s[g] = lo16(b); s[g + 1] = hi16(b);
  }
  float mx = fmaxf(fmaxf(fmaxf(s[0], s[1]), fmaxf(s[2], s[3])), fmaxf(fmaxf(s[4], s[5]), fmaxf(s[6], s[7])));
  mx = xr16_max(mx);
  const float mn = fmaxf(m2, mx * LOG2E);
  const float alpha = __builtin_amdgcn_exp2f(m2 - mn);
  m2 = mn;
  lsum *= alpha;
  int ai = __builtin_bit_cast(int, alpha), ar;
  ar = __builtin_amdgcn_update_dpp(ai, ai, 0x150, 0x1, 0xf, false);
  ar = __builtin_amdgcn_update_dpp(ar, ai, 0x154, 0x2, 0xf, false);
  ar = __builtin_amdgcn_update_dpp(ar, ai, 0x158, 0x4, 0xf, false);
  ar = __builtin_amdgcn_update_dpp(ar, ai, 0x15c, 0x8, 0xf, false);
  const float al = __builtin_bit_cast(float, ar);
  acc0 *= al; acc1 *= al;
#pragma unroll
  for (int g = 0; g < 8; ++g) {
    const float p = __builtin_amdgcn_exp2f(fmaf(s[g], LOG2E, -mn));
    lsum += p;
    const unsigned pp = cvtpk(p, p);
    const v2u a = {pp & mk0, pp & mk1};
    acc0 = mfma16(a, t.v[g][0], t.v[g][1], acc0);
    acc1 = mfma16(a, t.v[g][2], t.v[g][3], acc1);
    if (REFILL) t.v[g] = ld16<VAUX>(rv, voff, snext + g * 1024);
    SB();
  }
}

template <int N>
__device__ __forceinline__ bool tags_ok(const v4u (&x)[N], unsigned tag) {
  bool ok = true;
#pragma unroll
  for (int i = 0; i < N; ++i) ok = ok & (x[i][1] == tag) & (x[i][3] == tag);
  return __all(ok);
}
template <int N>
__device__ __forceinline__ void sweep(rsrc_t rs, const int (&off)[N], v4u (&x)[N]) {
  asm volatile("" ::: "memory");
#pragma unroll
  for (int i = 0; i < N; ++i) x[i] = ld16<AUX_SC1>(rs, off[i], 0);
}
template <int N>
__device__ __forceinline__ void gather(rsrc_t rs, const int (&off)[N], unsigned tag, v4u (&x)[N]) {
  for (unsigned spins = 0;; ++spins) {
    sweep<N>(rs, off, x);
    if (tags_ok<N>(x, tag) || spins > SPIN_CAP) break;
    __builtin_amdgcn_s_sleep(1);
  }
}
#define GATHER(N, off, x) gather<N>(rws, off, tag, x)
#define KEEP(N, x) do { } while (0)
template <int S> __device__ __forceinline__ float strided_sum(float x) {
  if (S <= 1) x += dpp<XOR1>(x);
  if (S <= 2) x += dpp<XOR2>(x);
  if (S <= 8) x += dpp<ROR8>(x);
  if (S <= 4) x += dpp<ROR4>(x);
  return xr16_sum(x);
}
__device__ __forceinline__ void publish512(rsrc_t rs, int base, int nprod, int p, f32x4 a0, f32x4 a1, LAS float *tr, int w, int lane, unsigned tag) {
  LAS unsigned *t = (LAS unsigned *)tr + w * 64;
  if (lane < 16) *(LAS v4u *)(t + 4 * lane) = v4u{cvtpk(a0[0], a0[1]), cvtpk(a0[2], a0[3]), cvtpk(a1[0], a1[1]), cvtpk(a1[2], a1[3])};
  LDS_WAIT();
  const v2u y = *(LAS v2u *)(t + 2 * (lane & 31));
  if (lane < 32) st16(rs, base + ((8 * w + (lane >> 2)) * nprod + p) * 64 + (lane & 3) * 16, v4u{y[0], tag, y[1], tag});
}
template <int N>
__device__ __forceinline__ f32x4 reduce_cols(const v4u (&x)[N], bool last_live, LAS float *red, int w, int lane) {
  f32x4 s = {0.f, 0.f, 0.f, 0.f};
#pragma unroll
  for (int i = 0; i < N; ++i) {
    const f32x4 v = {lo16(x[i][0]), hi16(x[i][0]), lo16(x[i][2]), hi16(x[i][2])};
    if (i + 1 < N) s += v; else s += last_live ? v : f32x4{0.f, 0.f, 0.f, 0.f};
  }
  auto sa = __builtin_amdgcn_permlane32_swap(asu(s[0]), asu(s[2]), false, false);
  auto sb = __builtin_amdgcn_permlane32_swap(asu(s[1]), asu(s[3]), false, false);
  auto sc = __builtin_amdgcn_permlane16_swap(asu(asf(sa[0]) + asf(sa[1])), asu(asf(sb[0]) + asf(sb[1])), false, false);
  float v = asf(sc[0]) + asf(sc[1]);
  v += dpp<ROR8>(v); v += dpp<ROR4>(v);
  if ((lane & 12) == 0) red[w * 64 + 4 * (lane & 3) + 2 * (lane >> 5) + ((lane >> 4) & 1)] = v;
  WG_BARRIER();
  f32x4 r = {0.f, 0.f, 0.f, 0.f};
#pragma unroll
  for (int ww = 0; ww < 4; ++ww) r += *(LAS f32x4 *)(red + ww * 64 + 4 * (lane & 3));
  return r;
}
__device__ __forceinline__ void gemv_step(unsigned pp, unsigned mk0, unsigned mk1, v4u f, f32x4 &c0, f32x4 &c1) {
  const v2u a = {pp & mk0, pp & mk1};
  c0 = mfma16(a, f[0], f[1], c0);
  c1 = mfma16(a, f[2], f[3], c1);
}

#define STAMP_AT(i) do { } while (0)
#define STAMP_FLUSH() do { } while (0)

constexpr int L_MRG = 0;
constexpr int L_ML = L_MRG + 2048;
constexpr int L_RED = L_ML + 32;
constexpr int L_RED2 = L_RED + 256;
constexpr int L_TR = L_RED2 + 256;
constexpr int L_Q = L_TR + 512;
constexpr int L_XN = L_Q + 256;
constexpr int L_HID = L_XN + 1024;
constexpr int L_END = L_HID + 32;
constexpr int LDS_BYTES = 84 * 1024;

__global__ void __launch_bounds__(NTHR, 2) fused_decode_kernel(Params p) {
  extern __shared__ __attribute__((aligned(16))) float smem[];
  LAS float *L = (LAS float *)smem;
  const int tid = threadIdx.x, lane = tid & 63, w = __builtin_amdgcn_readfirstlane(tid >> 6), b = blockIdx.x;
  asm volatile("" :: "s"(p.x), "s"(p.k), "s"(p.v), "s"(p.wq), "s"(p.wo), "s"(p.wg), "s"(p.wu), "s"(p.wd), "s"(p.out), "s"(p.ws));
  STAMP_AT(0);
  const unsigned tstart = (unsigned)__builtin_amdgcn_s_memrealtime();
  const int kh = HM ? b >> 5 : b & 7, sp = HM ? b & 31 : b >> 3;
  const rsrc_t rws = __builtin_amdgcn_make_buffer_rsrc(p.ws, 0, WS_USED, RS3);
  const rsrc_t rk = __builtin_amdgcn_make_buffer_rsrc((char *)p.k + (size_t)kh * SK * 256, 0, SK * 256, RS3);
  const rsrc_t rv = __builtin_amdgcn_make_buffer_rsrc((char *)p.v + (size_t)kh * SK * 256, 0, SK * 256, RS3);
  const int region = (b & 24) | ((b + RK1 * (b >> 3) + RK2 * w) & 7);
  const int tbase = region * 524288 + w * 8192;
  constexpr int TSTR = 4 * 8192;
  const rsrc_t rx = __builtin_amdgcn_make_buffer_rsrc((void *)p.x, 0, DM * 2, RS3);
  const unsigned epoch = *(const __attribute__((address_space(4))) unsigned *)((const char *)p.ws + WS_EPOCH);
  const bool hasq = (b & 7) < 4;
  const int pq = (b >> 3) * 4 + (b & 3), hq = pq & 31, rb = pq >> 5;
  const int g4 = lane >> 4, a4 = (lane >> 2) & 3, u4 = lane & 3;
  const int koff = a4 * 256 + (4 * g4 + u4) * 16, voff = lane * 16;
  const unsigned mk0 = u4 == 0 ? 0xFFFFu : u4 == 1 ? 0xFFFF0000u : 0u;
  const unsigned mk1 = u4 == 2 ? 0xFFFFu : u4 == 3 ? 0xFFFF0000u : 0u;
  v4u wqf[8];
  unsigned xq[8];
  Tile ta, tb;
  const rsrc_t rxq = __builtin_amdgcn_make_buffer_rsrc((void *)p.x, 0, hasq ? DM * 2 : 0, RS3);
  const rsrc_t rwq = __builtin_amdgcn_make_buffer_rsrc((void *)p.wq, 0, hasq ? DM * 4096 * 2 : 0, RS3);
  const v4u xr = ld16<0>(rxq, lane * 16, 0);
  const unsigned xown = __builtin_amdgcn_raw_buffer_load_b32(rxq, (64 * rb + 16 * w + (lane & 15)) * 4, 0, 0);
  const int wqoff = (128 * rb + 32 * w + g4) * 8192 + hq * 256 + (lane & 15) * 16;
#pragma unroll
  for (int i = 0; i < 8; ++i) wqf[i] = ld16<WAUX>(rwq, wqoff, i * 4 * 8192);
#pragma unroll
  for (int g = 0; g < EARLYK; ++g) ta.k[g] = ld16<KAUX>(rk, koff, tbase + g * 1024);
  SB();
  if (hasq) {
    ((LAS unsigned *)L)[L_XN + w * 64 + lane] = ln_pair(xown, ln_stats(xr));
    LDS_WAIT();
#pragma unroll
    for (int i = 0; i < 8; ++i) xq[i] = ((LAS unsigned short *)(L + L_XN + w * 64))[4 * i + g4];
    STAMP_AT(12);
    f32x4 c0 = {0.f, 0.f, 0.f, 0.f}, c1 = c0;
#pragma unroll
    for (int i = 0; i < 8; ++i) {
      gemv_step(xq[i] | (xq[i] << 16), mk0, mk1, wqf[i], c0, c1);
    }
    if (lane < 16) {
      *(LAS f32x4 *)(L + L_TR + w * 128 + 8 * lane) = c0;
      *(LAS f32x4 *)(L + L_TR + w * 128 + 8 * lane + 4) = c1;
    }
    STAMP_AT(13);
    WG_BARRIER();
    if (w < QREP) {
      f32x2 y = {0.f, 0.f};
#pragma unroll
      for (int ww = 0; ww < 4; ++ww) y += *(LAS f32x2 *)(L + L_TR + ww * 128 + 2 * lane);
      st16(rws, WS_QG + w * 131072 + ((hq * 4 + rb) * 128 + 2 * lane) * 8, v4u{asu(y[0]), epoch + 1u, asu(y[1]), epoch + 1u});
    }
  }
  STAMP_AT(1);
#pragma unroll
  for (int g = EARLYK; g < 8; ++g) ta.k[g] = ld16<KAUX>(rk, koff, tbase + g * 1024);
#pragma unroll
  for (int g = 0; g < 8; ++g) ta.v[g] = ld16<VAUX>(rv, voff, tbase + g * 1024);
  SB();
  unsigned tag = epoch + 1u;
  asm volatile("" : "+s"(tag));
  while ((unsigned)__builtin_amdgcn_s_memrealtime() - tstart < (unsigned)TPOLL) __builtin_amdgcn_s_sleep(1);
  v4u q;
  {
    const int qoff = WS_QG + (sp & (QREP - 1)) * 131072 + (4 * kh + w) * 4096 + lane * 16;
    unsigned touch;
    {
      const unsigned long long ta_ = (unsigned long long)(b >= NPW + NPF + 32 ? (const void *)p.out : p.x);
      const v4u td = {(unsigned)ta_, (unsigned)(ta_ >> 32) & 0xffffu, (TOUCH_OUT && b >= NPW + NPF && b < NPW + NPF + 64) ? 4u : 0u, (unsigned)RS3};
      asm volatile("buffer_load_dword %0, off, %1, 0" : "=v"(touch) : "s"(td));
    }
    v4u x[4];
#pragma unroll
    for (int i = 0; i < 4; ++i) x[i] = ld16<AUX_SC1>(rws, qoff, i * 1024);
    SB();
    asm volatile("s_barrier" ::: "memory");
    SB();
#pragma unroll
    for (int g = 0; g < 8; ++g) tb.k[g] = ld16<KAUX>(rk, koff, tbase + TSTR + g * 1024);
#pragma unroll
    for (int g = 0; g < 8; ++g) tb.v[g] = ld16<VAUX>(rv, voff, tbase + TSTR + g * 1024);
    SB();
    bool ok = true;
#pragma unroll
    for (int i = 0; i < 4; ++i) ok = ok & (x[i][1] == tag) & (x[i][3] == tag);
    asm volatile("" :: "v"(touch), "s"((int)__all(ok)));
    if (__builtin_expect(!__all(ok), 0)) {
      for (unsigned spins = 0; spins < SPIN_CAP; ++spins) {
        bool ok2 = true;
        asm volatile("" ::: "memory");
#pragma unroll
        for (int i = 0; i < 4; ++i) x[i] = ld16<AUX_SC1>(rws, qoff, i * 1024);
#pragma unroll
        for (int i = 0; i < 4; ++i) ok2 = ok2 & (x[i][1] == tag) & (x[i][3] == tag);
        if (__all(ok2)) break;
      }
      __builtin_amdgcn_s_waitcnt(0x0F70);
    }
    ((LAS unsigned *)L)[L_Q + w * 64 + lane] = cvtpk((asf(x[0][0]) + asf(x[1][0])) + (asf(x[2][0]) + asf(x[3][0])),
                                                     (asf(x[0][2]) + asf(x[1][2])) + (asf(x[2][2]) + asf(x[3][2])));
    WG_BARRIER();
    q = *(LAS v4u *)(L + L_Q + a4 * 64 + (4 * g4 + u4) * 4);
  }
  STAMP_AT(2);
  float m2 = -3.0e38f, lsum = 0.f;
  f32x4 acc0 = {0.f, 0.f, 0.f, 0.f}, acc1 = acc0;
  for (int it = 0; it < 7; ++it) {
    const int s0 = tbase + (2 * it + 2) * TSTR;
    if (TAU) { while ((unsigned)__builtin_amdgcn_s_memrealtime() - tstart < (unsigned)(TOFF + 2 * it * TAU)) __builtin_amdgcn_s_sleep(1); }
    tile_step<true>(ta, q, rk, rv, koff, voff, s0, mk0, mk1, m2, lsum, acc0, acc1);
    if (TAU) { while ((unsigned)__builtin_amdgcn_s_memrealtime() - tstart < (unsigned)(TOFF + (2 * it + 1) * TAU)) __builtin_amdgcn_s_sleep(1); }
    tile_step<true>(tb, q, rk, rv, koff, voff, s0 + TSTR, mk0, mk1, m2, lsum, acc0, acc1);
  }
  const bool role_wo = b < NPW;
  const rsrc_t rwo = __builtin_amdgcn_make_buffer_rsrc((char *)p.wo + (size_t)(role_wo ? b : 0) * 32768, 0, role_wo ? 32768 : 0, RS3);
  const int foff = g4 * 1024 + w * 256 + (lane & 15) * 16;
  v4u wo[8];
#pragma unroll
  for (int i = 0; i < 8; ++i) wo[i] = ld16<WAUX>(rwo, foff, i * 4096);
  SB();
  tile_step<false>(ta, q, rk, rv, koff, voff, 0, mk0, mk1, m2, lsum, acc0, acc1);
  tile_step<false>(tb, q, rk, rv, koff, voff, 0, mk0, mk1, m2, lsum, acc0, acc1);
  STAMP_AT(3);
  {
    const float lw = xr16_sum(lsum);
    *(LAS f32x4 *)(L + L_MRG + w * 512 + g4 * 128 + 8 * (lane & 15)) = acc0;
    *(LAS f32x4 *)(L + L_MRG + w * 512 + g4 * 128 + 8 * (lane & 15) + 4) = acc1;
    if (lane < 16 && u4 == 0) { f32x2 y = {m2, lw}; *(LAS f32x2 *)(L + L_ML + (w * 4 + a4) * 2) = y; }
    WG_BARRIER();
    f32x2 ml[4];
#pragma unroll
    for (int ww = 0; ww < 4; ++ww) ml[ww] = *(LAS f32x2 *)(L + L_ML + (ww * 4 + w) * 2);
    const float M = fmaxf(fmaxf(ml[0][0], ml[1][0]), fmaxf(ml[2][0], ml[3][0]));
    float Ls = 0.f; f32x2 o = {0.f, 0.f};
#pragma unroll
    for (int ww = 0; ww < 4; ++ww) {
      const float e = __builtin_amdgcn_exp2f(ml[ww][0] - M);
      Ls = fmaf(e, ml[ww][1], Ls);
      o += e * *(LAS f32x2 *)(L + L_MRG + ww * 512 + w * 128 + 2 * lane);
    }
    const int c4 = (4 * kh + w) * 4;
    st8(rws, WS_OP + ((c4 + g4) * 32 + sp) * 128 + (lane & 15) * 8, v2u{cvtpk(o[0], o[1]), tag});
    if (lane < 4) st16(rws, WS_ML + ((c4 + lane) * 32 + sp) * 16, v4u{asu(M), tag, asu(Ls), tag});
  }
  STAMP_AT(4);
  if (role_wo) {
    const int off[2] = {WS_OP + b * 4096 + tid * 16, WS_ML + (b * 32 + (tid >> 3)) * 16};
    v4u x[2];
    GATHER(2, off, x);
    STAMP_AT(5);
    const float ms = asf(x[1][0]), ls = asf(x[1][2]);
    const float Mw = xr16_max(fmaxf(ms, dpp<ROR8>(ms)));
    const float e = __builtin_amdgcn_exp2f(ms - Mw);
    auto sa = __builtin_amdgcn_permlane32_swap(asu(e * lo16(x[0][0])), asu(e * lo16(x[0][2])), false, false);
    auto sb = __builtin_amdgcn_permlane32_swap(asu(e * hi16(x[0][0])), asu(e * hi16(x[0][2])), false, false);
    auto sc = __builtin_amdgcn_permlane16_swap(asu(asf(sa[0]) + asf(sa[1])), asu(asf(sb[0]) + asf(sb[1])), false, false);
    float v = asf(sc[0]) + asf(sc[1]);
    v += dpp<ROR8>(v);
    const float lw = strided_sum<8>(e * ls);
    if ((lane & 8) == 0) L[L_RED2 + w * 64 + (2 * (lane >> 5) + ((lane >> 4) & 1)) * 8 + (lane & 7)] = v;
    if (lane == 0) *(LAS f32x2 *)(L + L_RED2 + w * 64 + 32) = f32x2{Mw, lw};
    WG_BARRIER();
    f32x2 ml[4];
#pragma unroll
    for (int ww = 0; ww < 4; ++ww) ml[ww] = *(LAS f32x2 *)(L + L_RED2 + ww * 64 + 32);
    const float M = fmaxf(fmaxf(ml[0][0], ml[1][0]), fmaxf(ml[2][0], ml[3][0]));
    f32x4 oa = {0.f, 0.f, 0.f, 0.f}, ob = oa;
    float Lt = 0.f;
#pragma unroll
    for (int ww = 0; ww < 4; ++ww) {
      const float ew = __builtin_amdgcn_exp2f(ml[ww][0] - M);
      Lt = fmaf(ew, ml[ww][1], Lt);
      oa += ew * *(LAS f32x4 *)(L + L_RED2 + ww * 64 + g4 * 8);
      ob += ew * *(LAS f32x4 *)(L + L_RED2 + ww * 64 + g4 * 8 + 4);
    }
    const float rl = __builtin_amdgcn_rcpf(Lt);
    f32x4 c0 = {0.f, 0.f, 0.f, 0.f}, c1 = c0;
#pragma unroll
    for (int i = 0; i < 8; ++i) { const float o = (i < 4 ? oa[i & 3] : ob[i & 3]) * rl; gemv_step(cvtpk(o, o), mk0, mk1, wo[i], c0, c1); }
    publish512(rws, WS_PB, NPW, b, c0, c1, L + L_TR, w, lane, tag);
    KEEP(2, x);
    STAMP_AT(6);
  } else if (b < NPW + NPF) {
    const int pf = (b & 7) * 7 + ((b - NPW) >> 3);
    const rsrc_t rwg = __builtin_amdgcn_make_buffer_rsrc((char *)p.wg + pf * 64, 0, DM * FF * 2 - pf * 64, RS3);
    const rsrc_t rwu = __builtin_amdgcn_make_buffer_rsrc((char *)p.wu + pf * 64, 0, DM * FF * 2 - pf * 64, RS3);
    const rsrc_t rwd = __builtin_amdgcn_make_buffer_rsrc((char *)p.wd + (size_t)pf * 32768, 0, 32768, RS3);
    const int go = (128 * w + 4 * g4 + a4) * (FF * 2) + u4 * 16;
    v4u wg[8], wu[8], wd[8];
#pragma unroll
    for (int i = 0; i < 8; ++i) { wg[i] = ld16<WAUX>(rwg, go, i * 16 * FF * 2); wu[i] = ld16<WAUX>(rwu, go, i * 16 * FF * 2); }
#pragma unroll
    for (int i = 0; i < 8; ++i) wd[i] = ld16<WAUX>(rwd, foff, i * 4096);
    SB();
      v4u x[1];
      const int off[1] = {WS_HG + (b % REP) * 2048 + (32 * w + (lane & 31)) * 16};
      GATHER(1, off, x);
      if (lane < 32) *(LAS v2u *)((LAS unsigned *)L + L_Q + 64 * w + 2 * lane) = v2u{x[0][0], x[0][2]};
    WG_BARRIER();
    STAMP_AT(9);
    {
      const v4u hb = *(LAS v4u *)(L + L_Q + 4 * lane);
      const unsigned own = ((LAS unsigned *)L)[L_Q + 64 * w + lane];
      ((LAS unsigned *)L)[L_XN + w * 64 + lane] = ln_pair(own, ln_stats(hb));
      LDS_WAIT();
    }
    STAMP_AT(12);
    f32x4 g0 = {0.f, 0.f, 0.f, 0.f}, g1 = g0, u0 = g0, u1 = g0;
#pragma unroll
    for (int i = 0; i < 8; ++i) {
      const unsigned xv = ((LAS unsigned short *)(L + L_XN + w * 64))[16 * i + 4 * g4 + a4];
      const unsigned pp = xv | (xv << 16);
      gemv_step(pp, mk0, mk1, wg[i], g0, g1);
      gemv_step(pp, mk0, mk1, wu[i], u0, u1);
    }
    if (g4 == a4) {
      LAS float *gu = L + L_MRG + (w * 4 + g4) * 64 + 8 * u4;
      *(LAS f32x4 *)(gu) = g0; *(LAS f32x4 *)(gu + 4) = g1; *(LAS f32x4 *)(gu + 32) = u0; *(LAS f32x4 *)(gu + 36) = u1;
    }
    STAMP_AT(13);
    WG_BARRIER();
    {
      const int c = tid >> 3, part = tid & 7;
      LAS float *gu = L + L_MRG + part * 128 + c;
      float g = gu[0] + gu[64], u = gu[32] + gu[96];
      g += dpp<XOR1>(g); u += dpp<XOR1>(u); g += dpp<XOR2>(g); u += dpp<XOR2>(u); g += dpp<HMIR>(g); u += dpp<HMIR>(u);
      const float hv = rbf(g) * rbf(u);
      if (part == 0) ((LAS unsigned *)L)[L_HID + (c & 3) * 8 + (c >> 2)] = cvtpk(hv, hv);
    }
    WG_BARRIER();
    STAMP_AT(14);
    const v4u ha = *(LAS v4u *)(L + L_HID + g4 * 8), hc = *(LAS v4u *)(L + L_HID + g4 * 8 + 4);
    f32x4 c0 = {0.f, 0.f, 0.f, 0.f}, c1 = c0;
#pragma unroll
    for (int i = 0; i < 8; ++i) gemv_step(i < 4 ? ha[i & 3] : hc[i & 3], mk0, mk1, wd[i], c0, c1);
    publish512(rws, WS_PD, NPF, pf, c0, c1, L + L_TR, w, lane, tag);
    KEEP(1, x);
    STAMP_AT(10);
  } else if (b < NPW + NPF + 32) {
    const int j = b - (NPW + NPF);
    const v2u xv = __builtin_amdgcn_raw_buffer_load_b64(rx, (16 * j + 4 * (tid & 3)) * 2, 0, 0);
    const int off[2] = {WS_PB + j * (NPW * 64) + tid * 16, WS_PB + j * (NPW * 64) + 4096 + tid * 16};
    v4u x[2];
    GATHER(2, off, x);
    STAMP_AT(7);
    const f32x4 r = reduce_cols<2>(x, true, L + L_RED, w, lane);
    const unsigned h0 = cvtpk(lo16(xv[0]) + rbf(r[0]), hi16(xv[0]) + rbf(r[1])), h1 = cvtpk(lo16(xv[1]) + rbf(r[2]), hi16(xv[1]) + rbf(r[3]));
    if (tid < 4 * REP) st16(rws, WS_HG + (tid >> 2) * 2048 + (4 * j + (tid & 3)) * 16, v4u{h0, tag, h1, tag});
    KEEP(2, x);
    STAMP_AT(8);
  } else if (b < NPW + NPF + 64) {
    const int j = b - (NPW + NPF + 32);
    const bool live = tid < NPF * 4;
    const int off[2] = {WS_HG + (b % REP) * 2048 + (4 * j + (tid & 3)) * 16, WS_PD + j * (NPF * 64) + (live ? tid : tid - 128) * 16};
    v4u x[2];
    GATHER(2, off, x);
    const v4u y[1] = {x[1]};
    const f32x4 r = reduce_cols<1>(y, live, L + L_RED, w, lane);
    const v2u o = {cvtpk(lo16(x[0][0]) + rbf(r[0]), hi16(x[0][0]) + rbf(r[1])), cvtpk(lo16(x[0][2]) + rbf(r[2]), hi16(x[0][2]) + rbf(r[3]))};
    const rsrc_t rout = __builtin_amdgcn_make_buffer_rsrc(p.out, 0, DM * 2, RS3);
    if (tid < 4) __builtin_amdgcn_raw_buffer_store_b64(o, rout, (16 * j + 4 * tid) * 2, 0, OUTAUX);
    if (j == 0 && tid == 0) __builtin_amdgcn_raw_buffer_store_b32(tag, rws, WS_EPOCH, 0, AUX_SC1);
    KEEP(2, x);
  }
  STAMP_AT(11);
  STAMP_FLUSH();
}
}

extern "C" void kernel_launch(void *const *d_in, const int *in_sizes, int n_in, void *d_out, int out_size, void *d_ws, size_t ws_size,
                              hipStream_t stream) {
  Params p;
  p.x = d_in[0]; p.k = d_in[1]; p.v = d_in[2]; p.wq = d_in[3]; p.wo = d_in[4];
  p.wg = d_in[6]; p.wu = d_in[7]; p.wd = d_in[8];
  p.out = d_out; p.ws = d_ws;
  (void)hipFuncSetAttribute((const void *)fused_decode_kernel, hipFuncAttributeMaxDynamicSharedMemorySize, LDS_BYTES);
  fused_decode_kernel<<<NWG, NTHR, LDS_BYTES, stream>>>(p);
}
```

```cpp
#include <hip/hip_runtime.h>
#include <stdint.h>

#define STAMP 0
#define REP 16
#define KAUX 2
#define VAUX 2
#define WAUX 2
#define PINARGS 1
#define ORD 1
#define HM 1
#define TAU 0
#define TOFF 500
#define TK 60
#define TPOLL 280
#define QREP 4

namespace {
typedef unsigned v4u __attribute__((ext_vector_type(4)));
typedef unsigned v2u __attribute__((ext_vector_type(2)));
typedef float f32x4 __attribute__((ext_vector_type(4)));
typedef float f32x2 __attribute__((ext_vector_type(2)));
typedef short s16x4 __attribute__((ext_vector_type(4)));
typedef __bf16 bf16x8 __attribute__((ext_vector_type(8)));
typedef __bf16 bf16x2 __attribute__((ext_vector_type(2)));
typedef __amdgpu_buffer_rsrc_t rsrc_t;
#define LAS __attribute__((address_space(3)))

constexpr int DM = 512, SK = 65536, DH = 128, FF = 1792;
constexpr int NWG = 256, NTHR = 256;
constexpr int NPW = 128, NPF = 56;
constexpr int AUX_NT = 2, AUX_SC1 = 16;
constexpr int RS3 = 0x00020000;
constexpr int WS_EPOCH = 0;
constexpr int WS_ML = 4096;
constexpr int WS_HG = WS_ML + 65536;
constexpr int WS_OP = 1 << 20;
constexpr int WS_PB = 2 << 20;
constexpr int WS_PD = 3 << 20;
constexpr int WS_QG = 4 << 20;
constexpr int WS_USED = 5 << 20;
constexpr int WS_STAMP = 8 << 20;
constexpr unsigned SPIN_CAP = 1u << 14;
constexpr float LOG2E = 1.4426950408889634f;
constexpr float SCALE = 0.08838834764831845f;

struct Params {
  const void *x, *k, *v, *wq, *wo, *wg, *wu, *wd;
  void *out, *ws;
};

__device__ __forceinline__ float asf(unsigned u) { return __builtin_bit_cast(float, u); }
__device__ __forceinline__ unsigned asu(float f) { return __builtin_bit_cast(unsigned, f); }
template <int C> __device__ __forceinline__ float dpp(float x) {
  return __builtin_bit_cast(float, __builtin_amdgcn_mov_dpp(__builtin_bit_cast(int, x), C, 0xf, 0xf, true));
}
constexpr int XOR1 = 0xB1, XOR2 = 0x4E, HMIR = 0x141, MIR = 0x140, ROR4 = 0x124, ROR8 = 0x128;
__device__ __forceinline__ unsigned cvtpk(float lo, float hi) {
  f32x2 v = {lo, hi};
  bf16x2 r = __builtin_convertvector(v, bf16x2);
  return __builtin_bit_cast(unsigned, r);
}
__device__ __forceinline__ float lo16(unsigned u) { return asf(u << 16); }
__device__ __forceinline__ float hi16(unsigned u) { return asf(u & 0xffff0000u); }
__device__ __forceinline__ float rbf(float x) { return lo16(cvtpk(x, x)); }
__device__ __forceinline__ float xr16_max(float x) {
  auto s = __builtin_amdgcn_permlane16_swap(asu(x), asu(x), false, false);
  x = fmaxf(asf(s[0]), asf(s[1]));
  auto t = __builtin_amdgcn_permlane32_swap(asu(x), asu(x), false, false);
  return fmaxf(asf(t[0]), asf(t[1]));
}
__device__ __forceinline__ float xr16_sum(float x) {
  auto s = __builtin_amdgcn_permlane16_swap(asu(x), asu(x), false, false);
  x = asf(s[0]) + asf(s[1]);
  auto t = __builtin_amdgcn_permlane32_swap(asu(x), asu(x), false, false);
  return asf(t[0]) + asf(t[1]);
}
__device__ __forceinline__ float row_sum(float x) {
  x += dpp<XOR1>(x); x += dpp<XOR2>(x); x += dpp<HMIR>(x); x += dpp<MIR>(x); return x;
}
__device__ __forceinline__ float row_max(float x) {
  x = fmaxf(x, dpp<XOR1>(x)); x = fmaxf(x, dpp<XOR2>(x)); x = fmaxf(x, dpp<HMIR>(x)); x = fmaxf(x, dpp<MIR>(x)); return x;
}
__device__ __forceinline__ float wave_sum(float x) { return xr16_sum(row_sum(x)); }
__device__ __forceinline__ f32x4 mfma32(v4u a, v4u b, f32x4 c) {
  return __builtin_amdgcn_mfma_f32_16x16x32_bf16(__builtin_bit_cast(bf16x8, a), __builtin_bit_cast(bf16x8, b), c, 0, 0, 0);
}
__device__ __forceinline__ f32x4 mfma16(v2u a, unsigned b0, unsigned b1, f32x4 c) {
  v2u b = {b0, b1};
  return __builtin_amdgcn_mfma_f32_16x16x16bf16_1k(__builtin_bit_cast(s16x4, a), __builtin_bit_cast(s16x4, b), c, 0, 0, 0);
}
template <int AUX> __device__ __forceinline__ v4u ld16(rsrc_t r, int voff, int soff) {
  return __builtin_amdgcn_raw_buffer_load_b128(r, voff, soff, AUX);
}
__device__ __forceinline__ void st16(rsrc_t r, int voff, v4u v) { __builtin_amdgcn_raw_buffer_store_b128(v, r, voff, 0, AUX_SC1); }
__device__ __forceinline__ void st8(rsrc_t r, int voff, v2u v) { __builtin_amdgcn_raw_buffer_store_b64(v, r, voff, 0, AUX_SC1); }
#define WG_BARRIER() asm volatile("s_waitcnt lgkmcnt(0)\n\ts_barrier" ::: "memory")
#define LDS_WAIT() asm volatile("s_waitcnt lgkmcnt(0)" ::: "memory")
#define SB() __builtin_amdgcn_sched_barrier(0)

struct LnStat { float mb, rden; float den; };
__device__ __forceinline__ LnStat ln_stats(v4u p) {
  float x[8];
#pragma unroll
  for (int i = 0; i < 4; ++i) { x[2 * i] = lo16(p[i]); x[2 * i + 1] = hi16(p[i]); }
  float s = ((x[0] + x[1]) + (x[2] + x[3])) + ((x[4] + x[5]) + (x[6] + x[7]));
  const float mean = wave_sum(s) * (1.f / DM);
  float q = 0.f;
#pragma unroll
  for (int i = 0; i < 8; ++i) { const float d = x[i] - mean; q = fmaf(d, d, q); }
  const float var = wave_sum(q) * (1.f / DM);
  LnStat r; r.mb = rbf(mean); r.den = rbf(__builtin_amdgcn_sqrtf(rbf(rbf(var) + 1e-5f))); r.rden = __builtin_amdgcn_rcpf(r.den);
  return r;
}
__device__ __forceinline__ unsigned ln_pair(unsigned p, LnStat st) { return cvtpk(rbf(lo16(p) - st.mb) * st.rden, rbf(hi16(p) - st.mb) * st.rden); }
struct Tile { v4u k[8], v[8]; };

template <bool REFILL>
__device__ __forceinline__ void tile_step(Tile &t, v4u q, rsrc_t rk, rsrc_t rv, int koff, int voff, int snext, unsigned mk0, unsigned mk1,
                                          float &m2, float &lsum, f32x4 &acc0, f32x4 &acc1) {
  float s[8];
  const f32x4 z = {0.f, 0.f, 0.f, 0.f};
  f32x4 d[8];
#pragma unroll
  for (int g = 0; g < 8; ++g) {
    d[g] = mfma32(t.k[g], q, z);
    if (REFILL) t.k[g] = ld16<KAUX>(rk, koff, snext + g * 1024);
    SB();
  }
#pragma unroll
  for (int g = 0; g < 8; ++g) {
    s[g] = ((dpp<0x00>(d[g][0]) + dpp<0x55>(d[g][1])) + dpp<0xAA>(d[g][2])) + dpp<0xFF>(d[g][3]);
    asm("" : "+v"(s[g]));
  }
#pragma unroll
  for (int g = 0; g < 8; g += 2) {
    const unsigned a = cvtpk(s[g], s[g + 1]);
    const unsigned b = cvtpk(lo16(a) * SCALE, hi16(a) * SCALE);
    s[g] = lo16(b); s[g + 1] = hi16(b);
  }
  float mx = fmaxf(fmaxf(fmaxf(s[0], s[1]), fmaxf(s[2], s[3])), fmaxf(fmaxf(s[4], s[5]), fmaxf(s[6], s[7])));
  mx = xr16_max(mx);
  const float mn = fmaxf(m2, mx * LOG2E);
  const float alpha = __builtin_amdgcn_exp2f(m2 - mn);
  m2 = mn;
  lsum *= alpha;
  int ai = __builtin_bit_cast(int, alpha), ar;
  ar = __builtin_amdgcn_update_dpp(ai, ai, 0x150, 0x1, 0xf, false);
  ar = __builtin_amdgcn_update_dpp(ar, ai, 0x154, 0x2, 0xf, false);
  ar = __builtin_amdgcn_update_dpp(ar, ai, 0x158, 0x4, 0xf, false);
  ar = __builtin_amdgcn_update_dpp(ar, ai, 0x15c, 0x8, 0xf, false);
  const float al = __builtin_bit_cast(float, ar);
  acc0 *= al; acc1 *= al;
#pragma unroll
  for (int g = 0; g < 8; ++g) {
    const float p = __builtin_amdgcn_exp2f(fmaf(s[g], LOG2E, -mn));
    lsum += p;
    const unsigned pp = cvtpk(p, p);
    const v2u a = {pp & mk0, pp & mk1};
    acc0 = mfma16(a, t.v[g][0], t.v[g][1], acc0);
    acc1 = mfma16(a, t.v[g][2], t.v[g][3], acc1);
    if (REFILL) t.v[g] = ld16<VAUX>(rv, voff, snext + g * 1024);
    SB();
  }
}

template <int N>
__device__ __forceinline__ void gather(rsrc_t rs, const int (&off)[N], unsigned tag, v4u (&x)[N]) {
  for (unsigned spins = 0;; ++spins) {
    bool ok = true;
    asm volatile("" ::: "memory");
#pragma unroll
    for (int i = 0; i < N; ++i) x[i] = ld16<AUX_SC1>(rs, off[i], 0);
#pragma unroll
    for (int i = 0; i < N; ++i) ok = ok & (x[i][1] == tag) & (x[i][3] == tag);
    if (__all(ok) || spins > SPIN_CAP) break;
    __builtin_amdgcn_s_sleep(1);
  }
}
template <int S> __device__ __forceinline__ float strided_sum(float x) {
  if (S <= 1) x += dpp<XOR1>(x);
  if (S <= 2) x += dpp<XOR2>(x);
  if (S <= 8) x += dpp<ROR8>(x);
  if (S <= 4) x += dpp<ROR4>(x);
  return xr16_sum(x);
}
__device__ __forceinline__ void publish512(rsrc_t rs, int base, int nprod, int p, f32x4 a0, f32x4 a1, LAS float *tr, int w, int lane, unsigned tag) {
  LAS unsigned *t = (LAS unsigned *)tr + w * 64;
  if (lane < 16) *(LAS v4u *)(t + 4 * lane) = v4u{cvtpk(a0[0], a0[1]), cvtpk(a0[2], a0[3]), cvtpk(a1[0], a1[1]), cvtpk(a1[2], a1[3])};
  LDS_WAIT();
  const v2u y = *(LAS v2u *)(t + 2 * (lane & 31));
  if (lane < 32) st16(rs, base + ((8 * w + (lane >> 2)) * nprod + p) * 64 + (lane & 3) * 16, v4u{y[0], tag, y[1], tag});
}
template <int N>
__device__ __forceinline__ f32x4 reduce_cols(const v4u (&x)[N], bool last_live, LAS float *red, int w, int lane) {
  f32x4 s = {0.f, 0.f, 0.f, 0.f};
#pragma unroll
  for (int i = 0; i < N; ++i) {
    const f32x4 v = {lo16(x[i][0]), hi16(x[i][0]), lo16(x[i][2]), hi16(x[i][2])};
    if (i + 1 < N) s += v; else s += last_live ? v : f32x4{0.f, 0.f, 0.f, 0.f};
  }
#pragma unroll
  for (int j = 0; j < 4; ++j) s[j] = strided_sum<4>(s[j]);
  if (lane < 4) *(LAS f32x4 *)(red + w * 64 + 4 * lane) = s;
  WG_BARRIER();
  f32x4 r = {0.f, 0.f, 0.f, 0.f};
#pragma unroll
  for (int ww = 0; ww < 4; ++ww) r += *(LAS f32x4 *)(red + ww * 64 + 4 * (lane & 3));
  return r;
}
__device__ __forceinline__ void gemv_step(unsigned pp, unsigned mk0, unsigned mk1, v4u f, f32x4 &c0, f32x4 &c1) {
  const v2u a = {pp & mk0, pp & mk1};
  c0 = mfma16(a, f[0], f[1], c0);
  c1 = mfma16(a, f[2], f[3], c1);
}

#define STAMP_AT(i) do { } while (0)
#define STAMP_FLUSH() do { } while (0)

constexpr int L_MRG = 0;
constexpr int L_ML = L_MRG + 2048;
constexpr int L_RED = L_ML + 32;
constexpr int L_RED2 = L_RED + 256;
constexpr int L_TR = L_RED2 + 256;
constexpr int L_Q = L_TR + 512;
constexpr int L_XN = L_Q + 256;
constexpr int L_HID = L_XN + 1024;
constexpr int L_END = L_HID + 32;
constexpr int LDS_BYTES = 84 * 1024;

__global__ void __launch_bounds__(NTHR, 2) fused_decode_kernel(Params p) {
  extern __shared__ __attribute__((aligned(16))) float smem[];
  LAS float *L = (LAS float *)smem;
  const int tid = threadIdx.x, lane = tid & 63, w = __builtin_amdgcn_readfirstlane(tid >> 6), b = blockIdx.x;
  asm volatile("" :: "s"(p.x), "s"(p.k), "s"(p.v), "s"(p.wq), "s"(p.wo), "s"(p.wg), "s"(p.wu), "s"(p.wd), "s"(p.out), "s"(p.ws));
  STAMP_AT(0);
  const unsigned tstart = (unsigned)__builtin_amdgcn_s_memrealtime();
  const int kh = HM ? b >> 5 : b & 7, sp = HM ? b & 31 : b >> 3;
  const rsrc_t rws = __builtin_amdgcn_make_buffer_rsrc(p.ws, 0, WS_USED, RS3);
  const rsrc_t rk = __builtin_amdgcn_make_buffer_rsrc((char *)p.k + (size_t)kh * SK * 256, 0, SK * 256, RS3);
  const rsrc_t rv = __builtin_amdgcn_make_buffer_rsrc((char *)p.v + (size_t)kh * SK * 256, 0, SK * 256, RS3);
  const int tbase = ORD == 1 ? sp * 524288 + w * 8192 : ORD == 2 ? (sp * 4 + w) * 8192 : (w * 32 + sp) * 8192;
  constexpr int TSTR = ORD == 1 ? 4 * 8192 : 128 * 8192;
  const rsrc_t rx = __builtin_amdgcn_make_buffer_rsrc((void *)p.x, 0, DM * 2, RS3);
  const rsrc_t rwq = __builtin_amdgcn_make_buffer_rsrc((void *)p.wq, 0, DM * 4096 * 2, RS3);
  const unsigned epoch = *(const __attribute__((address_space(4))) unsigned *)((const char *)p.ws + WS_EPOCH);
  const bool hasq = (b & 7) < 4;
  const int pq = (b >> 3) * 4 + (b & 3), hq = pq & 31, rb = pq >> 5;
  const int g4 = lane >> 4, a4 = (lane >> 2) & 3, u4 = lane & 3;
  const int koff = a4 * 256 + (4 * g4 + u4) * 16, voff = lane * 16;
  const unsigned mk0 = u4 == 0 ? 0xFFFFu : u4 == 1 ? 0xFFFF0000u : 0u;
  const unsigned mk1 = u4 == 2 ? 0xFFFFu : u4 == 3 ? 0xFFFF0000u : 0u;
  v4u wqf[8];
  unsigned xq[8];
  Tile ta, tb;
  if (hasq) {
    const v4u xr = ld16<0>(rx, lane * 16, 0);
    const int wqoff = (128 * rb + 32 * w + g4) * 8192 + hq * 256 + (lane & 15) * 16;
#pragma unroll
    for (int i = 0; i < 8; ++i) wqf[i] = ld16<WAUX>(rwq, wqoff, i * 4 * 8192);
    SB();
    const LnStat st = ln_stats(xr);
    *(LAS v4u *)(L + L_XN + w * 256 + 4 * lane) = v4u{ln_pair(xr[0], st), ln_pair(xr[1], st), ln_pair(xr[2], st), ln_pair(xr[3], st)};
    LDS_WAIT();
#pragma unroll
    for (int i = 0; i < 8; ++i) xq[i] = ((LAS unsigned short *)(L + L_XN + w * 256))[128 * rb + 32 * w + 4 * i + g4];
    STAMP_AT(12);
  }
  if (hasq) {
    f32x4 c0 = {0.f, 0.f, 0.f, 0.f}, c1 = c0;
#pragma unroll
    for (int i = 0; i < 8; ++i) {
      gemv_step(xq[i] | (xq[i] << 16), mk0, mk1, wqf[i], c0, c1);
    }
    if (lane < 16) {
      *(LAS f32x4 *)(L + L_TR + w * 128 + 8 * lane) = c0;
      *(LAS f32x4 *)(L + L_TR + w * 128 + 8 * lane + 4) = c1;
    }
    STAMP_AT(13);
    WG_BARRIER();
    if (w < QREP) {
      f32x2 y = {0.f, 0.f};
#pragma unroll
      for (int ww = 0; ww < 4; ++ww) y += *(LAS f32x2 *)(L + L_TR + ww * 128 + 2 * lane);
      st16(rws, WS_QG + w * 131072 + ((hq * 4 + rb) * 128 + 2 * lane) * 8, v4u{asu(y[0]), epoch + 1u, asu(y[1]), epoch + 1u});
    }
  }
  STAMP_AT(1);
#pragma unroll
  for (int g = 0; g < 8; ++g) ta.k[g] = ld16<KAUX>(rk, koff, tbase + g * 1024);
#pragma unroll
  for (int g = 0; g < 8; ++g) ta.v[g] = ld16<VAUX>(rv, voff, tbase + g * 1024);
  SB();
  const unsigned tag = epoch + 1u;
  while ((unsigned)__builtin_amdgcn_s_memrealtime() - tstart < (unsigned)TPOLL) __builtin_amdgcn_s_sleep(1);
  v4u q;
  {
    const int qoff = WS_QG + (sp & (QREP - 1)) * 131072 + (4 * kh + w) * 4096 + lane * 16;
    v4u x[4];
#pragma unroll
    for (int i = 0; i < 4; ++i) x[i] = ld16<AUX_SC1>(rws, qoff, i * 1024);
    SB();
    asm volatile("s_barrier" ::: "memory");
    SB();
#pragma unroll
    for (int g = 0; g < 8; ++g) tb.k[g] = ld16<KAUX>(rk, koff, tbase + TSTR + g * 1024);
#pragma unroll
    for (int g = 0; g < 8; ++g) tb.v[g] = ld16<VAUX>(rv, voff, tbase + TSTR + g * 1024);
    SB();
    bool ok = true;
#pragma unroll
    for (int i = 0; i < 4; ++i) ok = ok & (x[i][1] == tag) & (x[i][3] == tag);
    if (__builtin_expect(!__all(ok), 0)) {
      for (unsigned spins = 0; spins < SPIN_CAP; ++spins) {
        bool ok2 = true;
        asm volatile("" ::: "memory");
#pragma unroll
        for (int i = 0; i < 4; ++i) x[i] = ld16<AUX_SC1>(rws, qoff, i * 1024);
#pragma unroll
        for (int i = 0; i < 4; ++i) ok2 = ok2 & (x[i][1] == tag) & (x[i][3] == tag);
        if (__all(ok2)) break;
      }
      __builtin_amdgcn_s_waitcnt(0x0F70);
    }
    ((LAS unsigned *)L)[L_Q + w * 64 + lane] = cvtpk((asf(x[0][0]) + asf(x[1][0])) + (asf(x[2][0]) + asf(x[3][0])),
                                                     (asf(x[0][2]) + asf(x[1][2])) + (asf(x[2][2]) + asf(x[3][2])));
    WG_BARRIER();
    q = *(LAS v4u *)(L + L_Q + a4 * 64 + (4 * g4 + u4) * 4);
  }
  STAMP_AT(2);
  float m2 = -3.0e38f, lsum = 0.f;
  f32x4 acc0 = {0.f, 0.f, 0.f, 0.f}, acc1 = acc0;
  for (int it = 0; it < 7; ++it) {
    const int s0 = tbase + (2 * it + 2) * TSTR;
    if (TAU) { while ((unsigned)__builtin_amdgcn_s_memrealtime() - tstart < (unsigned)(TOFF + 2 * it * TAU)) __builtin_amdgcn_s_sleep(1); }
    tile_step<true>(ta, q, rk, rv, koff, voff, s0, mk0, mk1, m2, lsum, acc0, acc1);
    if (TAU) { while ((unsigned)__builtin_amdgcn_s_memrealtime() - tstart < (unsigned)(TOFF + (2 * it + 1) * TAU)) __builtin_amdgcn_s_sleep(1); }
    tile_step<true>(tb, q, rk, rv, koff, voff, s0 + TSTR, mk0, mk1, m2, lsum, acc0, acc1);
  }
  const bool role_wo = b < NPW;
  const rsrc_t rwo = __builtin_amdgcn_make_buffer_rsrc((char *)p.wo + (size_t)(role_wo ? b : 0) * 32768, 0, role_wo ? 32768 : 0, RS3);
  const int foff = g4 * 1024 + w * 256 + (lane & 15) * 16;
  v4u wo[8];
#pragma unroll
  for (int i = 0; i < 8; ++i) wo[i] = ld16<WAUX>(rwo, foff, i * 4096);
  SB();
  tile_step<false>(ta, q, rk, rv, koff, voff, 0, mk0, mk1, m2, lsum, acc0, acc1);
  tile_step<false>(tb, q, rk, rv, koff, voff, 0, mk0, mk1, m2, lsum, acc0, acc1);
  STAMP_AT(3);
  {
    const float lw = xr16_sum(lsum);
    *(LAS f32x4 *)(L + L_MRG + w * 512 + g4 * 128 + 8 * (lane & 15)) = acc0;
    *(LAS f32x4 *)(L + L_MRG + w * 512 + g4 * 128 + 8 * (lane & 15) + 4) = acc1;
    if (lane < 16 && u4 == 0) { f32x2 y = {m2, lw}; *(LAS f32x2 *)(L + L_ML + (w * 4 + a4) * 2) = y; }
    WG_BARRIER();
    f32x2 ml[4];
#pragma unroll
    for (int ww = 0; ww < 4; ++ww) ml[ww] = *(LAS f32x2 *)(L + L_ML + (ww * 4 + w) * 2);
    const float M = fmaxf(fmaxf(ml[0][0], ml[1][0]), fmaxf(ml[2][0], ml[3][0]));
    float Ls = 0.f; f32x2 o = {0.f, 0.f};
#pragma unroll
    for (int ww = 0; ww < 4; ++ww) {
      const float e = __builtin_amdgcn_exp2f(ml[ww][0] - M);
      Ls = fmaf(e, ml[ww][1], Ls);
      o += e * *(LAS f32x2 *)(L + L_MRG + ww * 512 + w * 128 + 2 * lane);
    }
    const int c4 = (4 * kh + w) * 4;
    st8(rws, WS_OP + ((c4 + g4) * 32 + sp) * 128 + (lane & 15) * 8, v2u{cvtpk(o[0], o[1]), tag});
    if (lane < 4) st16(rws, WS_ML + ((c4 + lane) * 32 + sp) * 16, v4u{asu(M), tag, asu(Ls), tag});
  }
  STAMP_AT(4);
  if (role_wo) {
    const int off[2] = {WS_OP + b * 4096 + tid * 16, WS_ML + (b * 32 + (tid >> 3)) * 16};
    v4u x[2];
    gather<2>(rws, off, tag, x);
    STAMP_AT(5);
    const float ms = asf(x[1][0]), ls = asf(x[1][2]);
    const float Mw = xr16_max(fmaxf(ms, dpp<ROR8>(ms)));
    const float e = __builtin_amdgcn_exp2f(ms - Mw);
    const float v0 = strided_sum<8>(e * lo16(x[0][0])), v1 = strided_sum<8>(e * hi16(x[0][0]));
    const float v2 = strided_sum<8>(e * lo16(x[0][2])), v3 = strided_sum<8>(e * hi16(x[0][2])), lw = strided_sum<8>(e * ls);
    if (lane < 8) {
      L[L_RED2 + w * 64 + lane] = v0; L[L_RED2 + w * 64 + 8 + lane] = v1; L[L_RED2 + w * 64 + 16 + lane] = v2; L[L_RED2 + w * 64 + 24 + lane] = v3;
      if (lane == 0) *(LAS f32x2 *)(L + L_RED2 + w * 64 + 32) = f32x2{Mw, lw};
    }
    WG_BARRIER();
    f32x2 ml[4];
#pragma unroll
    for (int ww = 0; ww < 4; ++ww) ml[ww] = *(LAS f32x2 *)(L + L_RED2 + ww * 64 + 32);
    const float M = fmaxf(fmaxf(ml[0][0], ml[1][0]), fmaxf(ml[2][0], ml[3][0]));
    f32x4 oa = {0.f, 0.f, 0.f, 0.f}, ob = oa;
    float Lt = 0.f;
#pragma unroll
    for (int ww = 0; ww < 4; ++ww) {
      const float ew = __builtin_amdgcn_exp2f(ml[ww][0] - M);
      Lt = fmaf(ew, ml[ww][1], Lt);
      oa += ew * *(LAS f32x4 *)(L + L_RED2 + ww * 64 + g4 * 8);
      ob += ew * *(LAS f32x4 *)(L + L_RED2 + ww * 64 + g4 * 8 + 4);
    }
    const float rl = __builtin_amdgcn_rcpf(Lt);
    f32x4 c0 = {0.f, 0.f, 0.f, 0.f}, c1 = c0;
#pragma unroll
    for (int i = 0; i < 8; ++i) { const float o = (i < 4 ? oa[i & 3] : ob[i & 3]) * rl; gemv_step(cvtpk(o, o), mk0, mk1, wo[i], c0, c1); }
    publish512(rws, WS_PB, NPW, b, c0, c1, L + L_TR, w, lane, tag);
    STAMP_AT(6);
  } else if (b < NPW + NPF) {
    const int pf = (b & 7) * 7 + ((b - NPW) >> 3);
    const rsrc_t rwg = __builtin_amdgcn_make_buffer_rsrc((char *)p.wg + pf * 64, 0, DM * FF * 2 - pf * 64, RS3);
    const rsrc_t rwu = __builtin_amdgcn_make_buffer_rsrc((char *)p.wu + pf * 64, 0, DM * FF * 2 - pf * 64, RS3);
    const rsrc_t rwd = __builtin_amdgcn_make_buffer_rsrc((char *)p.wd + (size_t)pf * 32768, 0, 32768, RS3);
    const int go = (128 * w + 4 * g4 + a4) * (FF * 2) + u4 * 16;
    v4u wg[8], wu[8], wd[8];
#pragma unroll
    for (int i = 0; i < 8; ++i) { wg[i] = ld16<WAUX>(rwg, go, i * 16 * FF * 2); wu[i] = ld16<WAUX>(rwu, go, i * 16 * FF * 2); }
#pragma unroll
    for (int i = 0; i < 8; ++i) wd[i] = ld16<WAUX>(rwd, foff, i * 4096);
    SB();
    {
      const int off[1] = {WS_HG + (b % REP) * 2048 + (32 * w + (lane & 31)) * 16};
      v4u x[1];
      gather<1>(rws, off, tag, x);
      if (lane < 32) *(LAS v2u *)((LAS unsigned *)L + L_Q + 64 * w + 2 * lane) = v2u{x[0][0], x[0][2]};
    }
    WG_BARRIER();
    STAMP_AT(9);
    {
      const v4u hb = *(LAS v4u *)(L + L_Q + 4 * lane);
      const LnStat st = ln_stats(hb);
      *(LAS v4u *)(L + L_XN + w * 256 + 4 * lane) = v4u{ln_pair(hb[0], st), ln_pair(hb[1], st), ln_pair(hb[2], st), ln_pair(hb[3], st)};
      LDS_WAIT();
    }
    STAMP_AT(12);
    f32x4 g0 = {0.f, 0.f, 0.f, 0.f}, g1 = g0, u0 = g0, u1 = g0;
#pragma unroll
    for (int i = 0; i < 8; ++i) {
      const unsigned xv = ((LAS unsigned short *)(L + L_XN + w * 256))[128 * w + 16 * i + 4 * g4 + a4];
      const unsigned pp = xv | (xv << 16);
      gemv_step(pp, mk0, mk1, wg[i], g0, g1);
      gemv_step(pp, mk0, mk1, wu[i], u0, u1);
    }
    if (g4 == a4) {
      LAS float *gu = L + L_MRG + (w * 4 + g4) * 64 + 8 * u4;
      *(LAS f32x4 *)(gu) = g0; *(LAS f32x4 *)(gu + 4) = g1; *(LAS f32x4 *)(gu + 32) = u0; *(LAS f32x4 *)(gu + 36) = u1;
    }
    STAMP_AT(13);
    WG_BARRIER();
    {
      const int c = tid >> 3, part = tid & 7;
      LAS float *gu = L + L_MRG + part * 128 + c;
      float g = gu[0] + gu[64], u = gu[32] + gu[96];
      g += dpp<XOR1>(g); u += dpp<XOR1>(u); g += dpp<XOR2>(g); u += dpp<XOR2>(u); g += dpp<HMIR>(g); u += dpp<HMIR>(u);
      const float hv = rbf(g) * rbf(u);
      if (part == 0) ((LAS unsigned *)L)[L_HID + (c & 3) * 8 + (c >> 2)] = cvtpk(hv, hv);
    }
    WG_BARRIER();
    STAMP_AT(14);
    const v4u ha = *(LAS v4u *)(L + L_HID + g4 * 8), hc = *(LAS v4u *)(L + L_HID + g4 * 8 + 4);
    f32x4 c0 = {0.f, 0.f, 0.f, 0.f}, c1 = c0;
#pragma unroll
    for (int i = 0; i < 8; ++i) gemv_step(i < 4 ? ha[i & 3] : hc[i & 3], mk0, mk1, wd[i], c0, c1);
    publish512(rws, WS_PD, NPF, pf, c0, c1, L + L_TR, w, lane, tag);
    STAMP_AT(10);
  } else if (b < NPW + NPF + 32) {
    const int j = b - (NPW + NPF);
    const v2u xv = __builtin_amdgcn_raw_buffer_load_b64(rx, (16 * j + 4 * (tid & 3)) * 2, 0, 0);
    const int off[2] = {WS_PB + j * (NPW * 64) + tid * 16, WS_PB + j * (NPW * 64) + 4096 + tid * 16};
    v4u x[2];
    gather<2>(rws, off, tag, x);
    STAMP_AT(7);
    const f32x4 r = reduce_cols<2>(x, true, L + L_RED, w, lane);
    const unsigned h0 = cvtpk(lo16(xv[0]) + rbf(r[0]), hi16(xv[0]) + rbf(r[1])), h1 = cvtpk(lo16(xv[1]) + rbf(r[2]), hi16(xv[1]) + rbf(r[3]));
    if (tid < 4 * REP) st16(rws, WS_HG + (tid >> 2) * 2048 + (4 * j + (tid & 3)) * 16, v4u{h0, tag, h1, tag});
    STAMP_AT(8);
  } else if (b < NPW + NPF + 64) {
    const int j = b - (NPW + NPF + 32);
    const bool live = tid < NPF * 4;
    const int off[2] = {WS_HG + (b % REP) * 2048 + (4 * j + (tid & 3)) * 16, WS_PD + j * (NPF * 64) + (live ? tid : tid - 128) * 16};
    v4u x[2];
    gather<2>(rws, off, tag, x);
    const v4u y[1] = {x[1]};
    const f32x4 r = reduce_cols<1>(y, live, L + L_RED, w, lane);
    const v2u o = {cvtpk(lo16(x[0][0]) + rbf(r[0]), hi16(x[0][0]) + rbf(r[1])), cvtpk(lo16(x[0][2]) + rbf(r[2]), hi16(x[0][2]) + rbf(r[3]))};
    if (tid < 4) *(v2u *)((char *)p.out + (16 * j + 4 * tid) * 2) = o;
    if (j == 0 && tid == 0) __builtin_amdgcn_raw_buffer_store_b32(tag, rws, WS_EPOCH, 0, AUX_SC1);
  }
  STAMP_AT(11);
  STAMP_FLUSH();
}
}

extern "C" void kernel_launch(void *const *d_in, const int *in_sizes, int n_in, void *d_out, int out_size, void *d_ws, size_t ws_size,
                              hipStream_t stream) {
  Params p;
  p.x = d_in[0]; p.k = d_in[1]; p.v = d_in[2]; p.wq = d_in[3]; p.wo = d_in[4];
  p.wg = d_in[6]; p.wu = d_in[7]; p.wd = d_in[8];
  p.out = d_out; p.ws = d_ws;
  (void)hipFuncSetAttribute((const void *)fused_decode_kernel, hipFuncAttributeMaxDynamicSharedMemorySize, LDS_BYTES);
  fused_decode_kernel<<<NWG, NTHR, LDS_BYTES, stream>>>(p);
}
```
